```python
import torch

def _softmax(x, axis=-1):
    return torch.softmax(x, dim=axis)
def _gelu(x):
    return torch.nn.functional.gelu(x, approximate="tanh")
def _silu(x):
    return torch.nn.functional.silu(x)
def _sigmoid(x):
    return torch.sigmoid(x)
def _layernorm(x, axis=-1, eps=1e-5):
    m = x.mean(dim=axis, keepdim=True)
    v = x.var(dim=axis, keepdim=True, unbiased=False)
    return (x - m) / torch.sqrt(v + eps)
def _scatter_add(tgt, idx, src, axis=0):
    return tgt.clone().index_add_(axis, idx, src)
def _segment_sum(src, seg, n):
    out = torch.zeros((n,) + src.shape[1:], dtype=src.dtype)
    return out.index_add_(0, seg, src)
def _pad(x, pw):
    flat = tuple(v for pair in reversed(pw) for v in pair)
    return torch.nn.functional.pad(x, flat)

kernel_name = 'gated_mix__sliding_block__swiglu_ffn__prompt'

def setup_inputs():

    _w = (lambda a: (lambda t: t.to(torch.bfloat16) if t.is_floating_point() else t)(torch.as_tensor(a)))
    def _A_setup():
        B, S, D = 1, 2048, 640
        return {
            "x": _w((lambda *s: torch.randn(*s, dtype=torch.bfloat16) * 0.05)(B, S, D)),
            "wqkv": _w((lambda *s: torch.randn(*s, dtype=torch.bfloat16) * 1.6)(D, 3 * D)),
            "wo": _w((lambda *s: torch.randn(*s, dtype=torch.bfloat16) * 0.05)(D, D)),
        }
    _A_in = _A_setup()

    def _B_setup():
        B, S, D, F = 1, 2048, 640, 1728
        return {
            "x": _w((lambda *s: torch.randn(*s, dtype=torch.bfloat16) * 0.05)(B, S, D)),
            "w_gate": _w((lambda *s: torch.randn(*s, dtype=torch.bfloat16) * 0.4)(D, F)),
            "w_up": _w((lambda *s: torch.randn(*s, dtype=torch.bfloat16) * 0.4)(D, F)),
            "w_down": _w((lambda *s: torch.randn(*s, dtype=torch.bfloat16) * 0.4)(F, D)),
        }
    _B_in = _B_setup()

    return {
        "A_x": _A_in["x"],
        "A_wqkv": _A_in["wqkv"],
        "A_wo": _A_in["wo"],
        "B_x": _B_in["x"],
        "B_w_gate": _B_in["w_gate"],
        "B_w_up": _B_in["w_up"],
        "B_w_down": _B_in["w_down"],
        "gate_w": _w((lambda *s: torch.randn(*s, dtype=torch.bfloat16) * 0.05)(640) * 16.0),
    }

def reference(A_x, A_wqkv, A_wo, B_x, B_w_gate, B_w_up, B_w_down, gate_w):
    def _A_ref(x, wqkv, wo):
        B, S, D = x.shape
        H = 32; Dh = D // H; W = 256
        qkv = (lambda x, *d: x.permute(*d))((x @ wqkv).reshape(B, S, 3, H, Dh), 2, 0, 3, 1, 4)
        q, k, v = qkv[0], qkv[1], qkv[2]
        scores = torch.einsum("bhqd,bhkd->bhqk", q, k) * (Dh ** -0.5)
        idx = torch.arange(S)
        mask = (idx[:, None] - idx[None, :] >= 0) & (idx[:, None] - idx[None, :] < W)
        scores = torch.where(mask[None, None], scores, -1e9)
        p = _softmax(scores, axis=-1)
        o = torch.einsum("bhqk,bhkd->bhqd", p, v)
        return (lambda x, *d: x.permute(*d))(o, 0, 2, 1, 3).reshape(B, S, D) @ wo

    def _B_ref(x, w_gate, w_up, w_down):
        g = _silu(x @ w_gate)
        u = x @ w_up
        return (g * u) @ w_down

    x = A_x
    g = _sigmoid((x * gate_w).sum(-1, keepdims=True))
    a = _A_ref(x, A_wqkv, A_wo)
    b = _B_ref(x, B_w_gate, B_w_up, B_w_down)
    return g * a + (1 - g) * b
```

```cpp
#include <hip/hip_runtime.h>
#include <hip/hip_bf16.h>
#include <stdint.h>

typedef __attribute__((ext_vector_type(8))) __bf16 bfx8;
typedef __attribute__((ext_vector_type(4))) short s16x4;
typedef __attribute__((ext_vector_type(8))) short s16x8;
typedef __attribute__((ext_vector_type(4))) float f32x4;
typedef __attribute__((ext_vector_type(2))) unsigned u32x2;
typedef __attribute__((ext_vector_type(4))) unsigned u32x4;
typedef __attribute__((address_space(3))) s16x4* lds_s16x4_ptr;
typedef __attribute__((address_space(3))) unsigned* lds_u32_ptr;
typedef unsigned short u16;

#define S_LEN 2048
#define D_MODEL 640
#define F_DIM 1728
#define NHEAD 32
#define DHEAD 20

__device__ __forceinline__ float bf2f(u16 v) { return __uint_as_float(((unsigned)v) << 16); }
__device__ __forceinline__ u16 f2bf(float f) {
  __hip_bfloat16 b = __float2bfloat16(f);
  return __builtin_bit_cast(u16, b);
}
__device__ __forceinline__ float rbf(float f) { return bf2f(f2bf(f)); }
__device__ __forceinline__ unsigned pack2(float lo, float hi) {
  return (unsigned)f2bf(lo) | ((unsigned)f2bf(hi) << 16);
}
__device__ __forceinline__ f32x4 mfma16(bfx8 a, bfx8 b, f32x4 c) {
  return __builtin_amdgcn_mfma_f32_16x16x32_bf16(a, b, c, 0, 0, 0);
}
__device__ __forceinline__ void glds16(const void* g, void* l) {
  __builtin_amdgcn_global_load_lds((const unsigned*)g, (lds_u32_ptr)l, 16, 0, 0);
}
__device__ __forceinline__ bfx8 tr_frag(const char* p0, const char* p1) {
  s16x4 a = __builtin_amdgcn_ds_read_tr16_b64_v4i16((lds_s16x4_ptr)p0);
  s16x4 b = __builtin_amdgcn_ds_read_tr16_b64_v4i16((lds_s16x4_ptr)p1);
  s16x8 v = {a[0], a[1], a[2], a[3], b[0], b[1], b[2], b[3]};
  return __builtin_bit_cast(bfx8, v);
}

#define K1_XB 16384
#define K1_WPITCH 2688
#define K1_WB (16 * K1_WPITCH)
#define K1_STAGE (K1_XB + K1_WB)

template <int NU, int U0>
__device__ __forceinline__ void k1_compute(const char* sb, f32x4 (&acc)[11][2], int xoff0, int woff0, int woff1) {
#pragma unroll
  for (int kk = 0; kk < 2; ++kk) {
    bfx8 xf[2];
#pragma unroll
    for (int i = 0; i < 2; ++i) {
      xf[i] = *(const bfx8*)(sb + ((xoff0 + i * 2048) ^ (kk * 64)));
    }
#pragma unroll
    for (int u = 0; u < NU; ++u) {
      const char* b = sb + K1_XB + kk * 8 * K1_WPITCH + (U0 + u) * 128;
      bfx8 wf = tr_frag(b + woff0, b + woff1);
      acc[u][0] = mfma16(wf, xf[0], acc[u][0]);
      acc[u][1] = mfma16(wf, xf[1], acc[u][1]);
    }
  }
}

template <int NU, int U0>
__device__ __forceinline__ void k1_epilogue(f32x4 (&acc)[11][2], int nt, int tok0, int lane,
                                            u16* __restrict__ Qp, u16* __restrict__ Kp,
                                            u16* __restrict__ Vt, u16* __restrict__ Hb) {
  const int g = lane >> 4, c = lane & 15;
#pragma unroll
  for (int u = 0; u < NU; ++u) {
    const int U = nt * 21 + U0 + u;
#pragma unroll
    for (int i = 0; i < 2; ++i) {
      const int tok = tok0 + i * 16 + c;
      f32x4 a = acc[u][i];
      if (U < 80) {
        int col = U * 16 + 4 * g;
        u16* dst = Qp;
        if (col >= 640) { col -= 640; dst = Kp; }
        int h = col / 20, d = col - h * 20;
        u32x2 v = {pack2(a[0], a[1]), pack2(a[2], a[3])};
        *(u32x2*)(dst + ((size_t)h * S_LEN + tok) * 32 + d) = v;
      } else if (U < 120) {
        int col = (U - 80) * 16 + 4 * g;
#pragma unroll
        for (int r = 0; r < 4; ++r) Vt[(size_t)(col + r) * S_LEN + tok] = f2bf(a[r]);
      } else {
        int j = U - 120;
        u32x2 s0 = __builtin_amdgcn_permlane32_swap(__float_as_uint(a[0]), __float_as_uint(a[2]), false, false);
        u32x2 s1 = __builtin_amdgcn_permlane32_swap(__float_as_uint(a[1]), __float_as_uint(a[3]), false, false);
        float g0 = rbf(__uint_as_float(s0[0])), u0 = rbf(__uint_as_float(s0[1]));
        float g1 = rbf(__uint_as_float(s1[0])), u1 = rbf(__uint_as_float(s1[1]));
        float si0 = rbf(g0 / (1.0f + __expf(-g0)));
        float si1 = rbf(g1 / (1.0f + __expf(-g1)));
        int f = 8 * j + 4 * (g & 1) + 2 * (g >> 1);
        *(unsigned*)(Hb + (size_t)tok * F_DIM + f) = pack2(si0 * u0, si1 * u1);
      }
    }
  }
}

__global__ __launch_bounds__(512, 2) void k1_gemm(const u16* __restrict__ x, const u16* __restrict__ wqkv,
                                                  const u16* __restrict__ wg, const u16* __restrict__ wu,
                                                  u16* __restrict__ Qp, u16* __restrict__ Kp,
                                                  u16* __restrict__ Vt, u16* __restrict__ Hb) {
  __shared__ __attribute__((aligned(16))) char smem[2 * K1_STAGE];
  const int tid = threadIdx.x, lane = tid & 63;
  const int wave = __builtin_amdgcn_readfirstlane(tid >> 6);
  const int bid = blockIdx.x;
  const int xcd = bid & 7, idx = bid >> 3;
  const int mt = (xcd >> 2) * 8 + (idx & 7);
  const int nt = (xcd & 3) * 4 + (idx >> 3);
  const int m0 = mt * 128;

  const u16* gp[8];
  int gstride[8];
#pragma unroll
  for (int j = 0; j < 8; ++j) {
    int i = wave + 8 * j;
    if (i > 57) i = 57;
    if (j < 2) {
      int o = i * 1024 + lane * 16;
      int row = o >> 7, slot = (o >> 4) & 7;
      int chunk = slot ^ (row & 7);
      gp[j] = x + (size_t)(m0 + row) * D_MODEL + chunk * 8;
      gstride[j] = 64;
    } else {
      int o = (i - 16) * 1024 + lane * 16;
      int r = o / K1_WPITCH, rem = o - r * K1_WPITCH;
      int u = rem >> 7, q = (rem >> 5) & 3, half = (rem >> 4) & 1;
      int kb = r ^ ((r >> 1) & 1);
      int k = 4 * kb + q;
      int U = nt * 21 + u;
      if (U < 120) {
        gp[j] = wqkv + (size_t)k * 1920 + U * 16 + half * 8;
        gstride[j] = 64 * 1920;
      } else {
        gp[j] = (half ? wu : wg) + (size_t)k * F_DIM + (U - 120) * 8;
        gstride[j] = 64 * F_DIM;
      }
    }
  }
  auto stage = [&](int buf) {
    char* sb = smem + buf * K1_STAGE;
#pragma unroll
    for (int j = 0; j < 8; ++j) {
      int i = wave + 8 * j;
      if (j < 7 || i < 58) glds16(gp[j], sb + i * 1024);
      gp[j] += gstride[j];
    }
  };

  const int wm = wave & 3, wn = wave >> 2;
  const int g = lane >> 4, c = lane & 15;
  const int xrow = wm * 32 + c;
  const int xoff0 = xrow * 128 + ((g ^ (xrow & 7)) << 4);
  const int woff0 = (2 * g + (g & 1)) * K1_WPITCH + c * 8;
  const int woff1 = (2 * g + 1 - (g & 1)) * K1_WPITCH + c * 8;

  f32x4 acc[11][2];
#pragma unroll
  for (int u = 0; u < 11; ++u) { acc[u][0] = f32x4{0, 0, 0, 0}; acc[u][1] = f32x4{0, 0, 0, 0}; }

  stage(0);
  for (int t = 0; t < 10; ++t) {
    __syncthreads();
    if (t + 1 < 10) stage((t + 1) & 1);
    const char* sb = smem + (t & 1) * K1_STAGE;
    if (wn == 0) k1_compute<11, 0>(sb, acc, xoff0, woff0, woff1);
    else k1_compute<10, 11>(sb, acc, xoff0, woff0, woff1);
  }
  if (wn == 0) k1_epilogue<11, 0>(acc, nt, m0 + wm * 32, lane, Qp, Kp, Vt, Hb);
  else k1_epilogue<10, 11>(acc, nt, m0 + wm * 32, lane, Qp, Kp, Vt, Hb);
}

__device__ __forceinline__ bfx8 load_qk_frag(const u16* rowp, int g) {
  u32x4 v = {0, 0, 0, 0};
  if (g < 2) v = *(const u32x4*)(rowp + 8 * g);
  else if (g == 2) { u32x2 t = *(const u32x2*)(rowp + 16); v[0] = t[0]; v[1] = t[1]; }
  return __builtin_bit_cast(bfx8, v);
}

__global__ __launch_bounds__(256, 2) void k2_attn(const u16* __restrict__ Qp, const u16* __restrict__ Kp,
                                                  const u16* __restrict__ Vt, u16* __restrict__ Ob,
                                                  const u16* __restrict__ x, const u16* __restrict__ gw,
                                                  float* __restrict__ G) {
  const int tid = threadIdx.x, lane = tid & 63;
  const int wave = __builtin_amdgcn_readfirstlane(tid >> 6);
  const int bid = blockIdx.x;
  if (bid >= 512) {
    const int r0 = ((bid - 512) * 4 + wave) * 8;
    for (int rr = 0; rr < 8; ++rr) {
      const u16* xr = x + (size_t)(r0 + rr) * D_MODEL;
      float s = 0.f;
      for (int ch = lane; ch < 80; ch += 64) {
        s16x8 xv = *(const s16x8*)(xr + ch * 8);
        s16x8 wv = *(const s16x8*)(gw + ch * 8);
#pragma unroll
        for (int e = 0; e < 8; ++e) s += rbf(bf2f((u16)xv[e]) * bf2f((u16)wv[e]));
      }
#pragma unroll
      for (int o = 32; o >= 1; o >>= 1) s += __shfl_xor(s, o);
      s = rbf(s);
      float gv = rbf(1.0f / (1.0f + __expf(-s)));
      if (lane == 0) G[r0 + rr] = gv;
    }
    return;
  }
  const int item = bid * 4 + wave;
  const int h = item & 31, qb = item >> 5;
  const int q0 = qb * 32, kbase = q0 - 256;
  const int g = lane >> 4, c = lane & 15;
  const u16* Qh = Qp + (size_t)h * S_LEN * 32;
  const u16* Kh = Kp + (size_t)h * S_LEN * 32;

  bfx8 qf[2];
#pragma unroll
  for (int t = 0; t < 2; ++t) qf[t] = load_qk_frag(Qh + (size_t)(q0 + 16 * t + c) * 32, g);

  f32x4 s[9][2][2];
  const int krow = 8 * (c >> 2) + (c & 3);
#pragma unroll
  for (int j = 0; j < 9; ++j) {
#pragma unroll
    for (int a = 0; a < 2; ++a) {
      int key = kbase + 32 * j + krow + 4 * a;
      key = key < 0 ? 0 : key;
      bfx8 kf = load_qk_frag(Kh + (size_t)key * 32, g);
#pragma unroll
      for (int t = 0; t < 2; ++t) s[j][a][t] = mfma16(kf, qf[t], f32x4{0, 0, 0, 0});
    }
  }
  const float NEG = -1e30f;
#pragma unroll
  for (int a = 0; a < 2; ++a)
#pragma unroll
    for (int t = 0; t < 2; ++t)
#pragma unroll
      for (int r = 0; r < 4; ++r) {
        int ko = 8 * g + 4 * a + r, qo = 16 * t + c;
        if (!(ko > qo)) s[0][a][t][r] = NEG;
        if (!(ko <= qo)) s[8][a][t][r] = NEG;
      }
  if (qb < 8) {
#pragma unroll
    for (int j = 0; j < 8; ++j)
      if (j < 8 - qb) {
#pragma unroll
        for (int a = 0; a < 2; ++a)
#pragma unroll
          for (int t = 0; t < 2; ++t) s[j][a][t] = f32x4{NEG, NEG, NEG, NEG};
      }
  }
  const float cs = 0.22360679774997896f * 1.4426950408889634f;
  bfx8 pf[9][2];
#pragma unroll
  for (int t = 0; t < 2; ++t) {
    float m = NEG;
#pragma unroll
    for (int j = 0; j < 9; ++j)
#pragma unroll
      for (int a = 0; a < 2; ++a)
#pragma unroll
        for (int r = 0; r < 4; ++r) m = fmaxf(m, s[j][a][t][r]);
    m = fmaxf(m, __shfl_xor(m, 16));
    m = fmaxf(m, __shfl_xor(m, 32));
    const float mc = m * cs;
#pragma unroll
    for (int j = 0; j < 9; ++j) {
      u32x4 pk;
#pragma unroll
      for (int a = 0; a < 2; ++a) {
        float p0 = __builtin_amdgcn_exp2f(fmaf(s[j][a][t][0], cs, -mc));
        float p1 = __builtin_amdgcn_exp2f(fmaf(s[j][a][t][1], cs, -mc));
        float p2 = __builtin_amdgcn_exp2f(fmaf(s[j][a][t][2], cs, -mc));
        float p3 = __builtin_amdgcn_exp2f(fmaf(s[j][a][t][3], cs, -mc));
        pk[2 * a] = pack2(p0, p1);
        pk[2 * a + 1] = pack2(p2, p3);
      }
      pf[j][t] = __builtin_bit_cast(bfx8, pk);
    }
  }
  f32x4 o0[2] = {f32x4{0, 0, 0, 0}, f32x4{0, 0, 0, 0}}, o1[2] = {f32x4{0, 0, 0, 0}, f32x4{0, 0, 0, 0}};
  const u16* V0 = Vt + (size_t)(h * DHEAD + c) * S_LEN;
  const u16* V1 = Vt + (size_t)(h * DHEAD + 16 + (c & 3)) * S_LEN;
#pragma unroll
  for (int j = 0; j < 9; ++j) {
    int key = kbase + 32 * j + 8 * g;
    key = key < 0 ? 0 : key;
    u32x4 v0 = *(const u32x4*)(V0 + key);
    u32x4 v1 = *(const u32x4*)(V1 + key);
    if (c == 4) v1 = u32x4{0x3f803f80u, 0x3f803f80u, 0x3f803f80u, 0x3f803f80u};
    else if (c > 4) v1 = u32x4{0, 0, 0, 0};
    bfx8 vf0 = __builtin_bit_cast(bfx8, v0), vf1 = __builtin_bit_cast(bfx8, v1);
#pragma unroll
    for (int t = 0; t < 2; ++t) {
      o0[t] = mfma16(vf0, pf[j][t], o0[t]);
      o1[t] = mfma16(vf1, pf[j][t], o1[t]);
    }
  }
#pragma unroll
  for (int t = 0; t < 2; ++t) {
    float l = __shfl(o1[t][0], 16 + c);
    float inv = 1.0f / l;
    u16* dst = Ob + (size_t)(q0 + 16 * t + c) * D_MODEL + h * DHEAD;
    u32x2 v = {pack2(o0[t][0] * inv, o0[t][1] * inv), pack2(o0[t][2] * inv, o0[t][3] * inv)};
    *(u32x2*)(dst + 4 * g) = v;
    if (g == 0) {
      u32x2 w = {pack2(o1[t][0] * inv, o1[t][1] * inv), pack2(o1[t][2] * inv, o1[t][3] * inv)};
      *(u32x2*)(dst + 16) = w;
    }
  }
}

#define G2_XB 4096
#define G2_WPITCH 640
#define G2_WB (8 * G2_WPITCH)
#define G2_STAGE (G2_XB + G2_WB)
#define G2_NST 4

template <int KTOT, int EPI>
__global__ __launch_bounds__(256, 1) void k_gemm2(const u16* __restrict__ A, const u16* __restrict__ W,
                                                  u16* __restrict__ out, const u16* __restrict__ Bb,
                                                  const float* __restrict__ G) {
  __shared__ __attribute__((aligned(16))) char smem[4 * G2_NST * G2_STAGE];
  const int tid = threadIdx.x, lane = tid & 63;
  const int wave = __builtin_amdgcn_readfirstlane(tid >> 6);
  const int bid = blockIdx.x;
  const int xcd = bid & 7, idx = bid >> 3;
  const int mt = (xcd >> 1) * 8 + (idx & 7);
  const int nt = (xcd & 1) * 4 + (idx >> 3);
  const int m0 = mt * 64, n0 = nt * 80;
  constexpr int NSTEP = KTOT / 32;
  const int nmine = (NSTEP - wave + 3) / 4;

  const u16* gp[9];
#pragma unroll
  for (int j = 0; j < 4; ++j) {
    int o = j * 1024 + lane * 16;
    int row = o >> 6, slot = (o >> 4) & 3;
    int chunk = slot ^ ((0x78 >> (2 * ((row >> 2) & 3))) & 3);
    gp[j] = A + (size_t)(m0 + row) * KTOT + wave * 32 + chunk * 8;
  }
#pragma unroll
  for (int j = 0; j < 5; ++j) {
    int o = j * 1024 + lane * 16;
    int r = o / G2_WPITCH, rem = o - r * G2_WPITCH;
    int u = rem >> 7, q = (rem >> 5) & 3, half = (rem >> 4) & 1;
    int kb = r ^ ((r >> 1) & 1);
    int k = wave * 32 + 4 * kb + q;
    gp[4 + j] = W + (size_t)k * D_MODEL + n0 + u * 16 + half * 8;
  }
  char* wbase = smem + wave * (G2_NST * G2_STAGE);
  auto stage = [&](int buf) {
    char* sb = wbase + buf * G2_STAGE;
#pragma unroll
    for (int j = 0; j < 9; ++j) {
      glds16(gp[j], sb + j * 1024);
      gp[j] += (j < 4) ? 128 : 128 * D_MODEL;
    }
  };
  const int g = lane >> 4, c = lane & 15;
  const int xoff = c * 64 + ((g ^ ((0x78 >> (2 * ((c >> 2) & 3))) & 3)) << 4);
  const int woff0 = G2_XB + (2 * g + (g & 1)) * G2_WPITCH + c * 8;
  const int woff1 = G2_XB + (2 * g + 1 - (g & 1)) * G2_WPITCH + c * 8;

  f32x4 acc[5][4];
#pragma unroll
  for (int u = 0; u < 5; ++u)
#pragma unroll
    for (int i = 0; i < 4; ++i) acc[u][i] = f32x4{0, 0, 0, 0};

#pragma unroll
  for (int p = 0; p < G2_NST - 1; ++p)
    if (p < nmine) stage(p);
  for (int it = 0; it < nmine; ++it) {
    const int rem = nmine - 1 - it;
    if (rem >= 2) asm volatile("s_waitcnt vmcnt(18)" ::: "memory");
    else if (rem == 1) asm volatile("s_waitcnt vmcnt(9)" ::: "memory");
    else asm volatile("s_waitcnt vmcnt(0)" ::: "memory");
    const char* sb = wbase + (it & (G2_NST - 1)) * G2_STAGE;
    bfx8 xf[4], wf[5];
#pragma unroll
    for (int i = 0; i < 4; ++i) xf[i] = *(const bfx8*)(sb + xoff + i * 1024);
#pragma unroll
    for (int u = 0; u < 5; ++u) wf[u] = tr_frag(sb + woff0 + u * 128, sb + woff1 + u * 128);
    if (it + G2_NST - 1 < nmine) stage((it + G2_NST - 1) & (G2_NST - 1));
#pragma unroll
    for (int u = 0; u < 5; ++u)
#pragma unroll
      for (int i = 0; i < 4; ++i) acc[u][i] = mfma16(wf[u], xf[i], acc[u][i]);
  }
  __syncthreads();
  f32x4* red = (f32x4*)smem;
#pragma unroll
  for (int u = 0; u < 5; ++u)
#pragma unroll
    for (int i = 0; i < 4; ++i) red[((wave * 4 + i) * 5 + u) * 64 + lane] = acc[u][i];
  __syncthreads();
  const int tok = m0 + wave * 16 + c;
  float gv = 0.f, omg = 0.f;
  if (EPI == 1) { gv = G[tok]; omg = rbf(1.0f - gv); }
#pragma unroll
  for (int u = 0; u < 5; ++u) {
    f32x4 v = red[((0 * 4 + wave) * 5 + u) * 64 + lane];
#pragma unroll
    for (int w = 1; w < 4; ++w) v += red[((w * 4 + wave) * 5 + u) * 64 + lane];
    const size_t o = (size_t)tok * D_MODEL + n0 + u * 16 + 4 * g;
    if (EPI == 0) {
      u32x2 pv = {pack2(v[0], v[1]), pack2(v[2], v[3])};
      *(u32x2*)(out + o) = pv;
    } else {
      s16x4 bv = *(const s16x4*)(Bb + o);
      float r[4];
#pragma unroll
      for (int e = 0; e < 4; ++e) r[e] = rbf(gv * rbf(v[e])) + rbf(omg * bf2f((u16)bv[e]));
      u32x2 pv = {pack2(r[0], r[1]), pack2(r[2], r[3])};
      *(u32x2*)(out + o) = pv;
    }
  }
}

extern "C" void kernel_launch(void* const* d_in, const int* in_sizes, int n_in,
                              void* d_out, int out_size, void* d_ws, size_t ws_size,
                              hipStream_t stream) {
  const u16* x = (const u16*)d_in[0];
  const u16* wqkv = (const u16*)d_in[1];
  const u16* wo = (const u16*)d_in[2];
  const u16* wg = (const u16*)d_in[4];
  const u16* wu = (const u16*)d_in[5];
  const u16* wd = (const u16*)d_in[6];
  const u16* gw = (const u16*)d_in[7];
  char* ws = (char*)d_ws;
  u16* Qp = (u16*)(ws);
  u16* Kp = (u16*)(ws + (4u << 20));
  u16* Vt = (u16*)(ws + (8u << 20));
  u16* Hb = (u16*)(ws + (12u << 20));
  u16* Ob = (u16*)(ws + (20u << 20));
  u16* Bb = (u16*)(ws + (24u << 20));
  float* G = (float*)(ws + (28u << 20));
  u16* out = (u16*)d_out;

  k1_gemm<<<256, 512, 0, stream>>>(x, wqkv, wg, wu, Qp, Kp, Vt, Hb);
  k2_attn<<<576, 256, 0, stream>>>(Qp, Kp, Vt, Ob, x, gw, G);
  k_gemm2<F_DIM, 0><<<256, 256, 0, stream>>>(Hb, wd, Bb, nullptr, nullptr);
  k_gemm2<D_MODEL, 1><<<256, 256, 0, stream>>>(Ob, wo, out, Bb, G);
}
```

```cpp
#include <hip/hip_runtime.h>
#include <hip/hip_bf16.h>
#include <stdint.h>

typedef __attribute__((ext_vector_type(8))) __bf16 bfx8;
typedef __attribute__((ext_vector_type(4))) short s16x4;
typedef __attribute__((ext_vector_type(8))) short s16x8;
typedef __attribute__((ext_vector_type(4))) float f32x4;
typedef __attribute__((ext_vector_type(2))) unsigned u32x2;
typedef __attribute__((ext_vector_type(4))) unsigned u32x4;
typedef __attribute__((address_space(3))) s16x4* lds_s16x4_ptr;
typedef __attribute__((address_space(3))) unsigned* lds_u32_ptr;
typedef unsigned short u16;

#define S_LEN 2048
#define D_MODEL 640
#define F_DIM 1728
#define NHEAD 32
#define DHEAD 20

__device__ __forceinline__ float bf2f(u16 v) { return __uint_as_float(((unsigned)v) << 16); }
__device__ __forceinline__ u16 f2bf(float f) {
  __hip_bfloat16 b = __float2bfloat16(f);
  return __builtin_bit_cast(u16, b);
}
__device__ __forceinline__ float rbf(float f) { return bf2f(f2bf(f)); }
__device__ __forceinline__ unsigned pack2(float lo, float hi) {
  return (unsigned)f2bf(lo) | ((unsigned)f2bf(hi) << 16);
}
__device__ __forceinline__ f32x4 mfma16(bfx8 a, bfx8 b, f32x4 c) {
  return __builtin_amdgcn_mfma_f32_16x16x32_bf16(a, b, c, 0, 0, 0);
}
__device__ __forceinline__ void glds16(const void* g, unsigned lds_addr) {
  unsigned keep;
  asm volatile("s_mov_b32 %0, m0\n\ts_mov_b32 m0, %2\n\ts_nop 0\n\tglobal_load_lds_dwordx4 %1, off\n\ts_mov_b32 m0, %0"
               : "=&s"(keep) : "v"(g), "s"(lds_addr) : "memory");
}
#define WAIT_VM(n) asm volatile("s_waitcnt vmcnt(" #n ")" ::: "memory")
__device__ __forceinline__ bfx8 tr_frag(const char* p0, const char* p1) {
  s16x4 a = __builtin_amdgcn_ds_read_tr16_b64_v4i16((lds_s16x4_ptr)p0);
  s16x4 b = __builtin_amdgcn_ds_read_tr16_b64_v4i16((lds_s16x4_ptr)p1);
  s16x8 v = {a[0], a[1], a[2], a[3], b[0], b[1], b[2], b[3]};
  return __builtin_bit_cast(bfx8, v);
}

#define K1_XB 16384
#define K1_WPITCH 2688
#define K1_WB (16 * K1_WPITCH)
#define K1_STAGE (K1_XB + K1_WB)
#define K1_CP 688

#define GLOAD(r, p) asm volatile("global_load_dwordx4 %0, %1, off" : "=v"(r) : "v"(p))
#define GLOAD1(r, p) asm volatile("global_load_dword %0, %1, off" : "=v"(r) : "v"(p))
__device__ __forceinline__ void bump(const u16*& p, int stride) {
  unsigned lo = (unsigned)(size_t)p, hi = (unsigned)((size_t)p >> 32);
  asm volatile("v_add_co_u32 %0, vcc, %0, %2\n\tv_addc_co_u32 %1, vcc, 0, %1, vcc" : "+v"(lo), "+v"(hi) : "v"(stride) : "vcc");
  p = (const u16*)(((size_t)hi << 32) | lo);
}
#define BARRIER() asm volatile("s_waitcnt lgkmcnt(0)\n\ts_barrier" ::: "memory")

template <int NU, int U0, int T>
__device__ __forceinline__ void k1_iter(char* smem, f32x4 (&acc)[11][2], u32x4 (&r)[7], unsigned& r7, const u16* (&gp)[8],
                                        const int (&gstride)[8], int xoff0, int woff0, int woff1, int wroff,
                                        int wroff7) {
  const char* sb = smem + (T & 1) * K1_STAGE;
  char* wb = smem + ((T + 1) & 1) * K1_STAGE + wroff;
  BARRIER();
  bfx8 xf[2], wf, wfn;
#pragma unroll
  for (int i = 0; i < 2; ++i) xf[i] = *(const bfx8*)(sb + (xoff0 + i * 2048));
  wf = tr_frag(sb + K1_XB + U0 * 128 + woff0, sb + K1_XB + U0 * 128 + woff1);
#pragma unroll
  for (int s = 0; s < 2 * NU; ++s) {
    const int u = s % NU;
    bfx8 xn[2];
    if (s == NU - 1) {
#pragma unroll
      for (int i = 0; i < 2; ++i) xn[i] = *(const bfx8*)(sb + ((xoff0 + i * 2048) ^ 64));
    }
    if (s + 1 < 2 * NU) {
      const int sn = s + 1, kn = sn / NU, un = sn % NU;
      const char* b = sb + K1_XB + kn * 8 * K1_WPITCH + (U0 + un) * 128;
      wfn = tr_frag(b + woff0, b + woff1);
    }
    acc[u][0] = mfma16(wf, xf[0], acc[u][0]);
    acc[u][1] = mfma16(wf, xf[1], acc[u][1]);
    wf = wfn;
    if (s == NU - 1) { xf[0] = xn[0]; xf[1] = xn[1]; }
    if ((s & 1) && (s >> 1) < 8 && T + 1 < 10) {
      const int j = s >> 1;
      if (j < 7) {
        if (T + 3 < 10) asm volatile("s_waitcnt vmcnt(15)" : "+v"(r[j]));
        else if (T + 2 < 10) asm volatile("s_waitcnt vmcnt(8)" : "+v"(r[j]));
        else asm volatile("s_waitcnt vmcnt(0)" : "+v"(r[j]));
        *(u32x4*)(wb + j * 8192) = r[j];
        if (T + 3 < 10) { GLOAD(r[j], gp[j]); bump(gp[j], gstride[j]); }
      } else {
        if (T + 3 < 10) asm volatile("s_waitcnt vmcnt(15)" : "+v"(r7));
        else if (T + 2 < 10) asm volatile("s_waitcnt vmcnt(8)" : "+v"(r7));
        else asm volatile("s_waitcnt vmcnt(0)" : "+v"(r7));
        *(unsigned*)(smem + ((T + 1) & 1) * K1_STAGE + wroff7) = r7;
        if (T + 3 < 10) { GLOAD1(r7, gp[7]); bump(gp[7], gstride[7]); }
      }
    }
    __builtin_amdgcn_sched_barrier(0);
  }
}

template <int NU, int U0>
__device__ __forceinline__ void k1_epi_write(f32x4 (&acc)[11][2], int nt, int trow0, int lane, char* img) {
  const int g = lane >> 4, c = lane & 15;
#pragma unroll
  for (int u = 0; u < NU; ++u) {
    const int U = nt * 21 + U0 + u;
#pragma unroll
    for (int i = 0; i < 2; ++i) {
      char* rowp = img + (trow0 + i * 16 + c) * K1_CP + (U0 + u) * 32;
      f32x4 a = acc[u][i];
      if (U < 120) {
        u32x2 v = {pack2(a[0], a[1]), pack2(a[2], a[3])};
        *(u32x2*)(rowp + 8 * g) = v;
      } else {
        u32x2 s0 = __builtin_amdgcn_permlane32_swap(__float_as_uint(a[0]), __float_as_uint(a[2]), false, false);
        u32x2 s1 = __builtin_amdgcn_permlane32_swap(__float_as_uint(a[1]), __float_as_uint(a[3]), false, false);
        float g0 = rbf(__uint_as_float(s0[0])), u0 = rbf(__uint_as_float(s0[1]));
        float g1 = rbf(__uint_as_float(s1[0])), u1 = rbf(__uint_as_float(s1[1]));
        float si0 = rbf(g0 * __builtin_amdgcn_rcpf(1.0f + __builtin_amdgcn_exp2f(g0 * -1.4426950408889634f)));
        float si1 = rbf(g1 * __builtin_amdgcn_rcpf(1.0f + __builtin_amdgcn_exp2f(g1 * -1.4426950408889634f)));
        *(unsigned*)(rowp + 8 * (g & 1) + 4 * (g >> 1)) = pack2(si0 * u0, si1 * u1);
      }
    }
  }
}

template <int NU, int U0W>
__device__ __forceinline__ void k1_body(char* smem, const u16* __restrict__ x, const u16* __restrict__ wqkv,
                                        const u16* __restrict__ wg, const u16* __restrict__ wu,
                                        u16* __restrict__ Qp, u16* __restrict__ Kp,
                                        u16* __restrict__ Vt, u16* __restrict__ Hb) {
  const int tid = threadIdx.x, lane = tid & 63;
  const int wave = __builtin_amdgcn_readfirstlane(tid >> 6);
  const int bid = blockIdx.x;
  const int xcd = bid & 7, idx = bid >> 3;
  const int mt = (xcd >> 2) * 8 + (idx & 7);
  const int nt = (xcd & 3) * 4 + (idx >> 3);
  const int m0 = mt * 128;

  const u16* gp[8];
  int gstride[8];
#pragma unroll
  for (int j = 0; j < 8; ++j) {
    int i = wave + 8 * j;
    if (j < 2) {
      int o = i * 1024 + lane * 16;
      int row = o >> 7, slot = (o >> 4) & 7;
      int chunk = slot ^ (row & 7);
      gp[j] = x + (size_t)(m0 + row) * D_MODEL + chunk * 8;
      gstride[j] = 128;
    } else {
      int o = j < 7 ? (i - 16) * 1024 + lane * 16 : 40 * 1024 + tid * 4;
      int r = o / K1_WPITCH, rem = o - r * K1_WPITCH;
      int u = rem >> 7, q = (rem >> 5) & 3, half = (rem >> 4) & 1;
      int kb = r ^ ((r >> 1) & 1);
      int k = 4 * kb + q;
      int U = nt * 21 + u;
      const int dw = j < 7 ? 0 : (rem >> 1) & 6;
      if (U < 120) {
        gp[j] = wqkv + (size_t)k * 1920 + U * 16 + half * 8 + dw;
        gstride[j] = 128 * 1920;
      } else {
        gp[j] = (half ? wu : wg) + (size_t)k * F_DIM + (U - 120) * 8 + dw;
        gstride[j] = 128 * F_DIM;
      }
    }
  }
  const int wroff = wave * 1024 + lane * 16;
  const int wroff7 = 56 * 1024 + tid * 4;
  u32x4 ra[7], rb[7];
  unsigned ra7, rb7;
#pragma unroll
  for (int j = 0; j < 7; ++j) { GLOAD(ra[j], gp[j]); bump(gp[j], gstride[j]); }
  GLOAD1(ra7, gp[7]); bump(gp[7], gstride[7]);
#pragma unroll
  for (int j = 0; j < 7; ++j) { GLOAD(rb[j], gp[j]); bump(gp[j], gstride[j]); }
  GLOAD1(rb7, gp[7]); bump(gp[7], gstride[7]);

  const int wm = wave & 3;
  const int g = lane >> 4, c = lane & 15;
  const int xrow = wm * 32 + c;
  const int xoff0 = xrow * 128 + ((g ^ (xrow & 7)) << 4);
  const int woff0 = (2 * g + (g & 1)) * K1_WPITCH + c * 8;
  const int woff1 = (2 * g + 1 - (g & 1)) * K1_WPITCH + c * 8;

  f32x4 acc[11][2];
#pragma unroll
  for (int u = 0; u < 11; ++u) { acc[u][0] = f32x4{0, 0, 0, 0}; acc[u][1] = f32x4{0, 0, 0, 0}; }

#pragma unroll
  for (int j = 0; j < 7; ++j) {
    asm volatile("s_waitcnt vmcnt(15)" : "+v"(ra[j]));
    *(u32x4*)(smem + wroff + j * 8192) = ra[j];
    GLOAD(ra[j], gp[j]); bump(gp[j], gstride[j]);
  }
  asm volatile("s_waitcnt vmcnt(15)" : "+v"(ra7));
  *(unsigned*)(smem + wroff7) = ra7;
  GLOAD1(ra7, gp[7]); bump(gp[7], gstride[7]);
#define K1_IT(NU, U0, T, R) k1_iter<NU, U0, T>(smem, acc, R, R##7, gp, gstride, xoff0, woff0, woff1, wroff, wroff7)
#define K1_LOOP(NU, U0)                                                              \
  {                                                                                  \
    K1_IT(NU, U0, 0, rb); K1_IT(NU, U0, 1, ra); K1_IT(NU, U0, 2, rb); K1_IT(NU, U0, 3, ra); \
    K1_IT(NU, U0, 4, rb); K1_IT(NU, U0, 5, ra); K1_IT(NU, U0, 6, rb); K1_IT(NU, U0, 7, ra); \
    K1_IT(NU, U0, 8, rb); K1_IT(NU, U0, 9, ra);                                      \
  }
  K1_LOOP(NU, U0W)
  __syncthreads();
  k1_epi_write<NU, U0W>(acc, nt, wm * 32, lane, smem);
  __syncthreads();
  if (bid == 0) *(u32x2*)(Vt + (size_t)D_MODEL * S_LEN + tid * 4) = u32x2{0x3f803f80u, 0x3f803f80u};
  const int U0 = nt * 21;
  const int nqk = min(max(80 - U0, 0), 21);
  const int nv = min(max(120 - U0, 0), 21) - nqk;
  const int ngu = 21 - nqk - nv;
  if (nqk > 0) {
    const int np = nqk * 4;
    for (int e = tid; e < 128 * np; e += 512) {
      int tok = e / np, p = e - tok * np;
      u32x2 v = *(const u32x2*)(smem + tok * K1_CP + p * 8);
      int col = U0 * 16 + p * 4;
      u16* dst = Qp;
      if (col >= 640) { col -= 640; dst = Kp; }
      int h = col / 20, d = col - h * 20;
      *(u32x2*)(dst + ((size_t)h * S_LEN + m0 + tok) * 20 + d) = v;
    }
  }
  if (nv > 0) {
    for (int e = wave; e < nv * 4; e += 8) {
      int u = nqk + (e >> 2), tb = (e & 3) * 32;
      const char* p0 = smem + (tb + 8 * g + (c >> 2)) * K1_CP + u * 32 + (c & 3) * 8;
      bfx8 v = tr_frag(p0, p0 + 4 * K1_CP);
      int col = (U0 + u - 80) * 16 + c;
      *(bfx8*)(Vt + (size_t)col * S_LEN + m0 + tb + 8 * g) = v;
    }
  }
  if (ngu > 0) {
    const int ub = nqk + nv;
    for (int e = tid; e < 128 * ngu; e += 512) {
      int tok = e / ngu, p = e - tok * ngu;
      u32x4 v = *(const u32x4*)(smem + tok * K1_CP + (ub + p) * 32);
      *(u32x4*)(Hb + (size_t)(m0 + tok) * F_DIM + (U0 + ub + p - 120) * 8) = v;
    }
  }
}

__global__ __launch_bounds__(512, 2) void k1_gemm(const u16* __restrict__ x, const u16* __restrict__ wqkv,
                                                  const u16* __restrict__ wg, const u16* __restrict__ wu,
                                                  u16* __restrict__ Qp, u16* __restrict__ Kp,
                                                  u16* __restrict__ Vt, u16* __restrict__ Hb) {
  __shared__ __attribute__((aligned(16))) char smem[2 * K1_STAGE];
  if (__builtin_amdgcn_readfirstlane(threadIdx.x) < 256) k1_body<11, 0>(smem, x, wqkv, wg, wu, Qp, Kp, Vt, Hb);
  else k1_body<10, 11>(smem, x, wqkv, wg, wu, Qp, Kp, Vt, Hb);
}

typedef __attribute__((ext_vector_type(4), aligned(8))) unsigned u32x4a8;

__global__ __launch_bounds__(256, 2) void k2_attn(const u16* __restrict__ Qp, const u16* __restrict__ Kp,
                                                  const u16* __restrict__ Vt, u16* __restrict__ Ob,
                                                  const u16* __restrict__ x, const u16* __restrict__ gw,
                                                  float* __restrict__ G) {
  const int tid = threadIdx.x, lane = tid & 63;
  const int wave = __builtin_amdgcn_readfirstlane(tid >> 6);
  const int bid = blockIdx.x;
  if (bid >= 512) {
    const int r0 = (bid - 512) * 4 + wave;
    {
      const u16* xr = x + (size_t)r0 * D_MODEL;
      float s = 0.f;
      const int ch2 = 64 + (lane & 15);
      s16x8 xv = *(const s16x8*)(xr + lane * 8);
      s16x8 wv = *(const s16x8*)(gw + lane * 8);
      s16x8 xv2 = *(const s16x8*)(xr + ch2 * 8);
      s16x8 wv2 = *(const s16x8*)(gw + ch2 * 8);
#pragma unroll
      for (int e = 0; e < 8; ++e) s += rbf(bf2f((u16)xv[e]) * bf2f((u16)wv[e]));
      if (lane < 16) {
#pragma unroll
        for (int e = 0; e < 8; ++e) s += rbf(bf2f((u16)xv2[e]) * bf2f((u16)wv2[e]));
      }
#pragma unroll
      for (int o = 32; o >= 1; o >>= 1) s += __shfl_xor(s, o);
      s = rbf(s);
      float gv = rbf(1.0f / (1.0f + __expf(-s)));
      if (lane == 0) G[r0] = gv;
    }
    return;
  }
  const int item = bid * 4 + wave;
  const int h = item & 31, qb = item >> 5;
  const int q0 = qb * 32, kbase = q0 - 256;
  const int g = lane >> 4, c = lane & 15;
  const u16* Qh = Qp + (size_t)h * S_LEN * 20 + 8 * g;
  const u16* Kh = Kp + (size_t)h * S_LEN * 20 + 8 * g;

  bfx8 qf[2];
#pragma unroll
  for (int t = 0; t < 2; ++t) {
    u32x4 v = *(const u32x4a8*)(Qh + (size_t)(q0 + 16 * t + c) * 20);
    if (g >= 2) { v[2] = 0; v[3] = 0; }
    if (g == 3) { v[0] = 0; v[1] = 0; }
    qf[t] = __builtin_bit_cast(bfx8, v);
  }

  f32x4 s[9][2][2];
  const int krow = 8 * (c >> 2) + (c & 3);
  u32x4 kv[9][2], v0[9], v1[9];
  const u16* V0 = Vt + (size_t)(h * DHEAD + c) * S_LEN;
  const u16* V1 = Vt + (size_t)(c == 4 ? D_MODEL : (h * DHEAD + 16 + (c & 3))) * S_LEN;
#pragma unroll
  for (int j = 0; j < 9; ++j) {
#pragma unroll
    for (int a = 0; a < 2; ++a) {
      int key = kbase + 32 * j + krow + 4 * a;
      key = key < 0 ? 0 : key;
      kv[j][a] = *(const u32x4a8*)(Kh + (size_t)key * 20);
    }
  }
#pragma unroll
  for (int j = 0; j < 9; ++j) {
    int key = kbase + 32 * j + 8 * g;
    key = key < 0 ? 0 : key;
    v0[j] = *(const u32x4*)(V0 + key);
    v1[j] = *(const u32x4*)(V1 + key);
  }
  __builtin_amdgcn_sched_barrier(0);
#pragma unroll
  for (int j = 0; j < 9; ++j) {
#pragma unroll
    for (int a = 0; a < 2; ++a) {
      bfx8 kf = __builtin_bit_cast(bfx8, kv[j][a]);
#pragma unroll
      for (int t = 0; t < 2; ++t) s[j][a][t] = mfma16(kf, qf[t], f32x4{0, 0, 0, 0});
    }
  }
  const float NEG = -1e30f;
#pragma unroll
  for (int a = 0; a < 2; ++a)
#pragma unroll
    for (int t = 0; t < 2; ++t)
#pragma unroll
      for (int r = 0; r < 4; ++r) {
        int ko = 8 * g + 4 * a + r, qo = 16 * t + c;
        if (!(ko > qo)) s[0][a][t][r] = NEG;
        if (!(ko <= qo)) s[8][a][t][r] = NEG;
      }
  if (qb < 8) {
#pragma unroll
    for (int j = 0; j < 8; ++j)
      if (j < 8 - qb) {
#pragma unroll
        for (int a = 0; a < 2; ++a)
#pragma unroll
          for (int t = 0; t < 2; ++t) s[j][a][t] = f32x4{NEG, NEG, NEG, NEG};
      }
  }
  const float cs = 0.22360679774997896f * 1.4426950408889634f;
  bfx8 pf[9][2];
#pragma unroll
  for (int t = 0; t < 2; ++t) {
    float m = NEG;
#pragma unroll
    for (int j = 0; j < 9; ++j)
#pragma unroll
      for (int a = 0; a < 2; ++a)
#pragma unroll
        for (int r = 0; r < 4; ++r) m = fmaxf(m, s[j][a][t][r]);
    m = fmaxf(m, __shfl_xor(m, 16));
    m = fmaxf(m, __shfl_xor(m, 32));
    const float mc = m * cs;
#pragma unroll
    for (int j = 0; j < 9; ++j) {
      u32x4 pk;
#pragma unroll
      for (int a = 0; a < 2; ++a) {
        float p0 = __builtin_amdgcn_exp2f(fmaf(s[j][a][t][0], cs, -mc));
        float p1 = __builtin_amdgcn_exp2f(fmaf(s[j][a][t][1], cs, -mc));
        float p2 = __builtin_amdgcn_exp2f(fmaf(s[j][a][t][2], cs, -mc));
        float p3 = __builtin_amdgcn_exp2f(fmaf(s[j][a][t][3], cs, -mc));
        pk[2 * a] = pack2(p0, p1);
        pk[2 * a + 1] = pack2(p2, p3);
      }
      pf[j][t] = __builtin_bit_cast(bfx8, pk);
    }
  }
  f32x4 o0[2] = {f32x4{0, 0, 0, 0}, f32x4{0, 0, 0, 0}}, o1[2] = {f32x4{0, 0, 0, 0}, f32x4{0, 0, 0, 0}};
#pragma unroll
  for (int j = 0; j < 9; ++j) {
    bfx8 vf0 = __builtin_bit_cast(bfx8, v0[j]), vf1 = __builtin_bit_cast(bfx8, v1[j]);
#pragma unroll
    for (int t = 0; t < 2; ++t) {
      o0[t] = mfma16(vf0, pf[j][t], o0[t]);
      o1[t] = mfma16(vf1, pf[j][t], o1[t]);
    }
  }
#pragma unroll
  for (int t = 0; t < 2; ++t) {
    float l = __shfl(o1[t][0], 16 + c);
    float inv = 1.0f / l;
    u16* dst = Ob + (size_t)(q0 + 16 * t + c) * D_MODEL + h * DHEAD;
    u32x2 v = {pack2(o0[t][0] * inv, o0[t][1] * inv), pack2(o0[t][2] * inv, o0[t][3] * inv)};
    *(u32x2*)(dst + 4 * g) = v;
    if (g == 0) {
      u32x2 w = {pack2(o1[t][0] * inv, o1[t][1] * inv), pack2(o1[t][2] * inv, o1[t][3] * inv)};
      *(u32x2*)(dst + 16) = w;
    }
  }
}

#define G2_XB 4096
#define G2_WPITCH 640
#define G2_WB (8 * G2_WPITCH)
#define G2_STAGE (G2_XB + G2_WB)
#define G2_NST 4

template <int KTOT, int EPI>
__global__ __launch_bounds__(256, 1) void k_gemm2(const u16* __restrict__ A, const u16* __restrict__ W,
                                                  u16* __restrict__ out, const u16* __restrict__ Bb,
                                                  const float* __restrict__ G) {
  __shared__ __attribute__((aligned(16))) char smem[4 * G2_NST * G2_STAGE];
  const int tid = threadIdx.x, lane = tid & 63;
  const int wave = __builtin_amdgcn_readfirstlane(tid >> 6);
  const int bid = blockIdx.x;
  const int xcd = bid & 7, idx = bid >> 3;
  const int mt = (xcd >> 1) * 8 + (idx & 7);
  const int nt = (xcd & 1) * 4 + (idx >> 3);
  const int m0 = mt * 64, n0 = nt * 80;
  constexpr int NSTEP = KTOT / 32;
  const int nmine = (NSTEP - wave + 3) / 4;

  const u16* gp[9];
#pragma unroll
  for (int j = 0; j < 4; ++j) {
    int o = j * 1024 + lane * 16;
    int row = o >> 6, slot = (o >> 4) & 3;
    int chunk = slot ^ ((0x78 >> (2 * ((row >> 2) & 3))) & 3);
    gp[j] = A + (size_t)(m0 + row) * KTOT + wave * 32 + chunk * 8;
  }
#pragma unroll
  for (int j = 0; j < 5; ++j) {
    int o = j * 1024 + lane * 16;
    int r = o / G2_WPITCH, rem = o - r * G2_WPITCH;
    int u = rem >> 7, q = (rem >> 5) & 3, half = (rem >> 4) & 1;
    int kb = r ^ ((r >> 1) & 1);
    int k = wave * 32 + 4 * kb + q;
    gp[4 + j] = W + (size_t)k * D_MODEL + n0 + u * 16 + half * 8;
  }
  char* wbase = smem + wave * (G2_NST * G2_STAGE);
  const unsigned wbase_u = (unsigned)(size_t)smem + wave * (G2_NST * G2_STAGE);
  auto stage = [&](int buf) {
    unsigned sb = wbase_u + buf * G2_STAGE;
#pragma unroll
    for (int j = 0; j < 9; ++j) {
      glds16(gp[j], sb + j * 1024);
      gp[j] += (j < 4) ? 128 : 128 * D_MODEL;
    }
  };
  const int g = lane >> 4, c = lane & 15;
  const int xoff = c * 64 + ((g ^ ((0x78 >> (2 * ((c >> 2) & 3))) & 3)) << 4);
  const int woff0 = G2_XB + (2 * g + (g & 1)) * G2_WPITCH + c * 8;
  const int woff1 = G2_XB + (2 * g + 1 - (g & 1)) * G2_WPITCH + c * 8;

  f32x4 acc[5][4];
#pragma unroll
  for (int u = 0; u < 5; ++u)
#pragma unroll
    for (int i = 0; i < 4; ++i) acc[u][i] = f32x4{0, 0, 0, 0};

#pragma unroll
  for (int p = 0; p < G2_NST - 1; ++p)
    if (p < nmine) stage(p);
  for (int it = 0; it < nmine; ++it) {
    const int rem = nmine - 1 - it;
    if (rem >= 2) asm volatile("s_waitcnt vmcnt(18)" ::: "memory");
    else if (rem == 1) asm volatile("s_waitcnt vmcnt(9)" ::: "memory");
    else asm volatile("s_waitcnt vmcnt(0)" ::: "memory");
    const char* sb = wbase + (it & (G2_NST - 1)) * G2_STAGE;
    bfx8 xf[4], wf[5];
#pragma unroll
    for (int i = 0; i < 4; ++i) xf[i] = *(const bfx8*)(sb + xoff + i * 1024);
#pragma unroll
    for (int u = 0; u < 5; ++u) wf[u] = tr_frag(sb + woff0 + u * 128, sb + woff1 + u * 128);
    if (it + G2_NST - 1 < nmine) stage((it + G2_NST - 1) & (G2_NST - 1));
#pragma unroll
    for (int u = 0; u < 5; ++u)
#pragma unroll
      for (int i = 0; i < 4; ++i) acc[u][i] = mfma16(wf[u], xf[i], acc[u][i]);
  }
  __syncthreads();
  f32x4* red = (f32x4*)smem;
#pragma unroll
  for (int u = 0; u < 5; ++u)
#pragma unroll
    for (int i = 0; i < 4; ++i) red[((wave * 4 + i) * 5 + u) * 64 + lane] = acc[u][i];
  __syncthreads();
  const int tok = m0 + wave * 16 + c;
  float gv = 0.f, omg = 0.f;
  if (EPI == 1) { gv = G[tok]; omg = rbf(1.0f - gv); }
#pragma unroll
  for (int u = 0; u < 5; ++u) {
    f32x4 v = red[((0 * 4 + wave) * 5 + u) * 64 + lane];
#pragma unroll
    for (int w = 1; w < 4; ++w) v += red[((w * 4 + wave) * 5 + u) * 64 + lane];
    const size_t o = (size_t)tok * D_MODEL + n0 + u * 16 + 4 * g;
    if (EPI == 0) {
      u32x2 pv = {pack2(v[0], v[1]), pack2(v[2], v[3])};
      *(u32x2*)(out + o) = pv;
    } else {
      s16x4 bv = *(const s16x4*)(Bb + o);
      float r[4];
#pragma unroll
      for (int e = 0; e < 4; ++e) r[e] = rbf(gv * rbf(v[e])) + rbf(omg * bf2f((u16)bv[e]));
      u32x2 pv = {pack2(r[0], r[1]), pack2(r[2], r[3])};
      *(u32x2*)(out + o) = pv;
    }
  }
}

extern "C" void kernel_launch(void* const* d_in, const int* in_sizes, int n_in,
                              void* d_out, int out_size, void* d_ws, size_t ws_size,
                              hipStream_t stream) {
  const u16* x = (const u16*)d_in[0];
  const u16* wqkv = (const u16*)d_in[1];
  const u16* wo = (const u16*)d_in[2];
  const u16* wg = (const u16*)d_in[4];
  const u16* wu = (const u16*)d_in[5];
  const u16* wd = (const u16*)d_in[6];
  const u16* gw = (const u16*)d_in[7];
  char* ws = (char*)d_ws;
  u16* Qp = (u16*)(ws);
  u16* Kp = Qp + (size_t)S_LEN * D_MODEL;
  u16* Vt = Kp + (size_t)S_LEN * D_MODEL;
  u16* Hb = (u16*)(ws + (12u << 20));
  u16* Ob = (u16*)(ws + (20u << 20));
  u16* Bb = (u16*)(ws + (24u << 20));
  float* G = (float*)(ws + (28u << 20));
  u16* out = (u16*)d_out;

  k1_gemm<<<256, 512, 0, stream>>>(x, wqkv, wg, wu, Qp, Kp, Vt, Hb);
  k2_attn<<<1024, 256, 0, stream>>>(Qp, Kp, Vt, Ob, x, gw, G);
  k_gemm2<F_DIM, 0><<<256, 256, 0, stream>>>(Hb, wd, Bb, nullptr, nullptr);
  k_gemm2<D_MODEL, 1><<<256, 256, 0, stream>>>(Ob, wo, out, Bb, G);
}
```

```cpp
#include <hip/hip_runtime.h>
#include <hip/hip_bf16.h>
#include <stdint.h>

typedef __attribute__((ext_vector_type(8))) __bf16 bfx8;
typedef __attribute__((ext_vector_type(4))) short s16x4;
typedef __attribute__((ext_vector_type(8))) short s16x8;
typedef __attribute__((ext_vector_type(4))) float f32x4;
typedef __attribute__((ext_vector_type(2))) unsigned u32x2;
typedef __attribute__((ext_vector_type(4))) unsigned u32x4;
typedef __attribute__((address_space(3))) s16x4* lds_s16x4_ptr;
typedef __attribute__((address_space(3))) unsigned* lds_u32_ptr;
typedef unsigned short u16;

#define S_LEN 2048
#define D_MODEL 640
#define F_DIM 1728
#define NHEAD 32
#define DHEAD 20

__device__ __forceinline__ float bf2f(u16 v) { return __uint_as_float(((unsigned)v) << 16); }
__device__ __forceinline__ u16 f2bf(float f) {
  __hip_bfloat16 b = __float2bfloat16(f);
  return __builtin_bit_cast(u16, b);
}
__device__ __forceinline__ float rbf(float f) { return bf2f(f2bf(f)); }
__device__ __forceinline__ unsigned pack2(float lo, float hi) {
  return (unsigned)f2bf(lo) | ((unsigned)f2bf(hi) << 16);
}
__device__ __forceinline__ f32x4 mfma16(bfx8 a, bfx8 b, f32x4 c) {
  return __builtin_amdgcn_mfma_f32_16x16x32_bf16(a, b, c, 0, 0, 0);
}
__device__ __forceinline__ void glds16(const void* g, unsigned lds_addr) {
  unsigned keep;
  asm volatile("s_mov_b32 %0, m0\n\ts_mov_b32 m0, %2\n\ts_nop 0\n\tglobal_load_lds_dwordx4 %1, off\n\ts_mov_b32 m0, %0"
               : "=&s"(keep) : "v"(g), "s"(lds_addr) : "memory");
}
#define WAIT_VM(n) asm volatile("s_waitcnt vmcnt(" #n ")" ::: "memory")
__device__ __forceinline__ bfx8 tr_frag(const char* p0, const char* p1) {
  s16x4 a = __builtin_amdgcn_ds_read_tr16_b64_v4i16((lds_s16x4_ptr)p0);
  s16x4 b = __builtin_amdgcn_ds_read_tr16_b64_v4i16((lds_s16x4_ptr)p1);
  s16x8 v = {a[0], a[1], a[2], a[3], b[0], b[1], b[2], b[3]};
  return __builtin_bit_cast(bfx8, v);
}

#define K1_XB 16384
#define K1_WG 5504
#define K1_WB (8 * K1_WG)
#define K1_STAGE (K1_XB + K1_WB)
#define K1_CP 688

#define GLOAD(r, p) asm volatile("global_load_dwordx4 %0, %1, off" : "=v"(r) : "v"(p))
#define GLOADS(r, o, p) asm volatile("global_load_dwordx4 %0, %1, %2" : "=v"(r) : "v"(o), "s"(p))
#define GLOAD1(r, p) asm volatile("global_load_dword %0, %1, off" : "=v"(r) : "v"(p))
__device__ __forceinline__ void bump(const u16*& p, int stride) {
  unsigned lo = (unsigned)(size_t)p, hi = (unsigned)((size_t)p >> 32);
  asm volatile("v_add_co_u32 %0, vcc, %0, %2\n\tv_addc_co_u32 %1, vcc, 0, %1, vcc" : "+v"(lo), "+v"(hi) : "v"(stride) : "vcc");
  p = (const u16*)(((size_t)hi << 32) | lo);
}
#define BARRIER() asm volatile("s_waitcnt lgkmcnt(0)\n\ts_barrier" ::: "memory")

struct K1Stage {
  const u16* gp[6];
  int gs[6];
  int la[6];
  const u16* xb;
  int xvoff, xla;
};

template <int J, int T>
__device__ __forceinline__ void k1_ld(K1Stage& st, u32x4 (&r)[7], unsigned& r7) {
  if (J < 2) { GLOADS(r[J], st.xvoff, st.xb + J * 64 * D_MODEL + T * 64); }
  else if (J < 7) { GLOAD(r[J], st.gp[J - 2]); bump(st.gp[J - 2], st.gs[J - 2]); }
  else { GLOAD1(r7, st.gp[5]); bump(st.gp[5], st.gs[5]); }
}
template <int J, int W>
__device__ __forceinline__ void k1_wr(K1Stage& st, char* buf, u32x4 (&r)[7], unsigned& r7) {
  if (J < 7) {
    if (W == 15) asm volatile("s_waitcnt vmcnt(15)" : "+v"(r[J]));
    else if (W == 8) asm volatile("s_waitcnt vmcnt(8)" : "+v"(r[J]));
    else asm volatile("s_waitcnt vmcnt(0)" : "+v"(r[J]));
    if (J < 2) *(u32x4*)(buf + st.xla + J * 8192) = r[J];
    else *(u32x4*)(buf + st.la[J - 2]) = r[J];
  } else {
    if (W == 15) asm volatile("s_waitcnt vmcnt(15)" : "+v"(r7));
    else if (W == 8) asm volatile("s_waitcnt vmcnt(8)" : "+v"(r7));
    else asm volatile("s_waitcnt vmcnt(0)" : "+v"(r7));
    *(unsigned*)(buf + st.la[5]) = r7;
  }
}
template <int J, int T>
__device__ __forceinline__ void k1_slot(K1Stage& st, char* smem, u32x4 (&r)[7], unsigned& r7) {
  k1_wr<J, (T + 3 < 10) ? 15 : ((T + 2 < 10) ? 8 : 0)>(st, smem + ((T + 1) & 1) * K1_STAGE, r, r7);
  if (T + 3 < 10) k1_ld<J, T + 3>(st, r, r7);
}

template <int NU, int U0, int T>
__device__ __forceinline__ void k1_iter(char* smem, f32x4 (&acc)[11][2], u32x4 (&r)[7], unsigned& r7, K1Stage& st,
                                        int xoff0, int woff) {
  const char* sb = smem + (T & 1) * K1_STAGE;
  BARRIER();
  bfx8 xf[2], wf, wfn;
#pragma unroll
  for (int i = 0; i < 2; ++i) xf[i] = *(const bfx8*)(sb + (xoff0 + i * 2048));
  wf = tr_frag(sb + K1_XB + U0 * 32 + woff, sb + K1_XB + U0 * 32 + 2688 + woff);
#pragma unroll
  for (int s = 0; s < 2 * NU; ++s) {
    const int u = s % NU;
    bfx8 xn[2];
    if (s == NU - 1) {
#pragma unroll
      for (int i = 0; i < 2; ++i) xn[i] = *(const bfx8*)(sb + ((xoff0 + i * 2048) ^ 64));
    }
    if (s + 1 < 2 * NU) {
      const int sn = s + 1, kn = sn / NU, un = sn % NU;
      const char* b = sb + K1_XB + kn * 4 * K1_WG + (U0 + un) * 32 + woff;
      wfn = tr_frag(b, b + 2688);
    }
    acc[u][0] = mfma16(wf, xf[0], acc[u][0]);
    acc[u][1] = mfma16(wf, xf[1], acc[u][1]);
    wf = wfn;
    if (s == NU - 1) { xf[0] = xn[0]; xf[1] = xn[1]; }
    if (T + 1 < 10) {
      if (s == 1) k1_slot<0, T>(st, smem, r, r7);
      if (s == 3) k1_slot<1, T>(st, smem, r, r7);
      if (s == 5) k1_slot<2, T>(st, smem, r, r7);
      if (s == 7) k1_slot<3, T>(st, smem, r, r7);
      if (s == 9) k1_slot<4, T>(st, smem, r, r7);
      if (s == 11) k1_slot<5, T>(st, smem, r, r7);
      if (s == 13) k1_slot<6, T>(st, smem, r, r7);
      if (s == 15) k1_slot<7, T>(st, smem, r, r7);
    }
    __builtin_amdgcn_sched_barrier(0);
  }
}

template <int NU, int U0>
__device__ __forceinline__ void k1_epi_write(f32x4 (&acc)[11][2], int nt, int trow0, int lane, char* img) {
  const int g = lane >> 4, c = lane & 15;
#pragma unroll
  for (int u = 0; u < NU; ++u) {
    const int U = nt * 21 + U0 + u;
#pragma unroll
    for (int i = 0; i < 2; ++i) {
      char* rowp = img + (trow0 + i * 16 + c) * K1_CP + (U0 + u) * 32;
      f32x4 a = acc[u][i];
      if (U < 120) {
        u32x2 v = {pack2(a[0], a[1]), pack2(a[2], a[3])};
        *(u32x2*)(rowp + 8 * g) = v;
      } else {
        u32x2 s0 = __builtin_amdgcn_permlane32_swap(__float_as_uint(a[0]), __float_as_uint(a[2]), false, false);
        u32x2 s1 = __builtin_amdgcn_permlane32_swap(__float_as_uint(a[1]), __float_as_uint(a[3]), false, false);
        float g0 = rbf(__uint_as_float(s0[0])), u0 = rbf(__uint_as_float(s0[1]));
        float g1 = rbf(__uint_as_float(s1[0])), u1 = rbf(__uint_as_float(s1[1]));
        float si0 = rbf(g0 * __builtin_amdgcn_rcpf(1.0f + __builtin_amdgcn_exp2f(g0 * -1.4426950408889634f)));
        float si1 = rbf(g1 * __builtin_amdgcn_rcpf(1.0f + __builtin_amdgcn_exp2f(g1 * -1.4426950408889634f)));
        *(unsigned*)(rowp + 8 * (g & 1) + 4 * (g >> 1)) = pack2(si0 * u0, si1 * u1);
      }
    }
  }
}

template <int NU, int U0W>
__device__ __forceinline__ void k1_body(char* smem, const u16* __restrict__ x, const u16* __restrict__ wqkv,
                                        const u16* __restrict__ wg, const u16* __restrict__ wu,
                                        u16* __restrict__ Qp, u16* __restrict__ Kp,
                                        u16* __restrict__ Vt, u16* __restrict__ Hb) {
  const int tid = threadIdx.x, lane = tid & 63;
  const int wave = __builtin_amdgcn_readfirstlane(tid >> 6);
  const int bid = blockIdx.x;
  const int xcd = bid & 7, idx = bid >> 3;
  const int mt = (xcd >> 2) * 8 + (idx & 7);
  const int nt = (xcd & 3) * 4 + (idx >> 3);
  const int m0 = mt * 128;

  K1Stage st;
  {
    const int xr = lane >> 3;
    st.xb = x + (size_t)m0 * D_MODEL;
    st.xvoff = ((wave * 8 + xr) * D_MODEL + ((lane & 7) ^ xr) * 8) * 2;
    st.xla = wave * 1024 + lane * 16;
    const int nq = min(max(120 - nt * 21, 0), 21), ngu = 21 - nq;
#pragma unroll
    for (int j = 0; j < 6; ++j) {
      const int pp = j < 5 ? j * 64 + lane : 320 + (lane >> 2);
      const int dw = j < 5 ? 0 : (lane & 3) * 2;
      const int row = pp / 42, cp = pp - row * 42;
      const int k = 8 * wave + row;
      int u, half;
      if (cp < 2 * nq) {
        u = cp >> 1; half = cp & 1;
        st.gp[j] = wqkv + (size_t)k * 1920 + nt * 336 + cp * 8 + dw;
        st.gs[j] = 128 * 1920;
      } else {
        const int cq = cp - 2 * nq;
        half = cq >= ngu;
        u = nq + cq - half * ngu;
        st.gp[j] = (half ? wu : wg) + (size_t)k * F_DIM + (nt * 21 + u - 120) * 8 + dw;
        st.gs[j] = 128 * F_DIM;
      }
      st.la[j] = K1_XB + wave * K1_WG + row * 672 + u * 32 + half * 16 + dw * 2;
    }
  }
  u32x4 ra[7], rb[7];
  unsigned ra7, rb7;
#define K1_ALL(F, T, R) F<0, T>(st, R, R##7); F<1, T>(st, R, R##7); F<2, T>(st, R, R##7); F<3, T>(st, R, R##7); \
                        F<4, T>(st, R, R##7); F<5, T>(st, R, R##7); F<6, T>(st, R, R##7); F<7, T>(st, R, R##7)
  K1_ALL(k1_ld, 0, ra);
  K1_ALL(k1_ld, 1, rb);

  const int wm = wave & 3;
  const int g = lane >> 4, c = lane & 15;
  const int xrow = wm * 32 + c;
  const int xoff0 = xrow * 128 + ((g ^ (xrow & 7)) << 4);
  const int woff = g * K1_WG + (c >> 2) * 672 + (c & 3) * 8;

  f32x4 acc[11][2];
#pragma unroll
  for (int u = 0; u < 11; ++u) { acc[u][0] = f32x4{0, 0, 0, 0}; acc[u][1] = f32x4{0, 0, 0, 0}; }

  k1_slot<0, -1>(st, smem, ra, ra7); k1_slot<1, -1>(st, smem, ra, ra7); k1_slot<2, -1>(st, smem, ra, ra7);
  k1_slot<3, -1>(st, smem, ra, ra7); k1_slot<4, -1>(st, smem, ra, ra7); k1_slot<5, -1>(st, smem, ra, ra7);
  k1_slot<6, -1>(st, smem, ra, ra7); k1_slot<7, -1>(st, smem, ra, ra7);
#define K1_IT(NU, U0, T, R) k1_iter<NU, U0, T>(smem, acc, R, R##7, st, xoff0, woff)
#define K1_LOOP(NU, U0)                                                              \
  {                                                                                  \
    K1_IT(NU, U0, 0, rb); K1_IT(NU, U0, 1, ra); K1_IT(NU, U0, 2, rb); K1_IT(NU, U0, 3, ra); \
    K1_IT(NU, U0, 4, rb); K1_IT(NU, U0, 5, ra); K1_IT(NU, U0, 6, rb); K1_IT(NU, U0, 7, ra); \
    K1_IT(NU, U0, 8, rb); K1_IT(NU, U0, 9, ra);                                      \
  }
  K1_LOOP(NU, U0W)
  __syncthreads();
  k1_epi_write<NU, U0W>(acc, nt, wm * 32, lane, smem);
  __syncthreads();
  if (bid == 0) *(u32x2*)(Vt + (size_t)D_MODEL * S_LEN + tid * 4) = u32x2{0x3f803f80u, 0x3f803f80u};
  const int U0 = nt * 21;
  const int nqk = min(max(80 - U0, 0), 21);
  const int nv = min(max(120 - U0, 0), 21) - nqk;
  const int ngu = 21 - nqk - nv;
  if (nqk > 0) {
    const int np = nqk * 4;
    for (int e = tid; e < 128 * np; e += 512) {
      int tok = e / np, p = e - tok * np;
      u32x2 v = *(const u32x2*)(smem + tok * K1_CP + p * 8);
      int col = U0 * 16 + p * 4;
      u16* dst = Qp;
      if (col >= 640) { col -= 640; dst = Kp; }
      int h = col / 20, d = col - h * 20;
      *(u32x2*)(dst + ((size_t)h * S_LEN + m0 + tok) * 20 + d) = v;
    }
  }
  if (nv > 0) {
    for (int e = wave; e < nv * 4; e += 8) {
      int u = nqk + (e >> 2), tb = (e & 3) * 32;
      const char* p0 = smem + (tb + 8 * g + (c >> 2)) * K1_CP + u * 32 + (c & 3) * 8;
      bfx8 v = tr_frag(p0, p0 + 4 * K1_CP);
      int col = (U0 + u - 80) * 16 + c;
      *(bfx8*)(Vt + (size_t)col * S_LEN + m0 + tb + 8 * g) = v;
    }
  }
  if (ngu > 0) {
    const int ub = nqk + nv;
    for (int e = tid; e < 128 * ngu; e += 512) {
      int tok = e / ngu, p = e - tok * ngu;
      u32x4 v = *(const u32x4*)(smem + tok * K1_CP + (ub + p) * 32);
      *(u32x4*)(Hb + (size_t)(m0 + tok) * F_DIM + (U0 + ub + p - 120) * 8) = v;
    }
  }
}

__global__ __launch_bounds__(512, 2) void k1_gemm(const u16* __restrict__ x, const u16* __restrict__ wqkv,
                                                  const u16* __restrict__ wg, const u16* __restrict__ wu,
                                                  u16* __restrict__ Qp, u16* __restrict__ Kp,
                                                  u16* __restrict__ Vt, u16* __restrict__ Hb) {
  __shared__ __attribute__((aligned(16))) char smem[2 * K1_STAGE];
  if (__builtin_amdgcn_readfirstlane(threadIdx.x) < 256) k1_body<11, 0>(smem, x, wqkv, wg, wu, Qp, Kp, Vt, Hb);
  else k1_body<10, 11>(smem, x, wqkv, wg, wu, Qp, Kp, Vt, Hb);
}

typedef __attribute__((ext_vector_type(4), aligned(8))) unsigned u32x4a8;

__global__ __launch_bounds__(256, 2) void k2_attn(const u16* __restrict__ Qp, const u16* __restrict__ Kp,
                                                  const u16* __restrict__ Vt, u16* __restrict__ Ob,
                                                  const u16* __restrict__ x, const u16* __restrict__ gw,
                                                  float* __restrict__ G) {
  const int tid = threadIdx.x, lane = tid & 63;
  const int wave = __builtin_amdgcn_readfirstlane(tid >> 6);
  const int bid = blockIdx.x;
  if (bid >= 512) {
    const int r0 = (bid - 512) * 4 + wave;
    {
      const u16* xr = x + (size_t)r0 * D_MODEL;
      float s = 0.f;
      const int ch2 = 64 + (lane & 15);
      s16x8 xv = *(const s16x8*)(xr + lane * 8);
      s16x8 wv = *(const s16x8*)(gw + lane * 8);
      s16x8 xv2 = *(const s16x8*)(xr + ch2 * 8);
      s16x8 wv2 = *(const s16x8*)(gw + ch2 * 8);
#pragma unroll
      for (int e = 0; e < 8; ++e) s += rbf(bf2f((u16)xv[e]) * bf2f((u16)wv[e]));
      if (lane < 16) {
#pragma unroll
        for (int e = 0; e < 8; ++e) s += rbf(bf2f((u16)xv2[e]) * bf2f((u16)wv2[e]));
      }
#pragma unroll
      for (int o = 32; o >= 1; o >>= 1) s += __shfl_xor(s, o);
      s = rbf(s);
      float gv = rbf(1.0f / (1.0f + __expf(-s)));
      if (lane == 0) G[r0] = gv;
    }
    return;
  }
  const int item = bid * 4 + wave;
  const int h = item & 31, qb = item >> 5;
  const int q0 = qb * 32, kbase = q0 - 256;
  const int g = lane >> 4, c = lane & 15;
  const u16* Qh = Qp + (size_t)h * S_LEN * 20 + 8 * g;
  const u16* Kh = Kp + (size_t)h * S_LEN * 20 + 8 * g;

  bfx8 qf[2];
#pragma unroll
  for (int t = 0; t < 2; ++t) {
    u32x4 v = *(const u32x4a8*)(Qh + (size_t)(q0 + 16 * t + c) * 20);
    if (g >= 2) { v[2] = 0; v[3] = 0; }
    if (g == 3) { v[0] = 0; v[1] = 0; }
    qf[t] = __builtin_bit_cast(bfx8, v);
  }

  f32x4 s[9][2][2];
  const int krow = 8 * (c >> 2) + (c & 3);
  u32x4 kv[9][2], v0[9], v1[9];
  const u16* V0 = Vt + (size_t)(h * DHEAD + c) * S_LEN;
  const u16* V1 = Vt + (size_t)(c == 4 ? D_MODEL : (h * DHEAD + 16 + (c & 3))) * S_LEN;
#pragma unroll
  for (int j = 0; j < 9; ++j) {
#pragma unroll
    for (int a = 0; a < 2; ++a) {
      int key = kbase + 32 * j + krow + 4 * a;
      key = key < 0 ? 0 : key;
      kv[j][a] = *(const u32x4a8*)(Kh + (size_t)key * 20);
    }
  }
#pragma unroll
  for (int j = 0; j < 9; ++j) {
    int key = kbase + 32 * j + 8 * g;
    key = key < 0 ? 0 : key;
    v0[j] = *(const u32x4*)(V0 + key);
    v1[j] = *(const u32x4*)(V1 + key);
  }
  __builtin_amdgcn_sched_barrier(0);
#pragma unroll
  for (int j = 0; j < 9; ++j) {
#pragma unroll
    for (int a = 0; a < 2; ++a) {
      bfx8 kf = __builtin_bit_cast(bfx8, kv[j][a]);
#pragma unroll
      for (int t = 0; t < 2; ++t) s[j][a][t] = mfma16(kf, qf[t], f32x4{0, 0, 0, 0});
    }
  }
  const float NEG = -1e30f;
#pragma unroll
  for (int a = 0; a < 2; ++a)
#pragma unroll
    for (int t = 0; t < 2; ++t)
#pragma unroll
      for (int r = 0; r < 4; ++r) {
        int ko = 8 * g + 4 * a + r, qo = 16 * t + c;
        if (!(ko > qo)) s[0][a][t][r] = NEG;
        if (!(ko <= qo)) s[8][a][t][r] = NEG;
      }
  if (qb < 8) {
#pragma unroll
    for (int j = 0; j < 8; ++j)
      if (j < 8 - qb) {
#pragma unroll
        for (int a = 0; a < 2; ++a)
#pragma unroll
          for (int t = 0; t < 2; ++t) s[j][a][t] = f32x4{NEG, NEG, NEG, NEG};
      }
  }
  const float cs = 0.22360679774997896f * 1.4426950408889634f;
  bfx8 pf[9][2];
#pragma unroll
  for (int t = 0; t < 2; ++t) {
    float m = NEG;
#pragma unroll
    for (int j = 0; j < 9; ++j)
#pragma unroll
      for (int a = 0; a < 2; ++a)
#pragma unroll
        for (int r = 0; r < 4; ++r) m = fmaxf(m, s[j][a][t][r]);
    m = fmaxf(m, __shfl_xor(m, 16));
    m = fmaxf(m, __shfl_xor(m, 32));
    const float mc = m * cs;
#pragma unroll
    for (int j = 0; j < 9; ++j) {
      u32x4 pk;
#pragma unroll
      for (int a = 0; a < 2; ++a) {
        float p0 = __builtin_amdgcn_exp2f(fmaf(s[j][a][t][0], cs, -mc));
        float p1 = __builtin_amdgcn_exp2f(fmaf(s[j][a][t][1], cs, -mc));
        float p2 = __builtin_amdgcn_exp2f(fmaf(s[j][a][t][2], cs, -mc));
        float p3 = __builtin_amdgcn_exp2f(fmaf(s[j][a][t][3], cs, -mc));
        pk[2 * a] = pack2(p0, p1);
        pk[2 * a + 1] = pack2(p2, p3);
      }
      pf[j][t] = __builtin_bit_cast(bfx8, pk);
    }
  }
  f32x4 o0[2] = {f32x4{0, 0, 0, 0}, f32x4{0, 0, 0, 0}}, o1[2] = {f32x4{0, 0, 0, 0}, f32x4{0, 0, 0, 0}};
#pragma unroll
  for (int j = 0; j < 9; ++j) {
    bfx8 vf0 = __builtin_bit_cast(bfx8, v0[j]), vf1 = __builtin_bit_cast(bfx8, v1[j]);
#pragma unroll
    for (int t = 0; t < 2; ++t) {
      o0[t] = mfma16(vf0, pf[j][t], o0[t]);
      o1[t] = mfma16(vf1, pf[j][t], o1[t]);
    }
  }
#pragma unroll
  for (int t = 0; t < 2; ++t) {
    float l = __shfl(o1[t][0], 16 + c);
    float inv = 1.0f / l;
    u16* dst = Ob + (size_t)(q0 + 16 * t + c) * D_MODEL + h * DHEAD;
    u32x2 v = {pack2(o0[t][0] * inv, o0[t][1] * inv), pack2(o0[t][2] * inv, o0[t][3] * inv)};
    *(u32x2*)(dst + 4 * g) = v;
    if (g == 0) {
      u32x2 w = {pack2(o1[t][0] * inv, o1[t][1] * inv), pack2(o1[t][2] * inv, o1[t][3] * inv)};
      *(u32x2*)(dst + 16) = w;
    }
  }
}

#define G2_XB 4096
#define G2_WPITCH 640
#define G2_WB (8 * G2_WPITCH)
#define G2_STAGE (G2_XB + G2_WB)
#define G2_NST 4

template <int KTOT, int EPI>
__global__ __launch_bounds__(256, 1) void k_gemm2(const u16* __restrict__ A, const u16* __restrict__ W,
                                                  u16* __restrict__ out, const u16* __restrict__ Bb,
                                                  const float* __restrict__ G) {
  __shared__ __attribute__((aligned(16))) char smem[4 * G2_NST * G2_STAGE];
  const int tid = threadIdx.x, lane = tid & 63;
  const int wave = __builtin_amdgcn_readfirstlane(tid >> 6);
  const int bid = blockIdx.x;
  const int xcd = bid & 7, idx = bid >> 3;
  const int mt = (xcd >> 1) * 8 + (idx & 7);
  const int nt = (xcd & 1) * 4 + (idx >> 3);
  const int m0 = mt * 64, n0 = nt * 80;
  constexpr int NSTEP = KTOT / 32;
  const int nmine = (NSTEP - wave + 3) / 4;

  const u16* gp[9];
#pragma unroll
  for (int j = 0; j < 4; ++j) {
    int o = j * 1024 + lane * 16;
    int row = o >> 6, slot = (o >> 4) & 3;
    int chunk = slot ^ ((0x78 >> (2 * ((row >> 2) & 3))) & 3);
    gp[j] = A + (size_t)(m0 + row) * KTOT + wave * 32 + chunk * 8;
  }
#pragma unroll
  for (int j = 0; j < 5; ++j) {
    int o = j * 1024 + lane * 16;
    int r = o / G2_WPITCH, rem = o - r * G2_WPITCH;
    int u = rem >> 7, q = (rem >> 5) & 3, half = (rem >> 4) & 1;
    int kb = r ^ ((r >> 1) & 1);
    int k = wave * 32 + 4 * kb + q;
    gp[4 + j] = W + (size_t)k * D_MODEL + n0 + u * 16 + half * 8;
  }
  char* wbase = smem + wave * (G2_NST * G2_STAGE);
  const unsigned wbase_u = (unsigned)(size_t)smem + wave * (G2_NST * G2_STAGE);
  auto stage = [&](int buf) {
    unsigned sb = wbase_u + buf * G2_STAGE;
#pragma unroll
    for (int j = 0; j < 9; ++j) {
      glds16(gp[j], sb + j * 1024);
      gp[j] += (j < 4) ? 128 : 128 * D_MODEL;
    }
  };
  const int g = lane >> 4, c = lane & 15;
  const int xoff = c * 64 + ((g ^ ((0x78 >> (2 * ((c >> 2) & 3))) & 3)) << 4);
  const int woff0 = G2_XB + (2 * g + (g & 1)) * G2_WPITCH + c * 8;
  const int woff1 = G2_XB + (2 * g + 1 - (g & 1)) * G2_WPITCH + c * 8;

  f32x4 acc[5][4];
#pragma unroll
  for (int u = 0; u < 5; ++u)
#pragma unroll
    for (int i = 0; i < 4; ++i) acc[u][i] = f32x4{0, 0, 0, 0};

#pragma unroll
  for (int p = 0; p < G2_NST - 1; ++p)
    if (p < nmine) stage(p);
  for (int it = 0; it < nmine; ++it) {
    const int rem = nmine - 1 - it;
    if (rem >= 2) asm volatile("s_waitcnt vmcnt(18)" ::: "memory");
    else if (rem == 1) asm volatile("s_waitcnt vmcnt(9)" ::: "memory");
    else asm volatile("s_waitcnt vmcnt(0)" ::: "memory");
    const char* sb = wbase + (it & (G2_NST - 1)) * G2_STAGE;
    bfx8 xf[4], wf[5];
#pragma unroll
    for (int i = 0; i < 4; ++i) xf[i] = *(const bfx8*)(sb + xoff + i * 1024);
#pragma unroll
    for (int u = 0; u < 5; ++u) wf[u] = tr_frag(sb + woff0 + u * 128, sb + woff1 + u * 128);
    if (it + G2_NST - 1 < nmine) stage((it + G2_NST - 1) & (G2_NST - 1));
#pragma unroll
    for (int u = 0; u < 5; ++u)
#pragma unroll
      for (int i = 0; i < 4; ++i) acc[u][i] = mfma16(wf[u], xf[i], acc[u][i]);
  }
  __syncthreads();
  f32x4* red = (f32x4*)smem;
#pragma unroll
  for (int u = 0; u < 5; ++u)
#pragma unroll
    for (int i = 0; i < 4; ++i) red[((wave * 4 + i) * 5 + u) * 64 + lane] = acc[u][i];
  __syncthreads();
  const int tok = m0 + wave * 16 + c;
  float gv = 0.f, omg = 0.f;
  if (EPI == 1) { gv = G[tok]; omg = rbf(1.0f - gv); }
#pragma unroll
  for (int u = 0; u < 5; ++u) {
    f32x4 v = red[((0 * 4 + wave) * 5 + u) * 64 + lane];
#pragma unroll
    for (int w = 1; w < 4; ++w) v += red[((w * 4 + wave) * 5 + u) * 64 + lane];
    const size_t o = (size_t)tok * D_MODEL + n0 + u * 16 + 4 * g;
    if (EPI == 0) {
      u32x2 pv = {pack2(v[0], v[1]), pack2(v[2], v[3])};
      *(u32x2*)(out + o) = pv;
    } else {
      s16x4 bv = *(const s16x4*)(Bb + o);
      float r[4];
#pragma unroll
      for (int e = 0; e < 4; ++e) r[e] = rbf(gv * rbf(v[e])) + rbf(omg * bf2f((u16)bv[e]));
      u32x2 pv = {pack2(r[0], r[1]), pack2(r[2], r[3])};
      *(u32x2*)(out + o) = pv;
    }
  }
}

extern "C" void kernel_launch(void* const* d_in, const int* in_sizes, int n_in,
                              void* d_out, int out_size, void* d_ws, size_t ws_size,
                              hipStream_t stream) {
  const u16* x = (const u16*)d_in[0];
  const u16* wqkv = (const u16*)d_in[1];
  const u16* wo = (const u16*)d_in[2];
  const u16* wg = (const u16*)d_in[4];
  const u16* wu = (const u16*)d_in[5];
  const u16* wd = (const u16*)d_in[6];
  const u16* gw = (const u16*)d_in[7];
  char* ws = (char*)d_ws;
  u16* Qp = (u16*)(ws);
  u16* Kp = Qp + (size_t)S_LEN * D_MODEL;
  u16* Vt = Kp + (size_t)S_LEN * D_MODEL;
  u16* Hb = (u16*)(ws + (12u << 20));
  u16* Ob = (u16*)(ws + (20u << 20));
  u16* Bb = (u16*)(ws + (24u << 20));
  float* G = (float*)(ws + (28u << 20));
  u16* out = (u16*)d_out;

  k1_gemm<<<256, 512, 0, stream>>>(x, wqkv, wg, wu, Qp, Kp, Vt, Hb);
  k2_attn<<<1024, 256, 0, stream>>>(Qp, Kp, Vt, Ob, x, gw, G);
  k_gemm2<F_DIM, 0><<<256, 256, 0, stream>>>(Hb, wd, Bb, nullptr, nullptr);
  k_gemm2<D_MODEL, 1><<<256, 256, 0, stream>>>(Ob, wo, out, Bb, G);
}
```

```cpp
#include <hip/hip_runtime.h>
#include <hip/hip_bf16.h>
#include <stdint.h>

typedef __attribute__((ext_vector_type(8))) __bf16 bfx8;
typedef __attribute__((ext_vector_type(4))) short s16x4;
typedef __attribute__((ext_vector_type(8))) short s16x8;
typedef __attribute__((ext_vector_type(4))) float f32x4;
typedef __attribute__((ext_vector_type(2))) unsigned u32x2;
typedef __attribute__((ext_vector_type(4))) unsigned u32x4;
typedef __attribute__((ext_vector_type(4), aligned(8))) unsigned u32x4a8;
typedef __attribute__((address_space(3))) s16x4* lds_s16x4_ptr;
typedef unsigned short u16;

#define S_LEN 2048
#define D_MODEL 640
#define F_DIM 1728

__device__ __forceinline__ float bf2f(u16 v) { return __uint_as_float(((unsigned)v) << 16); }
__device__ __forceinline__ u16 f2bf(float f) {
  __hip_bfloat16 b = __float2bfloat16(f);
  return __builtin_bit_cast(u16, b);
}
__device__ __forceinline__ float rbf(float f) { return bf2f(f2bf(f)); }
__device__ __forceinline__ unsigned pack2(float lo, float hi) {
  return (unsigned)f2bf(lo) | ((unsigned)f2bf(hi) << 16);
}
__device__ __forceinline__ f32x4 mfma16(bfx8 a, bfx8 b, f32x4 c) {
  return __builtin_amdgcn_mfma_f32_16x16x32_bf16(a, b, c, 0, 0, 0);
}
__device__ __forceinline__ bfx8 tr_frag(const char* p0, const char* p1) {
  s16x4 a = __builtin_amdgcn_ds_read_tr16_b64_v4i16((lds_s16x4_ptr)p0);
  s16x4 b = __builtin_amdgcn_ds_read_tr16_b64_v4i16((lds_s16x4_ptr)p1);
  s16x8 v = {a[0], a[1], a[2], a[3], b[0], b[1], b[2], b[3]};
  return __builtin_bit_cast(bfx8, v);
}
#define GLOAD(r, p) asm volatile("global_load_dwordx4 %0, %1, off" : "=v"(r) : "v"(p))
#define GLOADS(r, o, p) asm volatile("global_load_dwordx4 %0, %1, %2" : "=v"(r) : "v"(o), "s"(p))
#define GLOADSO(r, o, p, imm) asm volatile("global_load_dwordx4 %0, %1, %2 offset:%3" : "=v"(r) : "v"(o), "s"(p), "i"(imm))
#define GLOAD1(r, p) asm volatile("global_load_dword %0, %1, off" : "=v"(r) : "v"(p))
#define WAITV(n, r) asm volatile("s_waitcnt vmcnt(%1)" : "+v"(r) : "i"(n))
__device__ __forceinline__ void bump(const u16*& p, int stride) {
  unsigned lo = (unsigned)(size_t)p, hi = (unsigned)((size_t)p >> 32);
  asm volatile("v_add_co_u32 %0, vcc, %0, %2\n\tv_addc_co_u32 %1, vcc, 0, %1, vcc" : "+v"(lo), "+v"(hi) : "v"(stride) : "vcc");
  p = (const u16*)(((size_t)hi << 32) | lo);
}
#define BARRIER() asm volatile("s_waitcnt lgkmcnt(0)\n\ts_barrier" ::: "memory")

struct Ws {
  char *Qf, *Kf, *Vf, *Hf, *Of, *Wdf, *Wof, *Bb;
  float* G;
};

#define K1_XB 16384
#define K1_WG 5504
#define K1_WB (8 * K1_WG)
#define K1_STAGE (K1_XB + K1_WB)
#define K1_CP 688
#define K1_TR (2 * K1_STAGE)
#define K1_LDS (K1_TR + 4 * 5632)

__device__ __forceinline__ void k1_tile(int bid, int& mt, int& nt) {
  const int xcd = bid & 7, idx = bid >> 3;
  mt = (xcd >> 2) * 8 + (idx & 7);
  nt = (xcd & 3) * 4 + (idx >> 3);
}

template <int NU, int U0>
__device__ __forceinline__ void k1_ktile(const char* sb, f32x4 (&acc)[11][2], int xoff0, int woff) {
  bfx8 xf[2], wf, wfn;
#pragma unroll
  for (int i = 0; i < 2; ++i) xf[i] = *(const bfx8*)(sb + (xoff0 + i * 2048));
  wf = tr_frag(sb + K1_XB + U0 * 32 + woff, sb + K1_XB + U0 * 32 + 2688 + woff);
#pragma unroll
  for (int s = 0; s < 2 * NU; ++s) {
    const int u = s % NU;
    bfx8 xn[2];
    if (s == NU - 1) {
#pragma unroll
      for (int i = 0; i < 2; ++i) xn[i] = *(const bfx8*)(sb + ((xoff0 + i * 2048) ^ 64));
    }
    if (s + 1 < 2 * NU) {
      const int sn = s + 1, kn = sn / NU, un = sn % NU;
      const char* b = sb + K1_XB + kn * 4 * K1_WG + (U0 + un) * 32 + woff;
      wfn = tr_frag(b, b + 2688);
    }
    acc[u][0] = mfma16(wf, xf[0], acc[u][0]);
    acc[u][1] = mfma16(wf, xf[1], acc[u][1]);
    wf = wfn;
    if (s == NU - 1) { xf[0] = xn[0]; xf[1] = xn[1]; }
    __builtin_amdgcn_sched_barrier(0);
  }
}

template <int NU, int U0>
__device__ __forceinline__ void k1_compute(char* smem) {
  const int tid = threadIdx.x, lane = tid & 63;
  const int wave = __builtin_amdgcn_readfirstlane(tid >> 6);
  int mt, nt;
  k1_tile(blockIdx.x, mt, nt);
  const int wm = wave & 3;
  const int g = lane >> 4, c = lane & 15;
  const int xrow = wm * 32 + c;
  const int xoff0 = xrow * 128 + ((g ^ (xrow & 7)) << 4);
  const int woff = g * K1_WG + (c >> 2) * 672 + (c & 3) * 8;
  f32x4 acc[11][2];
#pragma unroll
  for (int u = 0; u < 11; ++u) { acc[u][0] = f32x4{0, 0, 0, 0}; acc[u][1] = f32x4{0, 0, 0, 0}; }
#pragma unroll 1
  for (int t = 0; t < 10; t += 2) {
    BARRIER();
    k1_ktile<NU, U0>(smem, acc, xoff0, woff);
    BARRIER();
    k1_ktile<NU, U0>(smem + K1_STAGE, acc, xoff0, woff);
  }
  BARRIER();
#pragma unroll
  for (int u = 0; u < NU; ++u) {
    const int U = nt * 21 + U0 + u;
#pragma unroll
    for (int i = 0; i < 2; ++i) {
      char* rowp = smem + (wm * 32 + i * 16 + c) * K1_CP + (U0 + u) * 32;
      f32x4 a = acc[u][i];
      if (U < 120) {
        u32x2 v = {pack2(a[0], a[1]), pack2(a[2], a[3])};
        *(u32x2*)(rowp + 8 * g) = v;
      } else {
        u32x2 s0 = __builtin_amdgcn_permlane32_swap(__float_as_uint(a[0]), __float_as_uint(a[2]), false, false);
        u32x2 s1 = __builtin_amdgcn_permlane32_swap(__float_as_uint(a[1]), __float_as_uint(a[3]), false, false);
        float g0 = rbf(__uint_as_float(s0[0])), u0 = rbf(__uint_as_float(s0[1]));
        float g1 = rbf(__uint_as_float(s1[0])), u1 = rbf(__uint_as_float(s1[1]));
        float si0 = rbf(g0 * __builtin_amdgcn_rcpf(1.0f + __builtin_amdgcn_exp2f(g0 * -1.4426950408889634f)));
        float si1 = rbf(g1 * __builtin_amdgcn_rcpf(1.0f + __builtin_amdgcn_exp2f(g1 * -1.4426950408889634f)));
        *(unsigned*)(rowp + 8 * (g & 1) + 4 * (g >> 1)) = pack2(si0 * u0, si1 * u1);
      }
    }
  }
}

struct K1Ld {
  unsigned long long gp[6];
  int pitch[6];
  int la[6];
  const u16* xb;
  int xvoff, xla;
};
struct K1Regs {
  u32x4 v[14];
  unsigned d[2];
};
__device__ __forceinline__ unsigned long long rowaddr(unsigned long long p, int pitch, int rows) {
  unsigned long long r;
  asm volatile("v_mad_u64_u32 %0, vcc, %1, %2, %3" : "=v"(r) : "v"(pitch), "s"(rows), "v"(p) : "vcc");
  return r;
}
template <int J, int T>
__device__ __forceinline__ void k1_ld(K1Ld& st, K1Regs& r) {
  if (J < 4) { GLOADS(r.v[J], st.xvoff, st.xb + J * 8 * D_MODEL + T * 64); }
  else {
    constexpr int rg = (J - 4) / 6, j = (J - 4) % 6;
    unsigned long long p = rowaddr(st.gp[j], st.pitch[j], T * 64 + rg * 8);
    if (j < 5) { GLOAD(r.v[4 + rg * 5 + j], p); }
    else { GLOAD1(r.d[rg], p); }
  }
}
template <int J, int W>
__device__ __forceinline__ void k1_wr(K1Ld& st, char* smem, K1Regs& r) {
  if (J < 4) {
    WAITV(W, r.v[J]);
    *(u32x4*)(smem + st.xla + J * 1024) = r.v[J];
  } else {
    constexpr int rg = (J - 4) / 6, j = (J - 4) % 6;
    if (j < 5) {
      WAITV(W, r.v[4 + rg * 5 + j]);
      *(u32x4*)(smem + st.la[j] + rg * K1_WG) = r.v[4 + rg * 5 + j];
    } else {
      WAITV(W, r.d[rg]);
      *(unsigned*)(smem + st.la[j] + rg * K1_WG) = r.d[rg];
    }
  }
}
template <int J, int T>
__device__ __forceinline__ void k1_slots(K1Ld& st, char* smem, K1Regs& r) {
  k1_wr<J, (T + 3 < 10) ? 31 : ((T + 2 < 10) ? 16 : 0)>(st, smem, r);
  if (T + 3 < 10) k1_ld<J, T + 3>(st, r);
  if constexpr (J < 15) k1_slots<J + 1, T>(st, smem, r);
  else {
    const int dlt = (T & 1) ? K1_STAGE : -K1_STAGE;
#pragma unroll
    for (int j = 0; j < 6; ++j) { st.la[j] += dlt; asm volatile("" : "+v"(st.la[j])); }
    st.xla += dlt; asm volatile("" : "+v"(st.xla));
  }
}
template <int J, int T>
__device__ __forceinline__ void k1_lds(K1Ld& st, K1Regs& r) {
  k1_ld<J, T>(st, r);
  if constexpr (J < 15) k1_lds<J + 1, T>(st, r);
}

__device__ __forceinline__ void k1_loader(char* smem, const u16* __restrict__ x, const u16* __restrict__ wqkv,
                                          const u16* __restrict__ wg, const u16* __restrict__ wu,
                                          const u16* __restrict__ wd, const u16* __restrict__ wo,
                                          const u16* __restrict__ gw, char* __restrict__ Wdf, char* __restrict__ Wof,
                                          float* __restrict__ G) {
  const int tid = threadIdx.x, lane = tid & 63;
  const int lw = __builtin_amdgcn_readfirstlane(tid >> 6) - 8;
  int mt, nt;
  k1_tile(blockIdx.x, mt, nt);
  K1Ld st;
  {
    const int xr = lane >> 3;
    st.xb = x + (size_t)mt * 128 * D_MODEL;
    st.xvoff = ((lw * 32 + xr) * D_MODEL + ((lane & 7) ^ xr) * 8) * 2;
    st.xla = lw * 4096 + lane * 16;
    const int nq = min(max(120 - nt * 21, 0), 21), ngu = 21 - nq;
#pragma unroll
    for (int j = 0; j < 6; ++j) {
      const int pp = j < 5 ? j * 64 + lane : 320 + (lane >> 2);
      const int dw = j < 5 ? 0 : (lane & 3) * 2;
      const int row = pp / 42, cp = pp - row * 42;
      const int k = 16 * lw + row;
      int u, half;
      if (cp < 2 * nq) {
        u = cp >> 1; half = cp & 1;
        st.gp[j] = (unsigned long long)(wqkv + (size_t)k * 1920 + nt * 336 + cp * 8 + dw);
        st.pitch[j] = 2 * 1920;
      } else {
        const int cq = cp - 2 * nq;
        half = cq >= ngu;
        u = nq + cq - half * ngu;
        st.gp[j] = (unsigned long long)((half ? wu : wg) + (size_t)k * F_DIM + (nt * 21 + u - 120) * 8 + dw);
        st.pitch[j] = 2 * F_DIM;
      }
      st.la[j] = K1_XB + 2 * lw * K1_WG + row * 672 + u * 32 + half * 16 + dw * 2;
      asm volatile("" : "+v"(st.la[j]), "+v"(st.gp[j]), "+v"(st.pitch[j]));
    }
  }
  K1Regs ra, rb;
  k1_lds<0, 0>(st, ra);
  k1_lds<0, 1>(st, rb);
  k1_slots<0, -1>(st, smem, ra);
  BARRIER(); k1_slots<0, 0>(st, smem, rb);
  BARRIER(); k1_slots<0, 1>(st, smem, ra);
  BARRIER(); k1_slots<0, 2>(st, smem, rb);
  BARRIER(); k1_slots<0, 3>(st, smem, ra);
  BARRIER(); k1_slots<0, 4>(st, smem, rb);
  BARRIER(); k1_slots<0, 5>(st, smem, ra);
  BARRIER(); k1_slots<0, 6>(st, smem, rb);
  BARRIER(); k1_slots<0, 7>(st, smem, ra);
  BARRIER(); k1_slots<0, 8>(st, smem, rb);
  const int item = blockIdx.x * 4 + lw;
  const bool has_item = item < 740;
  const int isd = item < 540;
  const int it2 = isd ? item : item - 540;
  const int ks = it2 / 10, n64 = it2 - ks * 10;
  u32x4 tw[4];
  if (has_item) {
    const u16* src = (isd ? wd : wo) + (size_t)(ks * 32 + (lane >> 3)) * D_MODEL + n64 * 64 + (lane & 7) * 8;
#pragma unroll
    for (int i = 0; i < 4; ++i) tw[i] = *(const u32x4*)(src + i * 8 * D_MODEL);
  }
  const int ch2 = 64 + (lane & 15);
  s16x8 xv[2], xv2[2];
#pragma unroll
  for (int r = 0; r < 2; ++r) {
    const u16* xr = x + (size_t)(item * 2 + r) * D_MODEL;
    xv[r] = *(const s16x8*)(xr + lane * 8);
    xv2[r] = *(const s16x8*)(xr + ch2 * 8);
  }
  s16x8 wv = *(const s16x8*)(gw + lane * 8);
  s16x8 wv2 = *(const s16x8*)(gw + ch2 * 8);
  BARRIER();
  if (has_item) {
    char* tb = smem + K1_TR + lw * 5632;
#pragma unroll
    for (int i = 0; i < 4; ++i) *(u32x4*)(tb + i * 1408 + (lane >> 3) * 160 + (lane & 7) * 16) = tw[i];
    const int g = lane >> 4, c = lane & 15;
    const char* rp = tb + g * 1408 + (c >> 2) * 160 + (c & 3) * 8;
    char* dst = (isd ? Wdf : Wof) + ((size_t)(ks * 40 + n64 * 4) * 64 + lane) * 16;
#pragma unroll
    for (int nb = 0; nb < 4; ++nb) {
      bfx8 f = tr_frag(rp + nb * 32, rp + nb * 32 + 640);
      *(bfx8*)(dst + nb * 1024) = f;
    }
  }
#pragma unroll
  for (int r = 0; r < 2; ++r) {
    float s = 0.f;
#pragma unroll
    for (int e = 0; e < 8; ++e) s += rbf(bf2f((u16)xv[r][e]) * bf2f((u16)wv[e]));
    if (lane < 16) {
#pragma unroll
      for (int e = 0; e < 8; ++e) s += rbf(bf2f((u16)xv2[r][e]) * bf2f((u16)wv2[e]));
    }
#pragma unroll
    for (int o = 32; o >= 1; o >>= 1) s += __shfl_xor(s, o);
    s = rbf(s);
    float gv = rbf(1.0f / (1.0f + __expf(-s)));
    if (lane == 0) G[item * 2 + r] = gv;
  }
  BARRIER();
}

__device__ __forceinline__ void k1_store(const char* smem, char* __restrict__ Qf, char* __restrict__ Kf,
                                         char* __restrict__ Vf, char* __restrict__ Hf) {
  int tid = threadIdx.x;
  asm volatile("" : "+v"(tid));
  const int lane = tid & 63;
  const int wave = __builtin_amdgcn_readfirstlane(tid >> 6);
  int mt, nt;
  k1_tile(blockIdx.x, mt, nt);
  const int m0 = mt * 128;
  const int g = lane >> 4, c = lane & 15;
  const int U0 = nt * 21;
  const int nqk = min(max(80 - U0, 0), 21);
  const int nv = min(max(120 - U0, 0), 21) - nqk;
  const int ngu = 21 - nqk - nv;
  if (nqk > 0) {
    for (int e = tid; e < 512 * nqk; e += 768) {
      const int p = e >> 7, tok = e & 127;
      u32x2 v = *(const u32x2*)(smem + tok * K1_CP + p * 8);
      int col = U0 * 16 + p * 4;
      const int isk = col >= 640;
      col -= isk * 640;
      const int h = col / 20, q4 = (col - h * 20) >> 2;
      const int t = m0 + tok;
      const int cc = isk ? 4 * ((t >> 3) & 3) + (t & 3) : (t & 15);
      const int blk = isk ? (t >> 5) * 2 + ((t >> 2) & 1) : (t >> 4);
      const int off = q4 < 4 ? (q4 >> 1) * 256 + cc * 16 + (q4 & 1) * 8 : 512 + cc * 8;
      *(u32x2*)((isk ? Kf : Qf) + (size_t)(h * 128 + blk) * 640 + off) = v;
    }
  }
  if (nv > 0) {
    for (int e = wave; e < nv * 4; e += 12) {
      const int u = nqk + (e >> 2), tb = (e & 3) * 32;
      const char* p0 = smem + (tb + 8 * g + (c >> 2)) * K1_CP + u * 32 + (c & 3) * 8;
      bfx8 v = tr_frag(p0, p0 + 4 * K1_CP);
      const int col = (U0 + u - 80) * 16 + c;
      const int h = col / 20, d = col - h * 20;
      const int off = d < 16 ? g * 256 + d * 16 : 1024 + g * 64 + (d - 16) * 16;
      *(bfx8*)(Vf + (size_t)(h * 64 + ((m0 + tb) >> 5)) * 1280 + off) = v;
    }
  }
  if (ngu > 0) {
    const int ub = nqk + nv;
    for (int e = tid; e < 128 * ngu; e += 768) {
      const int p = e >> 7, tok = e & 127;
      u32x4 v = *(const u32x4*)(smem + tok * K1_CP + (ub + p) * 32);
      const int Uh = U0 + ub + p - 120;
      *(u32x4*)(Hf + ((size_t)((m0 + tok) >> 4) * 216 + Uh) * 256 + (tok & 15) * 16) = v;
    }
  }
}

__global__ __launch_bounds__(768, 3) void k1_gemm(const u16* __restrict__ x, const u16* __restrict__ wqkv,
                                                  const u16* __restrict__ wg, const u16* __restrict__ wu,
                                                  const u16* __restrict__ wd, const u16* __restrict__ wo,
                                                  const u16* __restrict__ gw, char* __restrict__ Qf,
                                                  char* __restrict__ Kf, char* __restrict__ Vf,
                                                  char* __restrict__ Hf, char* __restrict__ Wdf,
                                                  char* __restrict__ Wof, float* __restrict__ G) {
  __shared__ __attribute__((aligned(16))) char smem[K1_LDS];
  const int w4 = __builtin_amdgcn_readfirstlane(threadIdx.x) >> 8;
  if (w4 == 0) k1_compute<11, 0>(smem);
  else if (w4 == 1) k1_compute<10, 11>(smem);
  else k1_loader(smem, x, wqkv, wg, wu, wd, wo, gw, Wdf, Wof, G);
  __syncthreads();
  k1_store(smem, Qf, Kf, Vf, Hf);
}

__device__ __forceinline__ void attn_item(const char* __restrict__ Qf, const char* __restrict__ Kf,
                                          const char* __restrict__ Vf, char* __restrict__ Of, int h, int qb, int lane) {
  asm volatile("" : "+v"(lane));
  const int g = lane >> 4, c = lane & 15;
  const int qoff = g < 2 ? g * 256 + c * 16 : 512 + c * 8;
  bfx8 qf[2];
#pragma unroll
  for (int t = 0; t < 2; ++t) {
    u32x4 v = *(const u32x4a8*)(Qf + (size_t)(h * 128 + qb * 2 + t) * 640 + qoff);
    if (g >= 2) { v[2] = 0; v[3] = 0; }
    if (g == 3) { v[0] = 0; v[1] = 0; }
    qf[t] = __builtin_bit_cast(bfx8, v);
  }
  f32x4 s[9][2][2];
  u32x4 kv[9][2], v0[9], v1[9];
#pragma unroll
  for (int j = 0; j < 9; ++j) {
    const int grp = max(qb - 8 + j, 0);
    const char* kp = Kf + (size_t)(h * 64 + grp) * 1280 + qoff;
    kv[j][0] = *(const u32x4a8*)(kp);
    kv[j][1] = *(const u32x4a8*)(kp + 640);
  }
#pragma unroll
  for (int j = 0; j < 9; ++j) {
    const int grp = max(qb - 8 + j, 0);
    const char* vp = Vf + (size_t)(h * 64 + grp) * 1280;
    v0[j] = *(const u32x4*)(vp + lane * 16);
    v1[j] = *(const u32x4*)(vp + 1024 + g * 64 + (c & 3) * 16);
  }
  __builtin_amdgcn_sched_barrier(0);
#pragma unroll
  for (int j = 0; j < 9; ++j) {
#pragma unroll
    for (int a = 0; a < 2; ++a) {
      bfx8 kf = __builtin_bit_cast(bfx8, kv[j][a]);
#pragma unroll
      for (int t = 0; t < 2; ++t) s[j][a][t] = mfma16(kf, qf[t], f32x4{0, 0, 0, 0});
    }
  }
  const float NEG = -1e30f;
#pragma unroll
  for (int a = 0; a < 2; ++a)
#pragma unroll
    for (int t = 0; t < 2; ++t)
#pragma unroll
      for (int r = 0; r < 4; ++r) {
        int ko = 8 * g + 4 * a + r, qo = 16 * t + c;
        if (!(ko > qo)) s[0][a][t][r] = NEG;
        if (!(ko <= qo)) s[8][a][t][r] = NEG;
      }
  if (qb < 8) {
#pragma unroll
    for (int j = 0; j < 8; ++j)
      if (j < 8 - qb) {
#pragma unroll
        for (int a = 0; a < 2; ++a)
#pragma unroll
          for (int t = 0; t < 2; ++t) s[j][a][t] = f32x4{NEG, NEG, NEG, NEG};
      }
  }
  const float cs = 0.22360679774997896f * 1.4426950408889634f;
  bfx8 pf[9][2];
#pragma unroll
  for (int t = 0; t < 2; ++t) {
    float m = NEG;
#pragma unroll
    for (int j = 0; j < 9; ++j)
#pragma unroll
      for (int a = 0; a < 2; ++a)
#pragma unroll
        for (int r = 0; r < 4; ++r) m = fmaxf(m, s[j][a][t][r]);
    m = fmaxf(m, __shfl_xor(m, 16));
    m = fmaxf(m, __shfl_xor(m, 32));
    const float mc = m * cs;
#pragma unroll
    for (int j = 0; j < 9; ++j) {
      u32x4 pk;
#pragma unroll
      for (int a = 0; a < 2; ++a) {
        float p0 = __builtin_amdgcn_exp2f(fmaf(s[j][a][t][0], cs, -mc));
        float p1 = __builtin_amdgcn_exp2f(fmaf(s[j][a][t][1], cs, -mc));
        float p2 = __builtin_amdgcn_exp2f(fmaf(s[j][a][t][2], cs, -mc));
        float p3 = __builtin_amdgcn_exp2f(fmaf(s[j][a][t][3], cs, -mc));
        pk[2 * a] = pack2(p0, p1);
        pk[2 * a + 1] = pack2(p2, p3);
      }
      pf[j][t] = __builtin_bit_cast(bfx8, pk);
    }
  }
  f32x4 o0[2] = {f32x4{0, 0, 0, 0}, f32x4{0, 0, 0, 0}}, o1[2] = {f32x4{0, 0, 0, 0}, f32x4{0, 0, 0, 0}};
#pragma unroll
  for (int j = 0; j < 9; ++j) {
    u32x4 w1 = v1[j];
    if (c == 4) w1 = u32x4{0x3f803f80u, 0x3f803f80u, 0x3f803f80u, 0x3f803f80u};
    bfx8 vf0 = __builtin_bit_cast(bfx8, v0[j]), vf1 = __builtin_bit_cast(bfx8, w1);
#pragma unroll
    for (int t = 0; t < 2; ++t) {
      o0[t] = mfma16(vf0, pf[j][t], o0[t]);
      o1[t] = mfma16(vf1, pf[j][t], o1[t]);
    }
  }
#pragma unroll
  for (int t = 0; t < 2; ++t) {
    float l = __shfl(o1[t][0], 16 + c);
    float inv = 1.0f / l;
    char* dst = Of + (size_t)(qb * 2 + t) * 20480 + c * 16;
    const int k0 = h * 20 + 4 * g;
    u32x2 v = {pack2(o0[t][0] * inv, o0[t][1] * inv), pack2(o0[t][2] * inv, o0[t][3] * inv)};
    *(u32x2*)(dst + (k0 >> 3) * 256 + ((k0 >> 2) & 1) * 8) = v;
    if (g == 0) {
      const int k1 = h * 20 + 16;
      u32x2 w = {pack2(o1[t][0] * inv, o1[t][1] * inv), pack2(o1[t][2] * inv, o1[t][3] * inv)};
      *(u32x2*)(dst + (k1 >> 3) * 256 + ((k1 >> 2) & 1) * 8) = w;
    }
  }
}

struct G2Frag { u32x4 a[4], w[5]; };
template <int NKS>
__device__ __forceinline__ void g2_issue(G2Frag& f, const char* Ab, const char* Wb, int ks, int voff) {
  const char* ap = Ab + (size_t)ks * 1024;
  const char* wp = Wb + (size_t)ks * 40960 + 2048;
#pragma unroll
  for (int i = 0; i < 4; ++i) GLOADS(f.a[i], voff, ap + (size_t)i * NKS * 1024);
  GLOADSO(f.w[0], voff, wp, -2048);
  GLOADSO(f.w[1], voff, wp, -1024);
  GLOADSO(f.w[2], voff, wp, 0);
  GLOADSO(f.w[3], voff, wp, 1024);
  GLOADSO(f.w[4], voff, wp, 2048);
}
template <int N>
__device__ __forceinline__ void g2_wait(G2Frag& f) {
  asm volatile("s_waitcnt vmcnt(%9)"
               : "+v"(f.a[0]), "+v"(f.a[1]), "+v"(f.a[2]), "+v"(f.a[3]), "+v"(f.w[0]), "+v"(f.w[1]), "+v"(f.w[2]),
                 "+v"(f.w[3]), "+v"(f.w[4])
               : "i"(N));
}
template <int NKS, int NST, int S>
__device__ __forceinline__ void g2_steps(G2Frag (&f)[4], f32x4 (&acc)[5][4], const char* Ab, const char* Wb, int wave,
                                         int voff) {
  constexpr int ahead = (NST - 1 - S) < 3 ? (NST - 1 - S) : 3;
  G2Frag& cur = f[S & 3];
  g2_wait<9 * ahead>(cur);
  if (S * 4 + 3 < NKS || S * 4 + wave < NKS) {
#pragma unroll
    for (int u = 0; u < 5; ++u)
#pragma unroll
      for (int i = 0; i < 4; ++i)
        acc[u][i] = mfma16(__builtin_bit_cast(bfx8, cur.w[u]), __builtin_bit_cast(bfx8, cur.a[i]), acc[u][i]);
  }
  __builtin_amdgcn_sched_barrier(0);
  if constexpr (S + 4 < NST) g2_issue<NKS>(cur, Ab, Wb, min((S + 4) * 4 + wave, NKS - 1), voff);
  if constexpr (S + 1 < NST) g2_steps<NKS, NST, S + 1>(f, acc, Ab, Wb, wave, voff);
}
template <int NKS>
__device__ __forceinline__ void g2_main(const char* __restrict__ Af, const char* __restrict__ Wf, int mt, int nt,
                                        int wave, int lane, f32x4 (&acc)[5][4]) {
  constexpr int NST = (NKS + 3) / 4;
  const char* Ab = Af + (size_t)mt * 4 * NKS * 1024;
  const char* Wb = Wf + (size_t)nt * 5120;
  const int voff = lane * 16;
#pragma unroll
  for (int u = 0; u < 5; ++u)
#pragma unroll
    for (int i = 0; i < 4; ++i) acc[u][i] = f32x4{0, 0, 0, 0};
  G2Frag f[4];
#pragma unroll
  for (int s = 0; s < 4; ++s) g2_issue<NKS>(f[s], Ab, Wb, min(s * 4 + wave, NKS - 1), voff);
  g2_steps<NKS, NST, 0>(f, acc, Ab, Wb, wave, voff);
}
__device__ __forceinline__ void g2_tile(int bid, int& mt, int& nt) {
  const int xcd = bid & 7, idx = bid >> 3;
  mt = (xcd >> 1) * 8 + (idx & 7);
  nt = (xcd & 1) * 4 + (idx >> 3);
}

__global__ __launch_bounds__(512, 2) void k2_mix(const char* __restrict__ Qf, const char* __restrict__ Kf,
                                                 const char* __restrict__ Vf, char* __restrict__ Of,
                                                 const char* __restrict__ Hf, const char* __restrict__ Wdf,
                                                 char* __restrict__ Bb) {
  __shared__ __attribute__((aligned(16))) char smem[81920];
  const int tid = threadIdx.x, lane = tid & 63;
  const int wave = __builtin_amdgcn_readfirstlane(tid >> 6);
  const int bid = blockIdx.x;
  if (wave >= 4) {
    const int h = (bid & 7) * 4 + (wave - 4);
    const int qb0 = (bid >> 3) * 2;
#pragma unroll 1
    for (int it = 0; it < 2; ++it) attn_item(Qf, Kf, Vf, Of, h, qb0 + it, lane);
    __builtin_amdgcn_s_barrier();
    return;
  }
  int mt, nt;
  g2_tile(bid, mt, nt);
  f32x4 acc[5][4];
  g2_main<54>(Hf, Wdf, mt, nt, wave, lane, acc);
  f32x4* red = (f32x4*)smem;
#pragma unroll
  for (int u = 0; u < 5; ++u)
#pragma unroll
    for (int i = 0; i < 4; ++i) red[((wave * 4 + i) * 5 + u) * 64 + lane] = acc[u][i];
  BARRIER();
#pragma unroll
  for (int u = 0; u < 5; ++u) {
    f32x4 v = red[((0 * 4 + wave) * 5 + u) * 64 + lane];
#pragma unroll
    for (int w = 1; w < 4; ++w) v += red[((w * 4 + wave) * 5 + u) * 64 + lane];
    u32x2 pv = {pack2(v[0], v[1]), pack2(v[2], v[3])};
    *(u32x2*)(Bb + ((size_t)((bid * 4 + wave) * 5 + u) * 64 + lane) * 8) = pv;
  }
}

__global__ __launch_bounds__(256, 2) void k3_out(const char* __restrict__ Of, const char* __restrict__ Wof,
                                                 const char* __restrict__ Bb, const float* __restrict__ G,
                                                 u16* __restrict__ out) {
  __shared__ __attribute__((aligned(16))) char smem[81920];
  const int tid = threadIdx.x, lane = tid & 63;
  const int wave = __builtin_amdgcn_readfirstlane(tid >> 6);
  const int bid = blockIdx.x;
  int mt, nt;
  g2_tile(bid, mt, nt);
  f32x4 acc[5][4];
  g2_main<20>(Of, Wof, mt, nt, wave, lane, acc);
  const int g = lane >> 4, c = lane & 15;
  const int tok = mt * 64 + wave * 16 + c;
  const float gv = G[tok];
  s16x4 bv[5];
#pragma unroll
  for (int u = 0; u < 5; ++u) bv[u] = *(const s16x4*)(Bb + ((size_t)((bid * 4 + wave) * 5 + u) * 64 + lane) * 8);
  f32x4* red = (f32x4*)smem;
#pragma unroll
  for (int u = 0; u < 5; ++u)
#pragma unroll
    for (int i = 0; i < 4; ++i) red[((wave * 4 + i) * 5 + u) * 64 + lane] = acc[u][i];
  __syncthreads();
  const float omg = rbf(1.0f - gv);
#pragma unroll
  for (int u = 0; u < 5; ++u) {
    f32x4 v = red[((0 * 4 + wave) * 5 + u) * 64 + lane];
#pragma unroll
    for (int w = 1; w < 4; ++w) v += red[((w * 4 + wave) * 5 + u) * 64 + lane];
    float r[4];
#pragma unroll
    for (int e = 0; e < 4; ++e) r[e] = rbf(gv * rbf(v[e])) + rbf(omg * bf2f((u16)bv[u][e]));
    u32x2 pv = {pack2(r[0], r[1]), pack2(r[2], r[3])};
    *(u32x2*)(out + (size_t)tok * D_MODEL + nt * 80 + u * 16 + 4 * g) = pv;
  }
}

extern "C" void kernel_launch(void* const* d_in, const int* in_sizes, int n_in,
                              void* d_out, int out_size, void* d_ws, size_t ws_size,
                              hipStream_t stream) {
  const u16* x = (const u16*)d_in[0];
  const u16* wqkv = (const u16*)d_in[1];
  const u16* wo = (const u16*)d_in[2];
  const u16* wg = (const u16*)d_in[4];
  const u16* wu = (const u16*)d_in[5];
  const u16* wd = (const u16*)d_in[6];
  const u16* gw = (const u16*)d_in[7];
  char* ws = (char*)d_ws;
  const size_t QB = (size_t)S_LEN * D_MODEL * 2;
  char* Qf = ws;
  char* Kf = Qf + QB;
  char* Vf = Kf + QB;
  char* Hf = ws + (12u << 20);
  char* Of = ws + (20u << 20);
  char* Bb = ws + (24u << 20);
  char* Wdf = ws + (28u << 20);
  char* Wof = ws + (31u << 20);
  float* G = (float*)(ws + (32u << 20));
  u16* out = (u16*)d_out;

  k1_gemm<<<256, 768, 0, stream>>>(x, wqkv, wg, wu, wd, wo, gw, Qf, Kf, Vf, Hf, Wdf, Wof, G);
  k2_mix<<<256, 512, 0, stream>>>(Qf, Kf, Vf, Of, Hf, Wdf, Bb);
  k3_out<<<256, 256, 0, stream>>>(Of, Wof, Bb, G, out);
}
```

```cpp
#include <hip/hip_runtime.h>
#include <hip/hip_bf16.h>
#include <stdint.h>

typedef __attribute__((ext_vector_type(8))) __bf16 bfx8;
typedef __attribute__((ext_vector_type(4))) short s16x4;
typedef __attribute__((ext_vector_type(8))) short s16x8;
typedef __attribute__((ext_vector_type(4))) float f32x4;
typedef __attribute__((ext_vector_type(2))) unsigned u32x2;
typedef __attribute__((ext_vector_type(4))) unsigned u32x4;
typedef __attribute__((ext_vector_type(4), aligned(8))) unsigned u32x4a8;
typedef __attribute__((address_space(3))) s16x4* lds_s16x4_ptr;
typedef unsigned short u16;

#define S_LEN 2048
#define D_MODEL 640
#define F_DIM 1728

__device__ __forceinline__ float bf2f(u16 v) { return __uint_as_float(((unsigned)v) << 16); }
__device__ __forceinline__ u16 f2bf(float f) {
  __hip_bfloat16 b = __float2bfloat16(f);
  return __builtin_bit_cast(u16, b);
}
__device__ __forceinline__ float rbf(float f) { return bf2f(f2bf(f)); }
__device__ __forceinline__ unsigned pack2(float lo, float hi) {
  return (unsigned)f2bf(lo) | ((unsigned)f2bf(hi) << 16);
}
__device__ __forceinline__ f32x4 mfma16(bfx8 a, bfx8 b, f32x4 c) {
  return __builtin_amdgcn_mfma_f32_16x16x32_bf16(a, b, c, 0, 0, 0);
}
__device__ __forceinline__ bfx8 tr_frag(const char* p0, const char* p1) {
  s16x4 a = __builtin_amdgcn_ds_read_tr16_b64_v4i16((lds_s16x4_ptr)p0);
  s16x4 b = __builtin_amdgcn_ds_read_tr16_b64_v4i16((lds_s16x4_ptr)p1);
  s16x8 v = {a[0], a[1], a[2], a[3], b[0], b[1], b[2], b[3]};
  return __builtin_bit_cast(bfx8, v);
}
#define GLOAD(r, p) asm volatile("global_load_dwordx4 %0, %1, off" : "=v"(r) : "v"(p))
#define GLOADS(r, o, p) asm volatile("global_load_dwordx4 %0, %1, %2" : "=v"(r) : "v"(o), "s"(p))
#define GLOADSO(r, o, p, imm) asm volatile("global_load_dwordx4 %0, %1, %2 offset:%3" : "=v"(r) : "v"(o), "s"(p), "i"(imm))
#define GLOAD1(r, p) asm volatile("global_load_dword %0, %1, off" : "=v"(r) : "v"(p))
#define WAITV(n, r) asm volatile("s_waitcnt vmcnt(%1)" : "+v"(r) : "i"(n))
__device__ __forceinline__ void bump(const u16*& p, int stride) {
  unsigned lo = (unsigned)(size_t)p, hi = (unsigned)((size_t)p >> 32);
  asm volatile("v_add_co_u32 %0, vcc, %0, %2\n\tv_addc_co_u32 %1, vcc, 0, %1, vcc" : "+v"(lo), "+v"(hi) : "v"(stride) : "vcc");
  p = (const u16*)(((size_t)hi << 32) | lo);
}
#define BARRIER() asm volatile("s_waitcnt lgkmcnt(0)\n\ts_barrier" ::: "memory")
#define ST(role, i)

struct Ws {
  char *Qf, *Kf, *Vf, *Hf, *Of, *Wdf, *Wof, *Bb;
  float* G;
};

#define K1_XS 16384
#define K1_WG 5504
#define K1_WS (4 * K1_WG)
#define K1_W0 (3 * K1_XS)
#define K1_CP 688
#define K1_TR 90112
#define K1_LDS (K1_W0 + 5 * K1_WS + 256)

__device__ __forceinline__ void k1_tile(int bid, int& mt, int& nt) {
  const int xcd = bid & 7, idx = bid >> 3;
  mt = (xcd >> 2) * 8 + (idx & 7);
  nt = (xcd & 3) * 4 + (idx >> 3);
}

#define CST(i)

template <int NU, bool LAST>
__device__ __forceinline__ void k1_phase(const char* smem, f32x4 (&acc)[11][2], const int (&wa)[11], int wb, int wbn,
                                         int xan, bfx8 (&xf)[2], bfx8& wf) {
  bfx8 wfn, xn[2];
#pragma unroll
  for (int u = 0; u < NU; ++u) {
    if (u + 1 < NU) {
      const char* b = smem + (wa[u + 1] + wb);
      wfn = tr_frag(b, b + 2688);
    } else if (!LAST) {
      const char* b = smem + (wa[0] + wbn);
      wfn = tr_frag(b, b + 2688);
#pragma unroll
      for (int i = 0; i < 2; ++i) xn[i] = *(const bfx8*)(smem + xan + i * 2048);
    }
    acc[u][0] = mfma16(wf, xf[0], acc[u][0]);
    acc[u][1] = mfma16(wf, xf[1], acc[u][1]);
    wf = wfn;
    __builtin_amdgcn_sched_barrier(0);
  }
  if (!LAST) { xf[0] = xn[0]; xf[1] = xn[1]; }
}

template <int NU, int U0>
__device__ __forceinline__ void k1_compute(char* smem) {
  const int tid = threadIdx.x, lane = tid & 63;
  const int wave = __builtin_amdgcn_readfirstlane(tid >> 6);
  int mt, nt;
  k1_tile(blockIdx.x, mt, nt);
  const int wm = wave & 3;
  const int g = lane >> 4, c = lane & 15;
  const int xrow = wm * 32 + c;
  const int xoff0 = xrow * 128 + ((g ^ (xrow & 7)) << 4);
  const int nq = min(max(120 - nt * 21, 0), 21), ngu = 21 - nq;
  int wa[11];
#pragma unroll
  for (int u = 0; u < NU; ++u) {
    const int uu = U0 + u;
    const int rowp = K1_W0 + g * K1_WG + (c >> 2) * 672;
    wa[u] = rowp + (uu < nq ? uu * 32 + (c & 3) * 8 : nq * 32 + (uu - nq) * 16 + ((c & 2) ? ngu * 16 : 0) + (c & 1) * 8);
  }
  f32x4 acc[11][2];
#pragma unroll
  for (int u = 0; u < 11; ++u) { acc[u][0] = f32x4{0, 0, 0, 0}; acc[u][1] = f32x4{0, 0, 0, 0}; }
  bfx8 xf[2], wf;
  CST(0);
  BARRIER();
  CST(1);
#pragma unroll
  for (int i = 0; i < 2; ++i) xf[i] = *(const bfx8*)(smem + xoff0 + i * 2048);
  wf = tr_frag(smem + wa[0], smem + wa[0] + 2688);
  int ws = 0, xs = 0;
#pragma unroll 1
  for (int t = 0; t < 10; ++t) {
    const int ws1 = ws == 4 * K1_WS ? 0 : ws + K1_WS;
    const int ws2 = ws1 == 4 * K1_WS ? 0 : ws1 + K1_WS;
    const int xs1 = xs == 2 * K1_XS ? 0 : xs + K1_XS;
    k1_phase<NU, false>(smem, acc, wa, ws, ws1, xs + (xoff0 ^ 64), xf, wf);
    BARRIER();
    k1_phase<NU, false>(smem, acc, wa, ws1, ws2, xs1 + xoff0, xf, wf);
    CST(2 * t + 2);
    BARRIER();
    CST(2 * t + 3);
    ws = ws2; xs = xs1;
  }
#pragma unroll
  for (int u = 0; u < NU; ++u) {
    const int U = nt * 21 + U0 + u;
#pragma unroll
    for (int i = 0; i < 2; ++i) {
      char* rowp = smem + (wm * 32 + i * 16 + c) * K1_CP + (U0 + u) * 32;
      f32x4 a = acc[u][i];
      if (U < 120) {
        u32x2 v = {pack2(a[0], a[1]), pack2(a[2], a[3])};
        *(u32x2*)(rowp + 8 * g) = v;
      } else {
        u32x2 s0 = __builtin_amdgcn_permlane32_swap(__float_as_uint(a[0]), __float_as_uint(a[2]), false, false);
        u32x2 s1 = __builtin_amdgcn_permlane32_swap(__float_as_uint(a[1]), __float_as_uint(a[3]), false, false);
        float g0 = rbf(__uint_as_float(s0[0])), u0 = rbf(__uint_as_float(s0[1]));
        float g1 = rbf(__uint_as_float(s1[0])), u1 = rbf(__uint_as_float(s1[1]));
        float si0 = rbf(g0 * __builtin_amdgcn_rcpf(1.0f + __builtin_amdgcn_exp2f(g0 * -1.4426950408889634f)));
        float si1 = rbf(g1 * __builtin_amdgcn_rcpf(1.0f + __builtin_amdgcn_exp2f(g1 * -1.4426950408889634f)));
        *(unsigned*)(rowp + 8 * (g & 1) + 4 * (g >> 1)) = pack2(si0 * u0, si1 * u1);
      }
    }
  }
}

struct K1Ld {
  unsigned long long gp[6];
  int pitch[6];
  const u16* xb;
  int xvoff;
  unsigned lds0;
  int lw;
};
__device__ __forceinline__ unsigned long long rowaddr(unsigned long long p, int pitch, int rows) {
  unsigned long long r;
  asm volatile("v_mad_u64_u32 %0, vcc, %1, %2, %3" : "=v"(r) : "v"(pitch), "s"(rows), "v"(p) : "vcc");
  return r;
}
#define GLDS16(p, l) asm volatile("s_mov_b32 m0, %1\n\ts_nop 0\n\tglobal_load_lds_dwordx4 %0, off" :: "v"(p), "s"(l) : "memory")
#define GLDS4(p, l) asm volatile("s_mov_b32 m0, %1\n\ts_nop 0\n\tglobal_load_lds_dword %0, off" :: "v"(p), "s"(l) : "memory")
#define GLDS16S(o, p, l) asm volatile("s_mov_b32 m0, %2\n\ts_nop 0\n\tglobal_load_lds_dwordx4 %0, %1" :: "v"(o), "s"(p), "s"(l) : "memory")
#define WAITVM(n) asm volatile("s_waitcnt vmcnt(%0)" :: "i"(n) : "memory")
template <int H>
__device__ __forceinline__ void k1_ldw(const K1Ld& st) {
  const unsigned l = st.lds0 + K1_W0 + (H % 5) * K1_WS + st.lw * K1_WG;
#pragma unroll
  for (int j = 0; j < 6; ++j) {
    unsigned long long p = rowaddr(st.gp[j], st.pitch[j], H * 32);
    if (j < 5) GLDS16(p, l + j * 1024);
    else GLDS4(p, l + 5120);
  }
}
template <int T, int J0, int N>
__device__ __forceinline__ void k1_ldx(const K1Ld& st) {
#pragma unroll
  for (int j = J0; j < J0 + N; ++j)
    GLDS16S(st.xvoff, st.xb + j * 8 * D_MODEL + T * 64, st.lds0 + (T % 3) * K1_XS + (st.lw * 4 + j) * 1024);
}
template <int P>
__device__ __forceinline__ void k1_lphase(const K1Ld& st) {
  constexpr int xt = P / 2 + 2;
  if constexpr (xt < 10) k1_ldx<xt, (P & 1) * 2, 2>(st);
  if constexpr (P + 4 < 20) k1_ldw<P + 4>(st);
  constexpr int nx0 = xt < 10 ? 2 : 0, nw0 = P + 4 < 20 ? 6 : 0;
  constexpr int nw1 = (P >= 1) ? (P + 3 < 20 ? 6 : 0) : 6;
  WAITVM(nx0 + nw0 + nw1);
  BARRIER();
  if constexpr (P + 1 < 15) k1_lphase<P + 1>(st);
}

__device__ __forceinline__ void k1_loader(char* smem, const u16* __restrict__ x, const u16* __restrict__ wqkv,
                                          const u16* __restrict__ wg, const u16* __restrict__ wu,
                                          const u16* __restrict__ wd, const u16* __restrict__ wo,
                                          const u16* __restrict__ gw, char* __restrict__ Wdf, char* __restrict__ Wof,
                                          float* __restrict__ G) {
  const int tid = threadIdx.x, lane = tid & 63;
  const int lw = __builtin_amdgcn_readfirstlane(tid >> 6) - 8;
  int mt, nt;
  k1_tile(blockIdx.x, mt, nt);
  K1Ld st;
  st.lw = lw;
  st.lds0 = (unsigned)(size_t)smem;
  {
    const int xr = lane >> 3;
    st.xb = x + (size_t)mt * 128 * D_MODEL;
    st.xvoff = ((lw * 32 + xr) * D_MODEL + ((lane & 7) ^ xr) * 8) * 2;
    const int nq = min(max(120 - nt * 21, 0), 21), ngu = 21 - nq;
#pragma unroll
    for (int j = 0; j < 6; ++j) {
      const int pp = j < 5 ? j * 64 + lane : 320 + (lane >> 2);
      const int dw = j < 5 ? 0 : (lane & 3) * 2;
      const int row = pp / 42, cp = pp - row * 42;
      const int k = 8 * lw + row;
      if (cp < 2 * nq) {
        st.gp[j] = (unsigned long long)(wqkv + (size_t)k * 1920 + nt * 336 + cp * 8 + dw);
        st.pitch[j] = 2 * 1920;
      } else {
        const int cq = cp - 2 * nq;
        const int half = cq >= ngu;
        const int u = nq + cq - half * ngu;
        st.gp[j] = (unsigned long long)((half ? wu : wg) + (size_t)k * F_DIM + (nt * 21 + u - 120) * 8 + dw);
        st.pitch[j] = 2 * F_DIM;
      }
    }
  }
  k1_ldx<0, 0, 4>(st);
  k1_ldw<0>(st);
  k1_ldw<1>(st);
  k1_ldx<1, 0, 4>(st);
  k1_ldw<2>(st);
  k1_ldw<3>(st);
  WAITVM(16);
  BARRIER();
  k1_lphase<0>(st);
  k1_ldx<9, 2, 2>(st);
  k1_ldw<19>(st);
  const int item = blockIdx.x * 4 + lw;
  const bool has_item = item < 740;
  const int isd = item < 540;
  const int it2 = isd ? item : (has_item ? item - 540 : 0);
  const int ks = it2 / 10, n64 = it2 - ks * 10;
  u32x4 tw[4];
  {
    const u16* src = (isd ? wd : wo) + (size_t)(ks * 32 + (lane >> 3)) * D_MODEL + n64 * 64 + (lane & 7) * 8;
#pragma unroll
    for (int i = 0; i < 4; ++i) GLOAD(tw[i], src + i * 8 * D_MODEL);
  }
  const int ch2 = 64 + (lane & 15);
  u32x4 xv[2], xv2[2], wv, wv2;
#pragma unroll
  for (int r = 0; r < 2; ++r) {
    const u16* xr = x + (size_t)(item * 2 + r) * D_MODEL;
    GLOAD(xv[r], xr + lane * 8);
    GLOAD(xv2[r], xr + ch2 * 8);
  }
  GLOAD(wv, gw + lane * 8);
  GLOAD(wv2, gw + ch2 * 8);
  WAITVM(10 + 8 + 6);
  BARRIER();
  WAITVM(10 + 6);
  BARRIER();
  WAITVM(10);
  BARRIER();
  BARRIER();
  asm volatile("s_waitcnt vmcnt(0)" : "+v"(tw[0]), "+v"(tw[1]), "+v"(tw[2]), "+v"(tw[3]), "+v"(xv[0]), "+v"(xv[1]),
               "+v"(xv2[0]), "+v"(xv2[1]), "+v"(wv), "+v"(wv2));
#pragma unroll
  for (int r = 0; r < 2; ++r) {
    float s = 0.f;
#pragma unroll
    for (int e = 0; e < 4; ++e) {
      s += rbf(__uint_as_float(xv[r][e] << 16) * __uint_as_float(wv[e] << 16));
      s += rbf(__uint_as_float(xv[r][e] & 0xffff0000u) * __uint_as_float(wv[e] & 0xffff0000u));
    }
    if (lane < 16) {
#pragma unroll
      for (int e = 0; e < 4; ++e) {
        s += rbf(__uint_as_float(xv2[r][e] << 16) * __uint_as_float(wv2[e] << 16));
        s += rbf(__uint_as_float(xv2[r][e] & 0xffff0000u) * __uint_as_float(wv2[e] & 0xffff0000u));
      }
    }
#pragma unroll
    for (int o = 32; o >= 1; o >>= 1) s += __shfl_xor(s, o);
    s = rbf(s);
    float gv = rbf(1.0f / (1.0f + __expf(-s)));
    if (lane == 0) G[item * 2 + r] = gv;
  }
  BARRIER();
  if (has_item) {
    char* tb = smem + K1_TR + lw * 5632;
#pragma unroll
    for (int i = 0; i < 4; ++i) *(u32x4*)(tb + i * 1408 + (lane >> 3) * 160 + (lane & 7) * 16) = tw[i];
    const int g = lane >> 4, c = lane & 15;
    const char* rp = tb + g * 1408 + (c >> 2) * 160 + (c & 3) * 8;
    char* dst = (isd ? Wdf : Wof) + ((size_t)(ks * 40 + n64 * 4) * 64 + lane) * 16;
#pragma unroll
    for (int nb = 0; nb < 4; ++nb) {
      bfx8 f = tr_frag(rp + nb * 32, rp + nb * 32 + 640);
      *(bfx8*)(dst + nb * 1024) = f;
    }
  }
}

__device__ __forceinline__ void k1_store(const char* smem, char* __restrict__ Qf, char* __restrict__ Kf,
                                         char* __restrict__ Vf, char* __restrict__ Hf) {
  int tid = threadIdx.x;
  asm volatile("" : "+v"(tid));
  const int lane = tid & 63;
  const int wave = __builtin_amdgcn_readfirstlane(tid >> 6);
  int mt, nt;
  k1_tile(blockIdx.x, mt, nt);
  const int m0 = mt * 128;
  const int g = lane >> 4, c = lane & 15;
  const int U0 = nt * 21;
  const int nqk = min(max(80 - U0, 0), 21);
  const int nv = min(max(120 - U0, 0), 21) - nqk;
  const int ngu = 21 - nqk - nv;
  if (nqk > 0) {
    for (int e = tid; e < 512 * nqk; e += 768) {
      const int p = e >> 7, tok = e & 127;
      u32x2 v = *(const u32x2*)(smem + tok * K1_CP + p * 8);
      int col = U0 * 16 + p * 4;
      const int isk = col >= 640;
      col -= isk * 640;
      const int h = col / 20, q4 = (col - h * 20) >> 2;
      const int t = m0 + tok;
      const int cc = isk ? 4 * ((t >> 3) & 3) + (t & 3) : (t & 15);
      const int blk = isk ? (t >> 5) * 2 + ((t >> 2) & 1) : (t >> 4);
      const int off = q4 < 4 ? (q4 >> 1) * 256 + cc * 16 + (q4 & 1) * 8 : 512 + cc * 8;
      *(u32x2*)((isk ? Kf : Qf) + (size_t)(h * 128 + blk) * 640 + off) = v;
    }
  }
  if (nv > 0) {
    for (int e = wave; e < nv * 4; e += 12) {
      const int u = nqk + (e >> 2), tb = (e & 3) * 32;
      const char* p0 = smem + (tb + 8 * g + (c >> 2)) * K1_CP + u * 32 + (c & 3) * 8;
      bfx8 v = tr_frag(p0, p0 + 4 * K1_CP);
      const int col = (U0 + u - 80) * 16 + c;
      const int h = col / 20, d = col - h * 20;
      const int off = d < 16 ? g * 256 + d * 16 : 1024 + g * 64 + (d - 16) * 16;
      *(bfx8*)(Vf + (size_t)(h * 64 + ((m0 + tb) >> 5)) * 1280 + off) = v;
    }
  }
  if (ngu > 0) {
    const int ub = nqk + nv;
    for (int e = tid; e < 128 * ngu; e += 768) {
      const int p = e >> 7, tok = e & 127;
      u32x4 v = *(const u32x4*)(smem + tok * K1_CP + (ub + p) * 32);
      const int Uh = U0 + ub + p - 120;
      *(u32x4*)(Hf + ((size_t)((m0 + tok) >> 4) * 216 + Uh) * 256 + (tok & 15) * 16) = v;
    }
  }
}

__global__ __launch_bounds__(768, 3) void k1_gemm(const u16* __restrict__ x, const u16* __restrict__ wqkv,
                                                  const u16* __restrict__ wg, const u16* __restrict__ wu,
                                                  const u16* __restrict__ wd, const u16* __restrict__ wo,
                                                  const u16* __restrict__ gw, char* __restrict__ Qf,
                                                  char* __restrict__ Kf, char* __restrict__ Vf,
                                                  char* __restrict__ Hf, char* __restrict__ Wdf,
                                                  char* __restrict__ Wof, float* __restrict__ G) {
  __shared__ __attribute__((aligned(16))) char smem[K1_LDS];
  const int w4 = __builtin_amdgcn_readfirstlane(threadIdx.x) >> 8;
  if (w4 == 0) k1_compute<11, 0>(smem);
  else if (w4 == 1) k1_compute<10, 11>(smem);
  else k1_loader(smem, x, wqkv, wg, wu, wd, wo, gw, Wdf, Wof, G);
  __syncthreads();
  k1_store(smem, Qf, Kf, Vf, Hf);
}

__device__ __forceinline__ void attn_item(const char* __restrict__ Qf, const char* __restrict__ Kf,
                                          const char* __restrict__ Vf, char* __restrict__ Of, int h, int qb, int lane) {
  asm volatile("" : "+v"(lane));
  const int g = lane >> 4, c = lane & 15;
  const int qoff = g < 2 ? g * 256 + c * 16 : 512 + c * 8;
  bfx8 qf[2];
#pragma unroll
  for (int t = 0; t < 2; ++t) {
    u32x4 v = *(const u32x4a8*)(Qf + (size_t)(h * 128 + qb * 2 + t) * 640 + qoff);
    if (g >= 2) { v[2] = 0; v[3] = 0; }
    if (g == 3) { v[0] = 0; v[1] = 0; }
    qf[t] = __builtin_bit_cast(bfx8, v);
  }
  f32x4 s[9][2][2];
  u32x4 kv[9][2], v0[9], v1[9];
#pragma unroll
  for (int j = 0; j < 9; ++j) {
    const int grp = max(qb - 8 + j, 0);
    const char* kp = Kf + (size_t)(h * 64 + grp) * 1280 + qoff;
    kv[j][0] = *(const u32x4a8*)(kp);
    kv[j][1] = *(const u32x4a8*)(kp + 640);
  }
#pragma unroll
  for (int j = 0; j < 9; ++j) {
    const int grp = max(qb - 8 + j, 0);
    const char* vp = Vf + (size_t)(h * 64 + grp) * 1280;
    v0[j] = *(const u32x4*)(vp + lane * 16);
    v1[j] = *(const u32x4*)(vp + 1024 + g * 64 + (c & 3) * 16);
  }
  __builtin_amdgcn_sched_barrier(0);
#pragma unroll
  for (int j = 0; j < 9; ++j) {
#pragma unroll
    for (int a = 0; a < 2; ++a) {
      bfx8 kf = __builtin_bit_cast(bfx8, kv[j][a]);
#pragma unroll
      for (int t = 0; t < 2; ++t) s[j][a][t] = mfma16(kf, qf[t], f32x4{0, 0, 0, 0});
    }
  }
  const float NEG = -1e30f;
#pragma unroll
  for (int a = 0; a < 2; ++a)
#pragma unroll
    for (int t = 0; t < 2; ++t)
#pragma unroll
      for (int r = 0; r < 4; ++r) {
        int ko = 8 * g + 4 * a + r, qo = 16 * t + c;
        if (!(ko > qo)) s[0][a][t][r] = NEG;
        if (!(ko <= qo)) s[8][a][t][r] = NEG;
      }
  if (qb < 8) {
#pragma unroll
    for (int j = 0; j < 8; ++j)
      if (j < 8 - qb) {
#pragma unroll
        for (int a = 0; a < 2; ++a)
#pragma unroll
          for (int t = 0; t < 2; ++t) s[j][a][t] = f32x4{NEG, NEG, NEG, NEG};
      }
  }
  const float cs = 0.22360679774997896f * 1.4426950408889634f;
  bfx8 pf[9][2];
#pragma unroll
  for (int t = 0; t < 2; ++t) {
    float m = NEG;
#pragma unroll
    for (int j = 0; j < 9; ++j)
#pragma unroll
      for (int a = 0; a < 2; ++a)
#pragma unroll
        for (int r = 0; r < 4; ++r) m = fmaxf(m, s[j][a][t][r]);
    m = fmaxf(m, __shfl_xor(m, 16));
    m = fmaxf(m, __shfl_xor(m, 32));
    const float mc = m * cs;
#pragma unroll
    for (int j = 0; j < 9; ++j) {
      u32x4 pk;
#pragma unroll
      for (int a = 0; a < 2; ++a) {
        float p0 = __builtin_amdgcn_exp2f(fmaf(s[j][a][t][0], cs, -mc));
        float p1 = __builtin_amdgcn_exp2f(fmaf(s[j][a][t][1], cs, -mc));
        float p2 = __builtin_amdgcn_exp2f(fmaf(s[j][a][t][2], cs, -mc));
        float p3 = __builtin_amdgcn_exp2f(fmaf(s[j][a][t][3], cs, -mc));
        pk[2 * a] = pack2(p0, p1);
        pk[2 * a + 1] = pack2(p2, p3);
      }
      pf[j][t] = __builtin_bit_cast(bfx8, pk);
    }
  }
  f32x4 o0[2] = {f32x4{0, 0, 0, 0}, f32x4{0, 0, 0, 0}}, o1[2] = {f32x4{0, 0, 0, 0}, f32x4{0, 0, 0, 0}};
#pragma unroll
  for (int j = 0; j < 9; ++j) {
    u32x4 w1 = v1[j];
    if (c == 4) w1 = u32x4{0x3f803f80u, 0x3f803f80u, 0x3f803f80u, 0x3f803f80u};
    bfx8 vf0 = __builtin_bit_cast(bfx8, v0[j]), vf1 = __builtin_bit_cast(bfx8, w1);
#pragma unroll
    for (int t = 0; t < 2; ++t) {
      o0[t] = mfma16(vf0, pf[j][t], o0[t]);
      o1[t] = mfma16(vf1, pf[j][t], o1[t]);
    }
  }
#pragma unroll
  for (int t = 0; t < 2; ++t) {
    float l = __shfl(o1[t][0], 16 + c);
    float inv = 1.0f / l;
    char* dst = Of + (size_t)(qb * 2 + t) * 20480 + c * 16;
    const int k0 = h * 20 + 4 * g;
    u32x2 v = {pack2(o0[t][0] * inv, o0[t][1] * inv), pack2(o0[t][2] * inv, o0[t][3] * inv)};
    *(u32x2*)(dst + (k0 >> 3) * 256 + ((k0 >> 2) & 1) * 8) = v;
    if (g == 0) {
      const int k1 = h * 20 + 16;
      u32x2 w = {pack2(o1[t][0] * inv, o1[t][1] * inv), pack2(o1[t][2] * inv, o1[t][3] * inv)};
      *(u32x2*)(dst + (k1 >> 3) * 256 + ((k1 >> 2) & 1) * 8) = w;
    }
  }
}

struct G2Frag { u32x4 a[4], w[5]; };
template <int NKS>
__device__ __forceinline__ void g2_issue(G2Frag& f, const char* Ab, const char* Wb, int ks, int voff) {
  const char* ap = Ab + (size_t)ks * 1024;
  const char* wp = Wb + (size_t)ks * 40960 + 2048;
#pragma unroll
  for (int i = 0; i < 4; ++i) GLOADS(f.a[i], voff, ap + (size_t)i * NKS * 1024);
  GLOADSO(f.w[0], voff, wp, -2048);
  GLOADSO(f.w[1], voff, wp, -1024);
  GLOADSO(f.w[2], voff, wp, 0);
  GLOADSO(f.w[3], voff, wp, 1024);
  GLOADSO(f.w[4], voff, wp, 2048);
}
template <int N>
__device__ __forceinline__ void g2_wait(G2Frag& f) {
  asm volatile("s_waitcnt vmcnt(%9)"
               : "+v"(f.a[0]), "+v"(f.a[1]), "+v"(f.a[2]), "+v"(f.a[3]), "+v"(f.w[0]), "+v"(f.w[1]), "+v"(f.w[2]),
                 "+v"(f.w[3]), "+v"(f.w[4])
               : "i"(N));
}
template <int NKS, int NST, int S>
__device__ __forceinline__ void g2_steps(G2Frag (&f)[4], f32x4 (&acc)[5][4], const char* Ab, const char* Wb, int wave,
                                         int voff) {
  constexpr int ahead = (NST - 1 - S) < 3 ? (NST - 1 - S) : 3;
  G2Frag& cur = f[S & 3];
  g2_wait<9 * ahead>(cur);
  if (S * 4 + 3 < NKS || S * 4 + wave < NKS) {
#pragma unroll
    for (int u = 0; u < 5; ++u)
#pragma unroll
      for (int i = 0; i < 4; ++i)
        acc[u][i] = mfma16(__builtin_bit_cast(bfx8, cur.w[u]), __builtin_bit_cast(bfx8, cur.a[i]), acc[u][i]);
  }
  __builtin_amdgcn_sched_barrier(0);
  if constexpr (S + 4 < NST) g2_issue<NKS>(cur, Ab, Wb, min((S + 4) * 4 + wave, NKS - 1), voff);
  if constexpr (S + 1 < NST) g2_steps<NKS, NST, S + 1>(f, acc, Ab, Wb, wave, voff);
}
template <int NKS>
__device__ __forceinline__ void g2_main(const char* __restrict__ Af, const char* __restrict__ Wf, int mt, int nt,
                                        int wave, int lane, f32x4 (&acc)[5][4]) {
  constexpr int NST = (NKS + 3) / 4;
  const char* Ab = Af + (size_t)mt * 4 * NKS * 1024;
  const char* Wb = Wf + (size_t)nt * 5120;
  const int voff = lane * 16;
#pragma unroll
  for (int u = 0; u < 5; ++u)
#pragma unroll
    for (int i = 0; i < 4; ++i) acc[u][i] = f32x4{0, 0, 0, 0};
  G2Frag f[4];
#pragma unroll
  for (int s = 0; s < 4; ++s) g2_issue<NKS>(f[s], Ab, Wb, min(s * 4 + wave, NKS - 1), voff);
  g2_steps<NKS, NST, 0>(f, acc, Ab, Wb, wave, voff);
}
__device__ __forceinline__ void g2_tile(int bid, int& mt, int& nt) {
  const int xcd = bid & 7, idx = bid >> 3;
  mt = (xcd >> 1) * 8 + (idx & 7);
  nt = (xcd & 1) * 4 + (idx >> 3);
}

__global__ __launch_bounds__(512, 2) void k2_mix(const char* __restrict__ Qf, const char* __restrict__ Kf,
                                                 const char* __restrict__ Vf, char* __restrict__ Of,
                                                 const char* __restrict__ Hf, const char* __restrict__ Wdf,
                                                 char* __restrict__ Bb) {
  __shared__ __attribute__((aligned(16))) char smem[81920];
  const int tid = threadIdx.x, lane = tid & 63;
  const int wave = __builtin_amdgcn_readfirstlane(tid >> 6);
  const int bid = blockIdx.x;
  if (wave >= 4) {
    const int h = (bid & 7) * 4 + (wave - 4);
    const int qb0 = (bid >> 3) * 2;
#pragma unroll 1
    for (int it = 0; it < 2; ++it) attn_item(Qf, Kf, Vf, Of, h, qb0 + it, lane);
    __builtin_amdgcn_s_barrier();
    return;
  }
  int mt, nt;
  g2_tile(bid, mt, nt);
  f32x4 acc[5][4];
  g2_main<54>(Hf, Wdf, mt, nt, wave, lane, acc);
  f32x4* red = (f32x4*)smem;
#pragma unroll
  for (int u = 0; u < 5; ++u)
#pragma unroll
    for (int i = 0; i < 4; ++i) red[((wave * 4 + i) * 5 + u) * 64 + lane] = acc[u][i];
  BARRIER();
#pragma unroll
  for (int u = 0; u < 5; ++u) {
    f32x4 v = red[((0 * 4 + wave) * 5 + u) * 64 + lane];
#pragma unroll
    for (int w = 1; w < 4; ++w) v += red[((w * 4 + wave) * 5 + u) * 64 + lane];
    u32x2 pv = {pack2(v[0], v[1]), pack2(v[2], v[3])};
    *(u32x2*)(Bb + ((size_t)((bid * 4 + wave) * 5 + u) * 64 + lane) * 8) = pv;
  }
}

__global__ __launch_bounds__(256, 2) void k3_out(const char* __restrict__ Of, const char* __restrict__ Wof,
                                                 const char* __restrict__ Bb, const float* __restrict__ G,
                                                 u16* __restrict__ out) {
  __shared__ __attribute__((aligned(16))) char smem[81920];
  const int tid = threadIdx.x, lane = tid & 63;
  const int wave = __builtin_amdgcn_readfirstlane(tid >> 6);
  const int bid = blockIdx.x;
  int mt, nt;
  g2_tile(bid, mt, nt);
  f32x4 acc[5][4];
  g2_main<20>(Of, Wof, mt, nt, wave, lane, acc);
  const int g = lane >> 4, c = lane & 15;
  const int tok = mt * 64 + wave * 16 + c;
  const float gv = G[tok];
  s16x4 bv[5];
#pragma unroll
  for (int u = 0; u < 5; ++u) bv[u] = *(const s16x4*)(Bb + ((size_t)((bid * 4 + wave) * 5 + u) * 64 + lane) * 8);
  f32x4* red = (f32x4*)smem;
#pragma unroll
  for (int u = 0; u < 5; ++u)
#pragma unroll
    for (int i = 0; i < 4; ++i) red[((wave * 4 + i) * 5 + u) * 64 + lane] = acc[u][i];
  __syncthreads();
  const float omg = rbf(1.0f - gv);
#pragma unroll
  for (int u = 0; u < 5; ++u) {
    f32x4 v = red[((0 * 4 + wave) * 5 + u) * 64 + lane];
#pragma unroll
    for (int w = 1; w < 4; ++w) v += red[((w * 4 + wave) * 5 + u) * 64 + lane];
    float r[4];
#pragma unroll
    for (int e = 0; e < 4; ++e) r[e] = rbf(gv * rbf(v[e])) + rbf(omg * bf2f((u16)bv[u][e]));
    u32x2 pv = {pack2(r[0], r[1]), pack2(r[2], r[3])};
    *(u32x2*)(out + (size_t)tok * D_MODEL + nt * 80 + u * 16 + 4 * g) = pv;
  }
}

extern "C" void kernel_launch(void* const* d_in, const int* in_sizes, int n_in,
                              void* d_out, int out_size, void* d_ws, size_t ws_size,
                              hipStream_t stream) {
  const u16* x = (const u16*)d_in[0];
  const u16* wqkv = (const u16*)d_in[1];
  const u16* wo = (const u16*)d_in[2];
  const u16* wg = (const u16*)d_in[4];
  const u16* wu = (const u16*)d_in[5];
  const u16* wd = (const u16*)d_in[6];
  const u16* gw = (const u16*)d_in[7];
  char* ws = (char*)d_ws;
  const size_t QB = (size_t)S_LEN * D_MODEL * 2;
  char* Qf = ws;
  char* Kf = Qf + QB;
  char* Vf = Kf + QB;
  char* Hf = ws + (12u << 20);
  char* Of = ws + (20u << 20);
  char* Bb = ws + (24u << 20);
  char* Wdf = ws + (28u << 20);
  char* Wof = ws + (31u << 20);
  float* G = (float*)(ws + (32u << 20));
  u16* out = (u16*)d_out;

  k1_gemm<<<256, 768, 0, stream>>>(x, wqkv, wg, wu, wd, wo, gw, Qf, Kf, Vf, Hf, Wdf, Wof, G);
  k2_mix<<<256, 512, 0, stream>>>(Qf, Kf, Vf, Of, Hf, Wdf, Bb);
  k3_out<<<256, 256, 0, stream>>>(Of, Wof, Bb, G, out);
}
```

```cpp
#include <hip/hip_runtime.h>
#include <hip/hip_bf16.h>
#include <stdint.h>

typedef __attribute__((ext_vector_type(8))) __bf16 bfx8;
typedef __attribute__((ext_vector_type(4))) short s16x4;
typedef __attribute__((ext_vector_type(8))) short s16x8;
typedef __attribute__((ext_vector_type(4))) float f32x4;
typedef __attribute__((ext_vector_type(16))) float f32x16;
typedef __attribute__((ext_vector_type(2))) unsigned u32x2;
typedef __attribute__((ext_vector_type(4))) unsigned u32x4;
typedef __attribute__((ext_vector_type(4), aligned(8))) unsigned u32x4a8;
typedef __attribute__((address_space(3))) s16x4* lds_s16x4_ptr;
typedef unsigned short u16;

#define S_LEN 2048
#define D_MODEL 640
#define F_DIM 1728

__device__ __forceinline__ float bf2f(u16 v) { return __uint_as_float(((unsigned)v) << 16); }
__device__ __forceinline__ u16 f2bf(float f) {
  __hip_bfloat16 b = __float2bfloat16(f);
  return __builtin_bit_cast(u16, b);
}
__device__ __forceinline__ float rbf(float f) { return bf2f(f2bf(f)); }
__device__ __forceinline__ unsigned pack2(float lo, float hi) {
  return (unsigned)f2bf(lo) | ((unsigned)f2bf(hi) << 16);
}
__device__ __forceinline__ f32x4 mfma16(bfx8 a, bfx8 b, f32x4 c) {
  return __builtin_amdgcn_mfma_f32_16x16x32_bf16(a, b, c, 0, 0, 0);
}
__device__ __forceinline__ f32x16 mfma32(bfx8 a, bfx8 b, f32x16 c) {
  return __builtin_amdgcn_mfma_f32_32x32x16_bf16(a, b, c, 0, 0, 0);
}
__device__ __forceinline__ bfx8 tr_frag(const char* p0, const char* p1) {
  s16x4 a = __builtin_amdgcn_ds_read_tr16_b64_v4i16((lds_s16x4_ptr)p0);
  s16x4 b = __builtin_amdgcn_ds_read_tr16_b64_v4i16((lds_s16x4_ptr)p1);
  s16x8 v = {a[0], a[1], a[2], a[3], b[0], b[1], b[2], b[3]};
  return __builtin_bit_cast(bfx8, v);
}
#define GLOAD(r, p) asm volatile("global_load_dwordx4 %0, %1, off" : "=v"(r) : "v"(p))
#define GLOADS(r, o, p) asm volatile("global_load_dwordx4 %0, %1, %2" : "=v"(r) : "v"(o), "s"(p))
#define GLOADSO(r, o, p, imm) asm volatile("global_load_dwordx4 %0, %1, %2 offset:%3" : "=v"(r) : "v"(o), "s"(p), "i"(imm))
#define GLOAD1(r, p) asm volatile("global_load_dword %0, %1, off" : "=v"(r) : "v"(p))
#define WAITV(n, r) asm volatile("s_waitcnt vmcnt(%1)" : "+v"(r) : "i"(n))
__device__ __forceinline__ void bump(const u16*& p, int stride) {
  unsigned lo = (unsigned)(size_t)p, hi = (unsigned)((size_t)p >> 32);
  asm volatile("v_add_co_u32 %0, vcc, %0, %2\n\tv_addc_co_u32 %1, vcc, 0, %1, vcc" : "+v"(lo), "+v"(hi) : "v"(stride) : "vcc");
  p = (const u16*)(((size_t)hi << 32) | lo);
}
#define BARRIER0() asm volatile("s_barrier" ::: "memory")
#define BARRIER() asm volatile("s_waitcnt lgkmcnt(0)\n\ts_barrier" ::: "memory")
#define ST(role, i)

struct Ws {
  char *Qf, *Kf, *Vf, *Hf, *Of, *Wdf, *Wof, *Bb;
  float* G;
};

#define K1_XS 16384
#define K1_WP 704
#define K1_WG (8 * K1_WP)
#define K1_WS (4 * K1_WG)
#define K1_W0 (3 * K1_XS)
#define K1_TR K1_W0
#define K1_LDS (K1_W0 + 5 * K1_WS + 256)

__device__ __forceinline__ void k1_tile(int bid, int& mt, int& nt) {
  const int xcd = bid & 7, idx = bid >> 3;
  mt = (xcd >> 2) * 8 + (idx & 7);
  nt = (xcd & 3) * 4 + (idx >> 3);
}

#define CST(i)

struct K1C {
  f32x16 acc[5];
  f32x4 accs;
  int wa[6], wah[6];
  int xa[4], xsa[2];
  bfx8 wf[3], xf[2], xs;
};
template <int F>
__device__ __forceinline__ bfx8 k1_wfrag(const char* smem, const K1C& st) {
  constexpr int P = F / 11, i = F % 11, sl = P % 5;
  constexpr int u = i < 10 ? i % 5 : 5;
  constexpr int off = (sl < 3 ? sl : sl - 3) * K1_WS + (i >= 5 && i < 10 ? 16 * K1_WP : 0);
  const char* b = smem + (sl < 3 ? st.wa[u] : st.wah[u]);
  return tr_frag(b + off, b + off + 4 * K1_WP);
}
template <bool UNSW, int F>
__device__ __forceinline__ void k1_steps(const char* smem, K1C& st, int wave, int lane) {
  constexpr int P = F / 11, i = F % 11;
  if constexpr (F + 2 < 220) st.wf[(F + 2) % 3] = k1_wfrag<F + 2>(smem, st);
  if constexpr (i == 0) st.xf[1] = *(const bfx8*)(smem + st.xa[(P & 1) * 2 + 1] + ((P >> 1) % 3) * K1_XS);
  if constexpr (i == 5 && P < 19)
    st.xf[0] = *(const bfx8*)(smem + st.xa[((P + 1) & 1) * 2] + (((P + 1) >> 1) % 3) * K1_XS);
  if constexpr (i == 6) st.xs = *(const bfx8*)(smem + st.xsa[P & 1] + ((P >> 1) % 3) * K1_XS);
  if constexpr (i < 10) {
    if constexpr (UNSW) st.acc[i % 5] = mfma32(st.xf[i / 5], st.wf[F % 3], st.acc[i % 5]);
    else st.acc[i % 5] = mfma32(st.wf[F % 3], st.xf[i / 5], st.acc[i % 5]);
  } else {
    st.accs = mfma16(st.wf[F % 3], st.xs, st.accs);
  }
  __builtin_amdgcn_sched_barrier(0);
  if constexpr (i == 10 && P < 18) {
    if constexpr (P & 1) { CST(P + 1); }
    BARRIER0();
    if constexpr (P & 1) { CST(P + 2); }
  }
  if constexpr (F + 1 < 220) k1_steps<UNSW, F + 1>(smem, st, wave, lane);
}

__device__ __forceinline__ void k1_qk_store(char* __restrict__ Qf, int col, int t, u32x2 v) {
  const int isk = col >= 640;
  col -= isk * 640;
  const int h = col / 20, q4 = (col - h * 20) >> 2;
  const int cc = isk ? 4 * ((t >> 3) & 3) + (t & 3) : (t & 15);
  const int blk = isk ? (t >> 5) * 2 + ((t >> 2) & 1) : (t >> 4);
  const int off = q4 < 4 ? (q4 >> 1) * 256 + cc * 16 + (q4 & 1) * 8 : 512 + cc * 8;
  *(u32x2*)(Qf + (unsigned)(((isk * 32 + h) * 128 + blk) * 640 + off)) = v;
}
__device__ __forceinline__ float silu_mul(float g, float u) {
  return g * u * __builtin_amdgcn_rcpf(1.0f + __builtin_amdgcn_exp2f(g * -1.4426950408889634f));
}

template <bool UNSW>
__device__ __forceinline__ void k1_compute(char* smem, char* __restrict__ Qf, char* __restrict__ Vf,
                                           char* __restrict__ Hf) {
  const int tid = threadIdx.x, lane = tid & 63;
  const int wave = __builtin_amdgcn_readfirstlane(tid >> 6);
  int mt, nt;
  k1_tile(blockIdx.x, mt, nt);
  const int wm = wave & 3, wn = wave >> 2;
  const int g = lane >> 4, c = lane & 15, hh = lane >> 5;
  const int nq = min(max(52 - nt * 10, 0), 10), ngu = 10 - nq;
  K1C st;
  {
    const int xrow = wm * 32 + (lane & 31);
    const int x0 = xrow * 128 + ((hh ^ ((xrow >> 1) & 7)) << 4);
#pragma unroll
    for (int i = 0; i < 4; ++i) st.xa[i] = x0 ^ (i * 32);
    const int xrs = wm * 32 + wn * 16 + c;
    const int xs0 = xrs * 128 + ((g ^ ((xrs >> 1) & 7)) << 4);
    st.xsa[0] = xs0;
    st.xsa[1] = xs0 ^ 64;
    const int rowp = K1_W0 + (8 * hh + (c >> 2)) * K1_WP + (c & 3) * 8;
#pragma unroll
    for (int u = 0; u < 5; ++u) {
      const int b = wn * 5 + u;
      st.wa[u] = rowp + (b < nq ? b * 64 + (g & 1) * 32 : nq * 32 + b * 32 + ((g & 1) ? ngu * 32 + 32 : 0));
    }
    st.wa[5] = K1_W0 + (8 * g + (c >> 2)) * K1_WP + (c & 3) * 8 + nq * 64 + ngu * 32;
#pragma unroll
    for (int u = 0; u < 6; ++u) {
      st.wah[u] = st.wa[u] + 3 * K1_WS;
      asm volatile("" : "+v"(st.wa[u]), "+v"(st.wah[u]));
    }
#pragma unroll
    for (int i = 0; i < 4; ++i) asm volatile("" : "+v"(st.xa[i]));
    asm volatile("" : "+v"(st.xsa[0]), "+v"(st.xsa[1]));
  }
#pragma unroll
  for (int u = 0; u < 5; ++u)
#pragma unroll
    for (int r = 0; r < 16; ++r) st.acc[u][r] = 0.f;
  st.accs = f32x4{0, 0, 0, 0};
  CST(0);
  BARRIER();
  CST(1);
  st.wf[0] = k1_wfrag<0>(smem, st);
  st.wf[1] = k1_wfrag<1>(smem, st);
  st.xf[0] = *(const bfx8*)(smem + st.xa[0]);
  k1_steps<UNSW, 0>(smem, st, wave, lane);
  CST(20);
  const int m0 = mt * 128 + wm * 32;
  {
    u32x2 v = {pack2(st.accs[0], st.accs[1]), pack2(st.accs[2], st.accs[3])};
    k1_qk_store(Qf, 1024 + 16 * nt + 4 * g, m0 + wn * 16 + c, v);
  }
  const int t = m0 + (lane & 31);
#pragma unroll
  for (int u = 0; u < 5; ++u) {
    const int b = wn * 5 + u, B = nt * 10 + b;
    const f32x16 a = st.acc[u];
    if constexpr (UNSW) {
      const int col = 32 * B + (lane & 31);
      const int h = col / 20, d = col - h * 20;
      char* dst = Vf + (unsigned)((h * 64 + (m0 >> 5)) * 1280 + (d < 16 ? d * 16 : 1024 + (d - 16) * 16) + hh * 8);
      const int js = d < 16 ? 256 : 64;
#pragma unroll
      for (int j = 0; j < 4; ++j) {
        u32x2 v = {pack2(a[4 * j], a[4 * j + 1]), pack2(a[4 * j + 2], a[4 * j + 3])};
        *(u32x2*)(dst + j * js) = v;
      }
    } else {
      if (b < nq) {
#pragma unroll
        for (int j = 0; j < 4; ++j) {
          u32x2 v = {pack2(a[4 * j], a[4 * j + 1]), pack2(a[4 * j + 2], a[4 * j + 3])};
          k1_qk_store(Qf, 32 * (B - 20) + 8 * j + 4 * hh, t, v);
        }
      } else {
        char* dst = Hf + (unsigned)(((t >> 4) * 216 + 2 * (B - 52)) * 256 + (t & 15) * 16 + hh * 8);
#pragma unroll
        for (int j = 0; j < 2; ++j) {
          u32x2 v = {pack2(silu_mul(a[4 * j], a[8 + 4 * j]), silu_mul(a[4 * j + 1], a[9 + 4 * j])),
                     pack2(silu_mul(a[4 * j + 2], a[10 + 4 * j]), silu_mul(a[4 * j + 3], a[11 + 4 * j]))};
          *(u32x2*)(dst + j * 256) = v;
        }
      }
    }
  }
  CST(21);
}

struct K1Ld {
  unsigned long long gp[6];
  int pitch[6];
  const u16* xb;
  int xvoff[2];
  unsigned lds0;
  int lw, lane;
};
__device__ __forceinline__ unsigned long long rowaddr(unsigned long long p, int pitch, int rows) {
  unsigned long long r;
  asm volatile("v_mad_u64_u32 %0, vcc, %1, %2, %3" : "=v"(r) : "v"(pitch), "s"(rows), "v"(p) : "vcc");
  return r;
}
#define GLDS16(p, l) asm volatile("s_mov_b32 m0, %1\n\ts_nop 0\n\tglobal_load_lds_dwordx4 %0, off" :: "v"(p), "s"(l) : "memory")
#define GLDS16S(o, p, l) asm volatile("s_mov_b32 m0, %2\n\ts_nop 0\n\tglobal_load_lds_dwordx4 %0, %1" :: "v"(o), "s"(p), "s"(l) : "memory")
#define WAITVM(n) asm volatile("s_waitcnt vmcnt(%0)" :: "i"(n) : "memory")
template <int H>
__device__ __forceinline__ void k1_ldw(const K1Ld& st) {
  const unsigned l = st.lds0 + K1_W0 + (H % 5) * K1_WS + st.lw * K1_WG;
#pragma unroll
  for (int j = 0; j < 6; ++j) {
    unsigned long long p = rowaddr(st.gp[j], st.pitch[j], H * 32);
    if (j < 5) GLDS16(p, l + j * 1024);
    else if (st.lane < 32) GLDS16(p, l + 5120);
  }
}
template <int T, int J0, int N>
__device__ __forceinline__ void k1_ldx(const K1Ld& st) {
#pragma unroll
  for (int j = J0; j < J0 + N; ++j)
    GLDS16S(st.xvoff[j & 1], st.xb + j * 8 * D_MODEL + T * 64, st.lds0 + (T % 3) * K1_XS + (st.lw * 4 + j) * 1024);
}
template <int P>
__device__ __forceinline__ void k1_lphase(const K1Ld& st) {
  k1_ldx<P / 2 + 2, (P & 1) * 2, 2>(st);
  k1_ldw<P + 4>(st);
  WAITVM(14);
  BARRIER();
  if constexpr (P + 1 < 15) k1_lphase<P + 1>(st);
}

__device__ __forceinline__ void k1_loader(char* smem, const u16* __restrict__ x, const u16* __restrict__ wqkv,
                                          const u16* __restrict__ wg, const u16* __restrict__ wu,
                                          const u16* __restrict__ wd, const u16* __restrict__ wo,
                                          const u16* __restrict__ gw, char* __restrict__ Wdf, char* __restrict__ Wof,
                                          float* __restrict__ G) {
  const int tid = threadIdx.x, lane = tid & 63;
  const int lw = __builtin_amdgcn_readfirstlane(tid >> 6) - 8;
  int mt, nt;
  k1_tile(blockIdx.x, mt, nt);
  K1Ld st;
  st.lw = lw;
  st.lane = lane;
  st.lds0 = (unsigned)(size_t)smem;
  {
    const int xr = lane >> 3;
    st.xb = x + (size_t)mt * 128 * D_MODEL;
    st.xvoff[0] = ((lw * 32 + xr) * D_MODEL + ((lane & 7) ^ (xr >> 1)) * 8) * 2;
    st.xvoff[1] = st.xvoff[0] ^ 64;
    const int nq = min(max(52 - nt * 10, 0), 10), ngu = 10 - nq;
    const int qcol = nt < 2 ? 1280 + 320 * nt : 320 * nt - 640;
    const int gcol = 16 * (nt * 10 + nq - 52);
#pragma unroll
    for (int j = 0; j < 6; ++j) {
      const int pp = j * 64 + lane;
      const int row = pp / 44, cp = min(pp - row * 44, 41);
      const int k = 8 * lw + (row & 7);
      const int cq = cp - 4 * nq;
      if (cq < 0 || (cq >= 2 * ngu && cq < 2 * ngu + 2)) {
        const int col = cq < 0 ? qcol + cp * 8 : 1024 + 16 * nt + 8 * (cq - 2 * ngu);
        st.gp[j] = (unsigned long long)(wqkv + (size_t)k * 1920 + col);
        st.pitch[j] = 2 * 1920;
      } else {
        const int half = cq >= 2 * ngu;
        const int col = gcol + 8 * (cq - half * (2 * ngu + 2));
        st.gp[j] = (unsigned long long)((half ? wu : wg) + (size_t)k * F_DIM + col);
        st.pitch[j] = 2 * F_DIM;
      }
    }
  }
  k1_ldx<0, 0, 4>(st);
  k1_ldw<0>(st);
  k1_ldw<1>(st);
  k1_ldx<1, 0, 4>(st);
  k1_ldw<2>(st);
  k1_ldw<3>(st);
  WAITVM(16);
  BARRIER();
  k1_lphase<0>(st);
  k1_ldx<9, 2, 2>(st);
  k1_ldw<19>(st);
  const int item = blockIdx.x * 4 + lw;
  const bool has_item = item < 740;
  const int isd = item < 540;
  const int it2 = isd ? item : (has_item ? item - 540 : 0);
  const int ks = it2 / 10, n64 = it2 - ks * 10;
  u32x4 tw[4];
  {
    const u16* src = (isd ? wd : wo) + (size_t)(ks * 32 + (lane >> 3)) * D_MODEL + n64 * 64 + (lane & 7) * 8;
#pragma unroll
    for (int i = 0; i < 4; ++i) GLOAD(tw[i], src + i * 8 * D_MODEL);
  }
  const int ch2 = 64 + (lane & 15);
  u32x4 xv[2], xv2[2], wv, wv2;
#pragma unroll
  for (int r = 0; r < 2; ++r) {
    const u16* xr = x + (size_t)(item * 2 + r) * D_MODEL;
    GLOAD(xv[r], xr + lane * 8);
    GLOAD(xv2[r], xr + ch2 * 8);
  }
  GLOAD(wv, gw + lane * 8);
  GLOAD(wv2, gw + ch2 * 8);
  WAITVM(10 + 8 + 6);
  BARRIER();
  WAITVM(10 + 6);
  BARRIER();
  WAITVM(10);
  BARRIER();
  asm volatile("s_waitcnt vmcnt(0)" : "+v"(tw[0]), "+v"(tw[1]), "+v"(tw[2]), "+v"(tw[3]), "+v"(xv[0]), "+v"(xv[1]),
               "+v"(xv2[0]), "+v"(xv2[1]), "+v"(wv), "+v"(wv2));
  if (has_item) {
    char* tb = smem + K1_TR + lw * 5632;
#pragma unroll
    for (int i = 0; i < 4; ++i) *(u32x4*)(tb + i * 1408 + (lane >> 3) * 160 + (lane & 7) * 16) = tw[i];
    const int g = lane >> 4, c = lane & 15;
    const char* rp = tb + g * 1408 + (c >> 2) * 160 + (c & 3) * 8;
    char* dst = (isd ? Wdf : Wof) + ((size_t)(ks * 40 + n64 * 4) * 64 + lane) * 16;
#pragma unroll
    for (int nb = 0; nb < 4; ++nb) {
      bfx8 f = tr_frag(rp + nb * 32, rp + nb * 32 + 640);
      *(bfx8*)(dst + nb * 1024) = f;
    }
  }
#pragma unroll
  for (int r = 0; r < 2; ++r) {
    float s = 0.f;
#pragma unroll
    for (int e = 0; e < 4; ++e) {
      s += rbf(__uint_as_float(xv[r][e] << 16) * __uint_as_float(wv[e] << 16));
      s += rbf(__uint_as_float(xv[r][e] & 0xffff0000u) * __uint_as_float(wv[e] & 0xffff0000u));
    }
    if (lane < 16) {
#pragma unroll
      for (int e = 0; e < 4; ++e) {
        s += rbf(__uint_as_float(xv2[r][e] << 16) * __uint_as_float(wv2[e] << 16));
        s += rbf(__uint_as_float(xv2[r][e] & 0xffff0000u) * __uint_as_float(wv2[e] & 0xffff0000u));
      }
    }
#pragma unroll
    for (int o = 32; o >= 1; o >>= 1) s += __shfl_xor(s, o);
    s = rbf(s);
    float gv = rbf(1.0f / (1.0f + __expf(-s)));
    if (lane == 0) G[item * 2 + r] = gv;
  }
}

__global__ __launch_bounds__(768, 3) void k1_gemm(const u16* __restrict__ x, const u16* __restrict__ wqkv,
                                                  const u16* __restrict__ wg, const u16* __restrict__ wu,
                                                  const u16* __restrict__ wd, const u16* __restrict__ wo,
                                                  const u16* __restrict__ gw, char* __restrict__ Qf,
                                                  char* __restrict__ Vf, char* __restrict__ Hf,
                                                  char* __restrict__ Wdf, char* __restrict__ Wof,
                                                  float* __restrict__ G) {
  __shared__ __attribute__((aligned(16))) char smem[K1_LDS];
  const int w8 = __builtin_amdgcn_readfirstlane(threadIdx.x) >> 9;
  int mt, nt;
  k1_tile(blockIdx.x, mt, nt);
  if (w8) k1_loader(smem, x, wqkv, wg, wu, wd, wo, gw, Wdf, Wof, G);
  else if (nt < 2) k1_compute<true>(smem, Qf, Vf, Hf);
  else k1_compute<false>(smem, Qf, Vf, Hf);
}

__device__ __forceinline__ void attn_item(const char* __restrict__ Qf, const char* __restrict__ Kf,
                                          const char* __restrict__ Vf, char* __restrict__ Of, int h, int qb, int lane) {
  asm volatile("" : "+v"(lane));
  const int g = lane >> 4, c = lane & 15;
  const int qoff = g < 2 ? g * 256 + c * 16 : 512 + c * 8;
  bfx8 qf[2];
#pragma unroll
  for (int t = 0; t < 2; ++t) {
    u32x4 v = *(const u32x4a8*)(Qf + (size_t)(h * 128 + qb * 2 + t) * 640 + qoff);
    if (g >= 2) { v[2] = 0; v[3] = 0; }
    if (g == 3) { v[0] = 0; v[1] = 0; }
    qf[t] = __builtin_bit_cast(bfx8, v);
  }
  f32x4 s[9][2][2];
  u32x4 kv[9][2], v0[9], v1[9];
#pragma unroll
  for (int j = 0; j < 9; ++j) {
    const int grp = max(qb - 8 + j, 0);
    const char* kp = Kf + (size_t)(h * 64 + grp) * 1280 + qoff;
    kv[j][0] = *(const u32x4a8*)(kp);
    kv[j][1] = *(const u32x4a8*)(kp + 640);
  }
#pragma unroll
  for (int j = 0; j < 9; ++j) {
    const int grp = max(qb - 8 + j, 0);
    const char* vp = Vf + (size_t)(h * 64 + grp) * 1280;
    v0[j] = *(const u32x4*)(vp + lane * 16);
    v1[j] = *(const u32x4*)(vp + 1024 + g * 64 + (c & 3) * 16);
  }
  __builtin_amdgcn_sched_barrier(0);
#pragma unroll
  for (int j = 0; j < 9; ++j) {
#pragma unroll
    for (int a = 0; a < 2; ++a) {
      bfx8 kf = __builtin_bit_cast(bfx8, kv[j][a]);
#pragma unroll
      for (int t = 0; t < 2; ++t) s[j][a][t] = mfma16(kf, qf[t], f32x4{0, 0, 0, 0});
    }
  }
  const float NEG = -1e30f;
#pragma unroll
  for (int a = 0; a < 2; ++a)
#pragma unroll
    for (int t = 0; t < 2; ++t)
#pragma unroll
      for (int r = 0; r < 4; ++r) {
        int ko = 8 * g + 4 * a + r, qo = 16 * t + c;
        if (!(ko > qo)) s[0][a][t][r] = NEG;
        if (!(ko <= qo)) s[8][a][t][r] = NEG;
      }
  if (qb < 8) {
#pragma unroll
    for (int j = 0; j < 8; ++j)
      if (j < 8 - qb) {
#pragma unroll
        for (int a = 0; a < 2; ++a)
#pragma unroll
          for (int t = 0; t < 2; ++t) s[j][a][t] = f32x4{NEG, NEG, NEG, NEG};
      }
  }
  const float cs = 0.22360679774997896f * 1.4426950408889634f;
  bfx8 pf[9][2];
#pragma unroll
  for (int t = 0; t < 2; ++t) {
    float m = NEG;
#pragma unroll
    for (int j = 0; j < 9; ++j)
#pragma unroll
      for (int a = 0; a < 2; ++a)
#pragma unroll
        for (int r = 0; r < 4; ++r) m = fmaxf(m, s[j][a][t][r]);
    m = fmaxf(m, __shfl_xor(m, 16));
    m = fmaxf(m, __shfl_xor(m, 32));
    const float mc = m * cs;
#pragma unroll
    for (int j = 0; j < 9; ++j) {
      u32x4 pk;
#pragma unroll
      for (int a = 0; a < 2; ++a) {
        float p0 = __builtin_amdgcn_exp2f(fmaf(s[j][a][t][0], cs, -mc));
        float p1 = __builtin_amdgcn_exp2f(fmaf(s[j][a][t][1], cs, -mc));
        float p2 = __builtin_amdgcn_exp2f(fmaf(s[j][a][t][2], cs, -mc));
        float p3 = __builtin_amdgcn_exp2f(fmaf(s[j][a][t][3], cs, -mc));
        pk[2 * a] = pack2(p0, p1);
        pk[2 * a + 1] = pack2(p2, p3);
      }
      pf[j][t] = __builtin_bit_cast(bfx8, pk);
    }
  }
  f32x4 o0[2] = {f32x4{0, 0, 0, 0}, f32x4{0, 0, 0, 0}}, o1[2] = {f32x4{0, 0, 0, 0}, f32x4{0, 0, 0, 0}};
#pragma unroll
  for (int j = 0; j < 9; ++j) {
    u32x4 w1 = v1[j];
    if (c == 4) w1 = u32x4{0x3f803f80u, 0x3f803f80u, 0x3f803f80u, 0x3f803f80u};
    bfx8 vf0 = __builtin_bit_cast(bfx8, v0[j]), vf1 = __builtin_bit_cast(bfx8, w1);
#pragma unroll
    for (int t = 0; t < 2; ++t) {
      o0[t] = mfma16(vf0, pf[j][t], o0[t]);
      o1[t] = mfma16(vf1, pf[j][t], o1[t]);
    }
  }
#pragma unroll
  for (int t = 0; t < 2; ++t) {
    float l = __shfl(o1[t][0], 16 + c);
    float inv = 1.0f / l;
    char* dst = Of + (size_t)(qb * 2 + t) * 20480 + c * 16;
    const int k0 = h * 20 + 4 * g;
    u32x2 v = {pack2(o0[t][0] * inv, o0[t][1] * inv), pack2(o0[t][2] * inv, o0[t][3] * inv)};
    *(u32x2*)(dst + (k0 >> 3) * 256 + ((k0 >> 2) & 1) * 8) = v;
    if (g == 0) {
      const int k1 = h * 20 + 16;
      u32x2 w = {pack2(o1[t][0] * inv, o1[t][1] * inv), pack2(o1[t][2] * inv, o1[t][3] * inv)};
      *(u32x2*)(dst + (k1 >> 3) * 256 + ((k1 >> 2) & 1) * 8) = w;
    }
  }
}

struct G2Frag { u32x4 a[4], w[5]; };
template <int NKS>
__device__ __forceinline__ void g2_issue(G2Frag& f, const char* Ab, const char* Wb, int ks, int voff) {
  const char* ap = Ab + (size_t)ks * 1024;
  const char* wp = Wb + (size_t)ks * 40960 + 2048;
#pragma unroll
  for (int i = 0; i < 4; ++i) GLOADS(f.a[i], voff, ap + (size_t)i * NKS * 1024);
  GLOADSO(f.w[0], voff, wp, -2048);
  GLOADSO(f.w[1], voff, wp, -1024);
  GLOADSO(f.w[2], voff, wp, 0);
  GLOADSO(f.w[3], voff, wp, 1024);
  GLOADSO(f.w[4], voff, wp, 2048);
}
template <int N>
__device__ __forceinline__ void g2_wait(G2Frag& f) {
  asm volatile("s_waitcnt vmcnt(%9)"
               : "+v"(f.a[0]), "+v"(f.a[1]), "+v"(f.a[2]), "+v"(f.a[3]), "+v"(f.w[0]), "+v"(f.w[1]), "+v"(f.w[2]),
                 "+v"(f.w[3]), "+v"(f.w[4])
               : "i"(N));
}
template <int NKS, int NST, int S>
__device__ __forceinline__ void g2_steps(G2Frag (&f)[4], f32x4 (&acc)[5][4], const char* Ab, const char* Wb, int wave,
                                         int voff) {
  constexpr int ahead = (NST - 1 - S) < 3 ? (NST - 1 - S) : 3;
  G2Frag& cur = f[S & 3];
  g2_wait<9 * ahead>(cur);
  if (S * 4 + 3 < NKS || S * 4 + wave < NKS) {
#pragma unroll
    for (int u = 0; u < 5; ++u)
#pragma unroll
      for (int i = 0; i < 4; ++i)
        acc[u][i] = mfma16(__builtin_bit_cast(bfx8, cur.w[u]), __builtin_bit_cast(bfx8, cur.a[i]), acc[u][i]);
  }
  __builtin_amdgcn_sched_barrier(0);
  if constexpr (S + 4 < NST) g2_issue<NKS>(cur, Ab, Wb, min((S + 4) * 4 + wave, NKS - 1), voff);
  if constexpr (S + 1 < NST) g2_steps<NKS, NST, S + 1>(f, acc, Ab, Wb, wave, voff);
}
template <int NKS>
__device__ __forceinline__ void g2_main(const char* __restrict__ Af, const char* __restrict__ Wf, int mt, int nt,
                                        int wave, int lane, f32x4 (&acc)[5][4]) {
  constexpr int NST = (NKS + 3) / 4;
  const char* Ab = Af + (size_t)mt * 4 * NKS * 1024;
  const char* Wb = Wf + (size_t)nt * 5120;
  const int voff = lane * 16;
#pragma unroll
  for (int u = 0; u < 5; ++u)
#pragma unroll
    for (int i = 0; i < 4; ++i) acc[u][i] = f32x4{0, 0, 0, 0};
  G2Frag f[4];
#pragma unroll
  for (int s = 0; s < 4; ++s) g2_issue<NKS>(f[s], Ab, Wb, min(s * 4 + wave, NKS - 1), voff);
  g2_steps<NKS, NST, 0>(f, acc, Ab, Wb, wave, voff);
}
__device__ __forceinline__ void g2_tile(int bid, int& mt, int& nt) {
  const int xcd = bid & 7, idx = bid >> 3;
  mt = (xcd >> 1) * 8 + (idx & 7);
  nt = (xcd & 1) * 4 + (idx >> 3);
}

__global__ __launch_bounds__(512, 2) void k2_mix(const char* __restrict__ Qf, const char* __restrict__ Kf,
                                                 const char* __restrict__ Vf, char* __restrict__ Of,
                                                 const char* __restrict__ Hf, const char* __restrict__ Wdf,
                                                 char* __restrict__ Bb) {
  __shared__ __attribute__((aligned(16))) char smem[81920];
  const int tid = threadIdx.x, lane = tid & 63;
  const int wave = __builtin_amdgcn_readfirstlane(tid >> 6);
  const int bid = blockIdx.x;
  if (wave >= 4) {
    const int h = (bid & 7) * 4 + (wave - 4);
    const int qb0 = (bid >> 3) * 2;
#pragma unroll 1
    for (int it = 0; it < 2; ++it) attn_item(Qf, Kf, Vf, Of, h, qb0 + it, lane);
    __builtin_amdgcn_s_barrier();
    return;
  }
  int mt, nt;
  g2_tile(bid, mt, nt);
  f32x4 acc[5][4];
  g2_main<54>(Hf, Wdf, mt, nt, wave, lane, acc);
  f32x4* red = (f32x4*)smem;
#pragma unroll
  for (int u = 0; u < 5; ++u)
#pragma unroll
    for (int i = 0; i < 4; ++i) red[((wave * 4 + i) * 5 + u) * 64 + lane] = acc[u][i];
  BARRIER();
#pragma unroll
  for (int u = 0; u < 5; ++u) {
    f32x4 v = red[((0 * 4 + wave) * 5 + u) * 64 + lane];
#pragma unroll
    for (int w = 1; w < 4; ++w) v += red[((w * 4 + wave) * 5 + u) * 64 + lane];
    u32x2 pv = {pack2(v[0], v[1]), pack2(v[2], v[3])};
    *(u32x2*)(Bb + ((size_t)((bid * 4 + wave) * 5 + u) * 64 + lane) * 8) = pv;
  }
}

__global__ __launch_bounds__(256, 2) void k3_out(const char* __restrict__ Of, const char* __restrict__ Wof,
                                                 const char* __restrict__ Bb, const float* __restrict__ G,
                                                 u16* __restrict__ out) {
  __shared__ __attribute__((aligned(16))) char smem[81920];
  const int tid = threadIdx.x, lane = tid & 63;
  const int wave = __builtin_amdgcn_readfirstlane(tid >> 6);
  const int bid = blockIdx.x;
  int mt, nt;
  g2_tile(bid, mt, nt);
  f32x4 acc[5][4];
  g2_main<20>(Of, Wof, mt, nt, wave, lane, acc);
  const int g = lane >> 4, c = lane & 15;
  const int tok = mt * 64 + wave * 16 + c;
  const float gv = G[tok];
  s16x4 bv[5];
#pragma unroll
  for (int u = 0; u < 5; ++u) bv[u] = *(const s16x4*)(Bb + ((size_t)((bid * 4 + wave) * 5 + u) * 64 + lane) * 8);
  f32x4* red = (f32x4*)smem;
#pragma unroll
  for (int u = 0; u < 5; ++u)
#pragma unroll
    for (int i = 0; i < 4; ++i) red[((wave * 4 + i) * 5 + u) * 64 + lane] = acc[u][i];
  __syncthreads();
  const float omg = rbf(1.0f - gv);
#pragma unroll
  for (int u = 0; u < 5; ++u) {
    f32x4 v = red[((0 * 4 + wave) * 5 + u) * 64 + lane];
#pragma unroll
    for (int w = 1; w < 4; ++w) v += red[((w * 4 + wave) * 5 + u) * 64 + lane];
    float r[4];
#pragma unroll
    for (int e = 0; e < 4; ++e) r[e] = rbf(gv * rbf(v[e])) + rbf(omg * bf2f((u16)bv[u][e]));
    u32x2 pv = {pack2(r[0], r[1]), pack2(r[2], r[3])};
    *(u32x2*)(out + (size_t)tok * D_MODEL + nt * 80 + u * 16 + 4 * g) = pv;
  }
}

extern "C" void kernel_launch(void* const* d_in, const int* in_sizes, int n_in,
                              void* d_out, int out_size, void* d_ws, size_t ws_size,
                              hipStream_t stream) {
  const u16* x = (const u16*)d_in[0];
  const u16* wqkv = (const u16*)d_in[1];
  const u16* wo = (const u16*)d_in[2];
  const u16* wg = (const u16*)d_in[4];
  const u16* wu = (const u16*)d_in[5];
  const u16* wd = (const u16*)d_in[6];
  const u16* gw = (const u16*)d_in[7];
  char* ws = (char*)d_ws;
  const size_t QB = (size_t)S_LEN * D_MODEL * 2;
  char* Qf = ws;
  char* Kf = Qf + QB;
  char* Vf = Kf + QB;
  char* Hf = ws + (12u << 20);
  char* Of = ws + (20u << 20);
  char* Bb = ws + (24u << 20);
  char* Wdf = ws + (28u << 20);
  char* Wof = ws + (31u << 20);
  float* G = (float*)(ws + (32u << 20));
  u16* out = (u16*)d_out;

  k1_gemm<<<256, 768, 0, stream>>>(x, wqkv, wg, wu, wd, wo, gw, Qf, Vf, Hf, Wdf, Wof, G);
  k2_mix<<<256, 512, 0, stream>>>(Qf, Kf, Vf, Of, Hf, Wdf, Bb);
  k3_out<<<256, 256, 0, stream>>>(Of, Wof, Bb, G, out);
}
```

```cpp
#include <hip/hip_runtime.h>
#include <hip/hip_bf16.h>
#include <stdint.h>

typedef __attribute__((ext_vector_type(8))) __bf16 bfx8;
typedef __attribute__((ext_vector_type(4))) short s16x4;
typedef __attribute__((ext_vector_type(8))) short s16x8;
typedef __attribute__((ext_vector_type(4))) float f32x4;
typedef __attribute__((ext_vector_type(16))) float f32x16;
typedef __attribute__((ext_vector_type(2))) unsigned u32x2;
typedef __attribute__((ext_vector_type(4))) unsigned u32x4;
typedef __attribute__((ext_vector_type(4), aligned(8))) unsigned u32x4a8;
typedef __attribute__((address_space(3))) s16x4* lds_s16x4_ptr;
typedef unsigned short u16;

#define S_LEN 2048
#define D_MODEL 640
#define F_DIM 1728

__device__ __forceinline__ float bf2f(u16 v) { return __uint_as_float(((unsigned)v) << 16); }
__device__ __forceinline__ u16 f2bf(float f) {
  __hip_bfloat16 b = __float2bfloat16(f);
  return __builtin_bit_cast(u16, b);
}
__device__ __forceinline__ float rbf(float f) { return bf2f(f2bf(f)); }
__device__ __forceinline__ unsigned pack2(float lo, float hi) {
  return (unsigned)f2bf(lo) | ((unsigned)f2bf(hi) << 16);
}
__device__ __forceinline__ f32x4 mfma16(bfx8 a, bfx8 b, f32x4 c) {
  return __builtin_amdgcn_mfma_f32_16x16x32_bf16(a, b, c, 0, 0, 0);
}
__device__ __forceinline__ f32x16 mfma32(bfx8 a, bfx8 b, f32x16 c) {
  return __builtin_amdgcn_mfma_f32_32x32x16_bf16(a, b, c, 0, 0, 0);
}
__device__ __forceinline__ bfx8 tr_frag(const char* p0, const char* p1) {
  s16x4 a = __builtin_amdgcn_ds_read_tr16_b64_v4i16((lds_s16x4_ptr)p0);
  s16x4 b = __builtin_amdgcn_ds_read_tr16_b64_v4i16((lds_s16x4_ptr)p1);
  s16x8 v = {a[0], a[1], a[2], a[3], b[0], b[1], b[2], b[3]};
  return __builtin_bit_cast(bfx8, v);
}
#define GLOAD(r, p) asm volatile("global_load_dwordx4 %0, %1, off" : "=v"(r) : "v"(p))
#define GLOADS(r, o, p) asm volatile("global_load_dwordx4 %0, %1, %2" : "=v"(r) : "v"(o), "s"(p))
#define GLOADSO(r, o, p, imm) asm volatile("global_load_dwordx4 %0, %1, %2 offset:%3" : "=v"(r) : "v"(o), "s"(p), "i"(imm))
#define GLOAD1(r, p) asm volatile("global_load_dword %0, %1, off" : "=v"(r) : "v"(p))
#define WAITV(n, r) asm volatile("s_waitcnt vmcnt(%1)" : "+v"(r) : "i"(n))
__device__ __forceinline__ void bump(const u16*& p, int stride) {
  unsigned lo = (unsigned)(size_t)p, hi = (unsigned)((size_t)p >> 32);
  asm volatile("v_add_co_u32 %0, vcc, %0, %2\n\tv_addc_co_u32 %1, vcc, 0, %1, vcc" : "+v"(lo), "+v"(hi) : "v"(stride) : "vcc");
  p = (const u16*)(((size_t)hi << 32) | lo);
}
#define BARRIER0() asm volatile("s_barrier" ::: "memory")
#define BARRIER() asm volatile("s_waitcnt lgkmcnt(0)\n\ts_barrier" ::: "memory")
#define ST(role, i)

struct Ws {
  char *Qf, *Kf, *Vf, *Hf, *Of, *Wdf, *Wof, *Bb;
  float* G;
};

#define K1_XS 16384
#define K1_WP 704
#define K1_WG (8 * K1_WP)
#define K1_WS (4 * K1_WG)
#define K1_W0 (3 * K1_XS)
#define K1_TR K1_W0
#define K1_LDS (K1_W0 + 5 * K1_WS + 256)

__device__ __forceinline__ void k1_tile(int bid, int& mt, int& nt) {
  const int xcd = bid & 7, idx = bid >> 3;
  mt = (xcd >> 2) * 8 + (idx & 7);
  nt = (xcd & 3) * 4 + (idx >> 3);
}

#define CST(i)

#define K1_PD 4
struct K1C {
  f32x16 acc[5];
  f32x4 accs;
  int wa[6], wah[6];
  int xa[4], xsa[2];
  bfx8 wf[K1_PD + 1], xf[2], xs;
};
template <int F>
__device__ __forceinline__ bfx8 k1_wfrag(const char* smem, const K1C& st) {
  constexpr int P = F / 11, i = F % 11, sl = P % 5;
  constexpr int u = i < 10 ? i % 5 : 5;
  constexpr int off = (sl < 3 ? sl : sl - 3) * K1_WS + (i >= 5 && i < 10 ? 16 * K1_WP : 0);
  const char* b = smem + (sl < 3 ? st.wa[u] : st.wah[u]);
  return tr_frag(b + off, b + off + 4 * K1_WP);
}
template <bool UNSW, int F>
__device__ __forceinline__ void k1_steps(const char* smem, K1C& st, int wave, int lane) {
  constexpr int P = F / 11, i = F % 11;
  if constexpr (F + K1_PD < 220) st.wf[(F + K1_PD) % (K1_PD + 1)] = k1_wfrag<F + K1_PD>(smem, st);
  if constexpr (i == 0) st.xf[1] = *(const bfx8*)(smem + st.xa[(P & 1) * 2 + 1] + ((P >> 1) % 3) * K1_XS);
  if constexpr (i == 5 && P < 19)
    st.xf[0] = *(const bfx8*)(smem + st.xa[((P + 1) & 1) * 2] + (((P + 1) >> 1) % 3) * K1_XS);
  if constexpr (i == 6) st.xs = *(const bfx8*)(smem + st.xsa[P & 1] + ((P >> 1) % 3) * K1_XS);
  if constexpr (i < 10) {
    if constexpr (UNSW) st.acc[i % 5] = mfma32(st.xf[i / 5], st.wf[F % (K1_PD + 1)], st.acc[i % 5]);
    else st.acc[i % 5] = mfma32(st.wf[F % (K1_PD + 1)], st.xf[i / 5], st.acc[i % 5]);
  } else {
    st.accs = mfma16(st.wf[F % (K1_PD + 1)], st.xs, st.accs);
  }
  __builtin_amdgcn_sched_barrier(0);
  if constexpr (i == 10 && P < 18) {
    if constexpr (P & 1) { CST(P + 1); }
    BARRIER0();
    if constexpr (P & 1) { CST(P + 2); }
  }
  if constexpr (F + 1 < 220) k1_steps<UNSW, F + 1>(smem, st, wave, lane);
}

__device__ __forceinline__ float silu_mul(float g, float u) {
  return g * u * __builtin_amdgcn_rcpf(1.0f + __builtin_amdgcn_exp2f(g * -1.4426950408889634f));
}

template <bool UNSW>
__device__ __forceinline__ void k1_compute(char* smem, const u16* __restrict__ x, const u16* __restrict__ wqkv,
                                           const u16* __restrict__ wg, const u16* __restrict__ wu,
                                           char* __restrict__ Qf, char* __restrict__ Vf, char* __restrict__ Hf) {
  const int tid = threadIdx.x, lane = tid & 63;
  const int wave = __builtin_amdgcn_readfirstlane(tid >> 6);
  int mt, nt;
  k1_tile(blockIdx.x, mt, nt);
  const int wm = wave & 3, wn = wave >> 2;
  const int g = lane >> 4, c = lane & 15, hh = lane >> 5;
  const int nq = min(max(52 - nt * 10, 0), 10), ngu = 10 - nq;
  unsigned pf[2];
  {
    const int idx = blockIdx.x >> 3;
    const int qcol = nt < 2 ? 1280 + 320 * nt : 320 * nt - 640;
    const int gcol = 16 * (nt * 10 + nq - 52);
#pragma unroll
    for (int i = 0; i < 2; ++i) {
      const int e = i * 512 + tid;
      const u16* p;
      if (e < 720) {
        const int r = e / 9, q = e - r * 9;
        const int k = r * 8 + (idx & 7);
        if (q == 8) p = wqkv + (size_t)k * 1920 + 1024 + 16 * nt;
        else if (ngu == 0) p = wqkv + (size_t)k * 1920 + qcol + min(q * 64, 312);
        else if (q == 0 && nq > 0) p = wqkv + (size_t)k * 1920 + qcol;
        else p = ((q & 4) ? wu : wg) + (size_t)k * F_DIM + gcol + min((q & 3) * 64, ngu * 16 - 8);
      } else {
        const int f = min(e - 720, 319);
        const int kt = f >> 5, r = (f & 31) * 4 + (idx >> 3);
        p = x + (size_t)(mt * 128 + r) * D_MODEL + kt * 64;
      }
      pf[i] = 0;
    }
  }
  K1C st;
  {
    const int xrow = wm * 32 + (lane & 31);
    const int x0 = xrow * 128 + ((hh ^ ((xrow >> 1) & 7)) << 4);
#pragma unroll
    for (int i = 0; i < 4; ++i) st.xa[i] = x0 ^ (i * 32);
    const int xrs = wm * 32 + wn * 16 + c;
    const int xs0 = xrs * 128 + ((g ^ ((xrs >> 1) & 7)) << 4);
    st.xsa[0] = xs0;
    st.xsa[1] = xs0 ^ 64;
    const int rowp = K1_W0 + (8 * hh + (c >> 2)) * K1_WP + (c & 3) * 8;
#pragma unroll
    for (int u = 0; u < 5; ++u) {
      const int b = wn * 5 + u;
      st.wa[u] = rowp + (b < nq ? b * 64 + (g & 1) * 32 : nq * 32 + b * 32 + ((g & 1) ? ngu * 32 + 32 : 0));
    }
    st.wa[5] = K1_W0 + (8 * g + (c >> 2)) * K1_WP + (c & 3) * 8 + nq * 64 + ngu * 32;
#pragma unroll
    for (int u = 0; u < 6; ++u) {
      st.wah[u] = st.wa[u] + 3 * K1_WS;
      asm volatile("" : "+v"(st.wa[u]), "+v"(st.wah[u]));
    }
#pragma unroll
    for (int i = 0; i < 4; ++i) asm volatile("" : "+v"(st.xa[i]));
    asm volatile("" : "+v"(st.xsa[0]), "+v"(st.xsa[1]));
  }
#pragma unroll
  for (int u = 0; u < 5; ++u)
#pragma unroll
    for (int r = 0; r < 16; ++r) st.acc[u][r] = 0.f;
  st.accs = f32x4{0, 0, 0, 0};
  CST(0);
  BARRIER();
  CST(1);
  st.wf[0] = k1_wfrag<0>(smem, st);
  st.wf[1] = k1_wfrag<1>(smem, st);
  if constexpr (K1_PD > 2) st.wf[2] = k1_wfrag<2>(smem, st);
  if constexpr (K1_PD > 3) st.wf[3] = k1_wfrag<3>(smem, st);
  if constexpr (K1_PD > 4) st.wf[4] = k1_wfrag<4>(smem, st);
  st.xf[0] = *(const bfx8*)(smem + st.xa[0]);
  k1_steps<UNSW, 0>(smem, st, wave, lane);
  CST(20);
  asm volatile("s_waitcnt vmcnt(0)" : "+v"(pf[0]), "+v"(pf[1]));
  const int m0 = mt * 128 + wm * 32;
  {
    const int ts = m0 + wn * 16 + c;
    const int cc = 4 * ((ts >> 3) & 3) + (ts & 3), blk = (ts >> 5) * 2 + ((ts >> 2) & 1);
    u32x2 v = {pack2(st.accs[0], st.accs[1]), pack2(st.accs[2], st.accs[3])};
    *(u32x2*)(Qf + (unsigned)((128 + blk) * 20480 + (48 + 2 * nt + (g >> 1)) * 256 + cc * 16 + (g & 1) * 8)) = v;
  }
  const int t = m0 + (lane & 31);
  const int tq = (t >> 4) * 20480 + (t & 15) * 16 + hh * 8;
  const int tk = (128 + (t >> 5) * 2 + ((t >> 2) & 1)) * 20480 + (4 * ((t >> 3) & 3) + (t & 3)) * 16 + hh * 8;
#pragma unroll
  for (int u = 0; u < 5; ++u) {
    const int b = wn * 5 + u, B = nt * 10 + b;
    const f32x16 a = st.acc[u];
    if constexpr (UNSW) {
      const int col = 32 * B + (lane & 31);
      const int h = col / 20, d = col - h * 20;
      char* dst = Vf + (unsigned)((h * 64 + (m0 >> 5)) * 1280 + (d < 16 ? d * 16 : 1024 + (d - 16) * 16) + hh * 8);
      const int js = d < 16 ? 256 : 64;
#pragma unroll
      for (int j = 0; j < 4; ++j) {
        u32x2 v = {pack2(a[4 * j], a[4 * j + 1]), pack2(a[4 * j + 2], a[4 * j + 3])};
        *(u32x2*)(dst + j * js) = v;
      }
    } else {
      if (b < nq) {
        char* dst = Qf + (unsigned)(B >= 40 ? tk + (B - 40) * 1024 : tq + (B - 20) * 1024);
#pragma unroll
        for (int j = 0; j < 4; ++j) {
          u32x2 v = {pack2(a[4 * j], a[4 * j + 1]), pack2(a[4 * j + 2], a[4 * j + 3])};
          *(u32x2*)(dst + j * 256) = v;
        }
      } else {
        char* dst = Hf + (unsigned)(((t >> 4) * 216 + 2 * (B - 52)) * 256 + (t & 15) * 16 + hh * 8);
#pragma unroll
        for (int j = 0; j < 2; ++j) {
          u32x2 v = {pack2(silu_mul(a[4 * j], a[8 + 4 * j]), silu_mul(a[4 * j + 1], a[9 + 4 * j])),
                     pack2(silu_mul(a[4 * j + 2], a[10 + 4 * j]), silu_mul(a[4 * j + 3], a[11 + 4 * j]))};
          *(u32x2*)(dst + j * 256) = v;
        }
      }
    }
  }
  CST(21);
}

struct K1Ld {
  unsigned long long gp[6];
  int pitch[6];
  const u16* xb;
  int xvoff[2];
  unsigned lds0;
  int lw, lane;
};
__device__ __forceinline__ unsigned long long rowaddr(unsigned long long p, int pitch, int rows) {
  unsigned long long r;
  asm volatile("v_mad_u64_u32 %0, vcc, %1, %2, %3" : "=v"(r) : "v"(pitch), "s"(rows), "v"(p) : "vcc");
  return r;
}
#define GLDS16(p, l) asm volatile("s_mov_b32 m0, %1\n\ts_nop 0\n\tglobal_load_lds_dwordx4 %0, off" :: "v"(p), "s"(l) : "memory")
#define GLDS16S(o, p, l) asm volatile("s_mov_b32 m0, %2\n\ts_nop 0\n\tglobal_load_lds_dwordx4 %0, %1" :: "v"(o), "s"(p), "s"(l) : "memory")
#define WAITVM(n) asm volatile("s_waitcnt vmcnt(%0)" :: "i"(n) : "memory")
template <int H>
__device__ __forceinline__ void k1_ldw(const K1Ld& st) {
  const unsigned l = st.lds0 + K1_W0 + (H % 5) * K1_WS + st.lw * K1_WG;
#pragma unroll
  for (int j = 0; j < 6; ++j) {
    unsigned long long p = rowaddr(st.gp[j], st.pitch[j], H * 32);
    if (j < 5) GLDS16(p, l + j * 1024);
    else if (st.lane < 32) GLDS16(p, l + 5120);
  }
}
template <int T, int J0, int N>
__device__ __forceinline__ void k1_ldx(const K1Ld& st) {
#pragma unroll
  for (int j = J0; j < J0 + N; ++j)
    GLDS16S(st.xvoff[j & 1], st.xb + j * 8 * D_MODEL + T * 64, st.lds0 + (T % 3) * K1_XS + (st.lw * 4 + j) * 1024);
}
template <int P>
__device__ __forceinline__ void k1_lphase(const K1Ld& st) {
  k1_ldx<P / 2 + 2, (P & 1) * 2, 2>(st);
  k1_ldw<P + 4>(st);
  WAITVM(14);
  BARRIER();
  if constexpr (P + 1 < 15) k1_lphase<P + 1>(st);
}

__device__ __forceinline__ void k1_loader(char* smem, const u16* __restrict__ x, const u16* __restrict__ wqkv,
                                          const u16* __restrict__ wg, const u16* __restrict__ wu,
                                          const u16* __restrict__ wd, const u16* __restrict__ wo,
                                          const u16* __restrict__ gw, char* __restrict__ Wdf, char* __restrict__ Wof,
                                          float* __restrict__ G) {
  const int tid = threadIdx.x, lane = tid & 63;
  const int lw = __builtin_amdgcn_readfirstlane(tid >> 6) - 8;
  int mt, nt;
  k1_tile(blockIdx.x, mt, nt);
  K1Ld st;
  st.lw = lw;
  st.lane = lane;
  st.lds0 = (unsigned)(size_t)smem;
  {
    const int xr = lane >> 3;
    st.xb = x + (size_t)mt * 128 * D_MODEL;
    st.xvoff[0] = ((lw * 32 + xr) * D_MODEL + ((lane & 7) ^ (xr >> 1)) * 8) * 2;
    st.xvoff[1] = st.xvoff[0] ^ 64;
    const int nq = min(max(52 - nt * 10, 0), 10), ngu = 10 - nq;
    const int qcol = nt < 2 ? 1280 + 320 * nt : 320 * nt - 640;
    const int gcol = 16 * (nt * 10 + nq - 52);
#pragma unroll
    for (int j = 0; j < 6; ++j) {
      const int pp = j * 64 + lane;
      const int row = pp / 44, cp = min(pp - row * 44, 41);
      const int k = 8 * lw + (row & 7);
      const int cq = cp - 4 * nq;
      if (cq < 0 || (cq >= 2 * ngu && cq < 2 * ngu + 2)) {
        const int col = cq < 0 ? qcol + cp * 8 : 1024 + 16 * nt + 8 * (cq - 2 * ngu);
        st.gp[j] = (unsigned long long)(wqkv + (size_t)k * 1920 + col);
        st.pitch[j] = 2 * 1920;
      } else {
        const int half = cq >= 2 * ngu;
        const int col = gcol + 8 * (cq - half * (2 * ngu + 2));
        st.gp[j] = (unsigned long long)((half ? wu : wg) + (size_t)k * F_DIM + col);
        st.pitch[j] = 2 * F_DIM;
      }
    }
  }
  k1_ldx<0, 0, 4>(st);
  k1_ldw<0>(st);
  k1_ldw<1>(st);
  k1_ldx<1, 0, 4>(st);
  k1_ldw<2>(st);
  k1_ldw<3>(st);
  WAITVM(16);
  BARRIER();
  k1_lphase<0>(st);
  k1_ldx<9, 2, 2>(st);
  k1_ldw<19>(st);
  const int item = blockIdx.x * 4 + lw;
  const bool has_item = item < 740;
  const int isd = item < 540;
  const int it2 = isd ? item : (has_item ? item - 540 : 0);
  const int ks = it2 / 10, n64 = it2 - ks * 10;
  u32x4 tw[4];
  {
    const u16* src = (isd ? wd : wo) + (size_t)(ks * 32 + (lane >> 3)) * D_MODEL + n64 * 64 + (lane & 7) * 8;
#pragma unroll
    for (int i = 0; i < 4; ++i) GLOAD(tw[i], src + i * 8 * D_MODEL);
  }
  const int ch2 = 64 + (lane & 15);
  u32x4 xv[2], xv2[2], wv, wv2;
#pragma unroll
  for (int r = 0; r < 2; ++r) {
    const u16* xr = x + (size_t)(item * 2 + r) * D_MODEL;
    GLOAD(xv[r], xr + lane * 8);
    GLOAD(xv2[r], xr + ch2 * 8);
  }
  GLOAD(wv, gw + lane * 8);
  GLOAD(wv2, gw + ch2 * 8);
  WAITVM(10 + 8 + 6);
  BARRIER();
  WAITVM(10 + 6);
  BARRIER();
  WAITVM(10);
  BARRIER();
  asm volatile("s_waitcnt vmcnt(0)" : "+v"(tw[0]), "+v"(tw[1]), "+v"(tw[2]), "+v"(tw[3]), "+v"(xv[0]), "+v"(xv[1]),
               "+v"(xv2[0]), "+v"(xv2[1]), "+v"(wv), "+v"(wv2));
  if (has_item) {
    char* tb = smem + K1_TR + lw * 5632;
#pragma unroll
    for (int i = 0; i < 4; ++i) *(u32x4*)(tb + i * 1408 + (lane >> 3) * 160 + (lane & 7) * 16) = tw[i];
    const int g = lane >> 4, c = lane & 15;
    const char* rp = tb + g * 1408 + (c >> 2) * 160 + (c & 3) * 8;
    char* dst = (isd ? Wdf : Wof) + ((size_t)(ks * 40 + n64 * 4) * 64 + lane) * 16;
#pragma unroll
    for (int nb = 0; nb < 4; ++nb) {
      bfx8 f = tr_frag(rp + nb * 32, rp + nb * 32 + 640);
      *(bfx8*)(dst + nb * 1024) = f;
    }
  }
#pragma unroll
  for (int r = 0; r < 2; ++r) {
    float s = 0.f;
#pragma unroll
    for (int e = 0; e < 4; ++e) {
      s += rbf(__uint_as_float(xv[r][e] << 16) * __uint_as_float(wv[e] << 16));
      s += rbf(__uint_as_float(xv[r][e] & 0xffff0000u) * __uint_as_float(wv[e] & 0xffff0000u));
    }
    if (lane < 16) {
#pragma unroll
      for (int e = 0; e < 4; ++e) {
        s += rbf(__uint_as_float(xv2[r][e] << 16) * __uint_as_float(wv2[e] << 16));
        s += rbf(__uint_as_float(xv2[r][e] & 0xffff0000u) * __uint_as_float(wv2[e] & 0xffff0000u));
      }
    }
#pragma unroll
    for (int o = 32; o >= 1; o >>= 1) s += __shfl_xor(s, o);
    s = rbf(s);
    float gv = rbf(1.0f / (1.0f + __expf(-s)));
    if (lane == 0) G[item * 2 + r] = gv;
  }
}

__global__ __launch_bounds__(768, 3) void k1_gemm(const u16* __restrict__ x, const u16* __restrict__ wqkv,
                                                  const u16* __restrict__ wg, const u16* __restrict__ wu,
                                                  const u16* __restrict__ wd, const u16* __restrict__ wo,
                                                  const u16* __restrict__ gw, char* __restrict__ Qf,
                                                  char* __restrict__ Vf, char* __restrict__ Hf,
                                                  char* __restrict__ Wdf, char* __restrict__ Wof,
                                                  float* __restrict__ G) {
  __shared__ __attribute__((aligned(16))) char smem[K1_LDS];
  const int w8 = __builtin_amdgcn_readfirstlane(threadIdx.x) >> 9;
  int mt, nt;
  k1_tile(blockIdx.x, mt, nt);
  if (w8) k1_loader(smem, x, wqkv, wg, wu, wd, wo, gw, Wdf, Wof, G);
  else if (nt < 2) k1_compute<true>(smem, x, wqkv, wg, wu, Qf, Vf, Hf);
  else k1_compute<false>(smem, x, wqkv, wg, wu, Qf, Vf, Hf);
}

#define AT_K 15360
#define AT_LDS (AT_K + 12800)
__device__ __forceinline__ void attn_stage(const char* __restrict__ Kf, const char* __restrict__ Vf, unsigned lds,
                                           int h, int qb0, int lane) {
  const int slot0 = (20 * h) >> 3;
#pragma unroll
  for (int i = 0; i < 15; ++i) {
    const int p = i * 64 + lane;
    const int blk = p / 48, w = p - blk * 48;
    const int gb = max(qb0 - 8 + (blk >> 1), 0) * 2 + (blk & 1);
    GLDS16S(gb * 20480 + w * 16, Kf + slot0 * 256, lds + i * 1024);
  }
#pragma unroll
  for (int i = 0; i < 13; ++i) {
    const int p = i * 64 + lane;
    const int grp = p / 80, w = p - grp * 80;
    const int gg = max(qb0 - 8 + grp, 0);
    if (i < 12 || lane < 32) GLDS16S(gg * 1280 + w * 16, Vf + (size_t)h * 81920, lds + AT_K + i * 1024);
  }
}
__device__ __forceinline__ void attn_item(const u32x4 (&qv)[2], const char* kl, const char* vl, char* __restrict__ Of,
                                          int h, int qb, int lane) {
  asm volatile("" : "+v"(lane));
  const int g = lane >> 4, c = lane & 15;
  bfx8 qf[2];
#pragma unroll
  for (int t = 0; t < 2; ++t) {
    u32x4 v = qv[t];
    if (g == 3 || (g == 2 && !(h & 1))) { v[2] = 0; v[3] = 0; }
    if (g == 3 || (g == 0 && (h & 1))) { v[0] = 0; v[1] = 0; }
    qf[t] = __builtin_bit_cast(bfx8, v);
  }
  f32x4 s[9][2][2];
  const char* kp = kl + min(g, 2) * 256 + c * 16;
#pragma unroll
  for (int j = 0; j < 9; ++j) {
#pragma unroll
    for (int a = 0; a < 2; ++a) {
      bfx8 kf = *(const bfx8*)(kp + (j * 2 + a) * 768);
#pragma unroll
      for (int t = 0; t < 2; ++t) s[j][a][t] = mfma16(kf, qf[t], f32x4{0, 0, 0, 0});
    }
  }
  const float NEG = -1e30f;
#pragma unroll
  for (int a = 0; a < 2; ++a)
#pragma unroll
    for (int t = 0; t < 2; ++t)
#pragma unroll
      for (int r = 0; r < 4; ++r) {
        int ko = 8 * g + 4 * a + r, qo = 16 * t + c;
        if (!(ko > qo)) s[0][a][t][r] = NEG;
        if (!(ko <= qo)) s[8][a][t][r] = NEG;
      }
  if (qb < 8) {
#pragma unroll
    for (int j = 0; j < 8; ++j)
      if (j < 8 - qb) {
#pragma unroll
        for (int a = 0; a < 2; ++a)
#pragma unroll
          for (int t = 0; t < 2; ++t) s[j][a][t] = f32x4{NEG, NEG, NEG, NEG};
      }
  }
  const float cs = 0.22360679774997896f * 1.4426950408889634f;
  bfx8 pf[9][2];
#pragma unroll
  for (int t = 0; t < 2; ++t) {
    float m = NEG;
#pragma unroll
    for (int j = 0; j < 9; ++j)
#pragma unroll
      for (int a = 0; a < 2; ++a)
#pragma unroll
        for (int r = 0; r < 4; ++r) m = fmaxf(m, s[j][a][t][r]);
    m = fmaxf(m, __shfl_xor(m, 16));
    m = fmaxf(m, __shfl_xor(m, 32));
    const float mc = m * cs;
#pragma unroll
    for (int j = 0; j < 9; ++j) {
      u32x4 pk;
#pragma unroll
      for (int a = 0; a < 2; ++a) {
        float p0 = __builtin_amdgcn_exp2f(fmaf(s[j][a][t][0], cs, -mc));
        float p1 = __builtin_amdgcn_exp2f(fmaf(s[j][a][t][1], cs, -mc));
        float p2 = __builtin_amdgcn_exp2f(fmaf(s[j][a][t][2], cs, -mc));
        float p3 = __builtin_amdgcn_exp2f(fmaf(s[j][a][t][3], cs, -mc));
        pk[2 * a] = pack2(p0, p1);
        pk[2 * a + 1] = pack2(p2, p3);
      }
      pf[j][t] = __builtin_bit_cast(bfx8, pk);
    }
  }
  f32x4 o0[2] = {f32x4{0, 0, 0, 0}, f32x4{0, 0, 0, 0}}, o1[2] = {f32x4{0, 0, 0, 0}, f32x4{0, 0, 0, 0}};
#pragma unroll
  for (int j = 0; j < 9; ++j) {
    u32x4 w1 = *(const u32x4*)(vl + j * 1280 + 1024 + g * 64 + (c & 3) * 16);
    if (c == 4) w1 = u32x4{0x3f803f80u, 0x3f803f80u, 0x3f803f80u, 0x3f803f80u};
    bfx8 vf0 = *(const bfx8*)(vl + j * 1280 + lane * 16), vf1 = __builtin_bit_cast(bfx8, w1);
#pragma unroll
    for (int t = 0; t < 2; ++t) {
      o0[t] = mfma16(vf0, pf[j][t], o0[t]);
      o1[t] = mfma16(vf1, pf[j][t], o1[t]);
    }
  }
#pragma unroll
  for (int t = 0; t < 2; ++t) {
    float l = __shfl(o1[t][0], 16 + c);
    float inv = 1.0f / l;
    char* dst = Of + (size_t)(qb * 2 + t) * 20480 + c * 16;
    const int k0 = h * 20 + 4 * g;
    u32x2 v = {pack2(o0[t][0] * inv, o0[t][1] * inv), pack2(o0[t][2] * inv, o0[t][3] * inv)};
    *(u32x2*)(dst + (k0 >> 3) * 256 + ((k0 >> 2) & 1) * 8) = v;
    if (g == 0) {
      const int k1 = h * 20 + 16;
      u32x2 w = {pack2(o1[t][0] * inv, o1[t][1] * inv), pack2(o1[t][2] * inv, o1[t][3] * inv)};
      *(u32x2*)(dst + (k1 >> 3) * 256 + ((k1 >> 2) & 1) * 8) = w;
    }
  }
}

struct G2Frag { u32x4 a[4], w[5]; };
template <int NKS>
__device__ __forceinline__ void g2_issue(G2Frag& f, const char* Ab, const char* Wb, int ks, int voff) {
  const char* ap = Ab + (size_t)ks * 1024;
  const char* wp = Wb + (size_t)ks * 40960 + 2048;
#pragma unroll
  for (int i = 0; i < 4; ++i) GLOADS(f.a[i], voff, ap + (size_t)i * NKS * 1024);
  GLOADSO(f.w[0], voff, wp, -2048);
  GLOADSO(f.w[1], voff, wp, -1024);
  GLOADSO(f.w[2], voff, wp, 0);
  GLOADSO(f.w[3], voff, wp, 1024);
  GLOADSO(f.w[4], voff, wp, 2048);
}
template <int N>
__device__ __forceinline__ void g2_wait(G2Frag& f) {
  asm volatile("s_waitcnt vmcnt(%9)"
               : "+v"(f.a[0]), "+v"(f.a[1]), "+v"(f.a[2]), "+v"(f.a[3]), "+v"(f.w[0]), "+v"(f.w[1]), "+v"(f.w[2]),
                 "+v"(f.w[3]), "+v"(f.w[4])
               : "i"(N));
}
template <int NKS, int NST, int S>
__device__ __forceinline__ void g2_steps(G2Frag (&f)[4], f32x4 (&acc)[5][4], const char* Ab, const char* Wb, int wave,
                                         int voff) {
  constexpr int ahead = (NST - 1 - S) < 3 ? (NST - 1 - S) : 3;
  G2Frag& cur = f[S & 3];
  g2_wait<9 * ahead>(cur);
  if (S * 4 + 3 < NKS || S * 4 + wave < NKS) {
#pragma unroll
    for (int u = 0; u < 5; ++u)
#pragma unroll
      for (int i = 0; i < 4; ++i)
        acc[u][i] = mfma16(__builtin_bit_cast(bfx8, cur.w[u]), __builtin_bit_cast(bfx8, cur.a[i]), acc[u][i]);
  }
  __builtin_amdgcn_sched_barrier(0);
  if constexpr (S + 4 < NST) g2_issue<NKS>(cur, Ab, Wb, min((S + 4) * 4 + wave, NKS - 1), voff);
  if constexpr (S + 1 < NST) g2_steps<NKS, NST, S + 1>(f, acc, Ab, Wb, wave, voff);
}
template <int NKS>
__device__ __forceinline__ void g2_main(const char* __restrict__ Af, const char* __restrict__ Wf, int mt, int nt,
                                        int wave, int lane, f32x4 (&acc)[5][4]) {
  constexpr int NST = (NKS + 3) / 4;
  const char* Ab = Af + (size_t)mt * 4 * NKS * 1024;
  const char* Wb = Wf + (size_t)nt * 5120;
  const int voff = lane * 16;
#pragma unroll
  for (int u = 0; u < 5; ++u)
#pragma unroll
    for (int i = 0; i < 4; ++i) acc[u][i] = f32x4{0, 0, 0, 0};
  G2Frag f[4];
#pragma unroll
  for (int s = 0; s < 4; ++s) g2_issue<NKS>(f[s], Ab, Wb, min(s * 4 + wave, NKS - 1), voff);
  g2_steps<NKS, NST, 0>(f, acc, Ab, Wb, wave, voff);
}
__device__ __forceinline__ void g2_tile(int bid, int& mt, int& nt) {
  const int xcd = bid & 7, idx = bid >> 3;
  mt = (xcd >> 1) * 8 + (idx & 7);
  nt = (xcd & 1) * 4 + (idx >> 3);
}

__global__ __launch_bounds__(512, 2) void k2_mix(const char* __restrict__ Qf, const char* __restrict__ Kf,
                                                 const char* __restrict__ Vf, char* __restrict__ Of,
                                                 const char* __restrict__ Hf, const char* __restrict__ Wdf,
                                                 char* __restrict__ Bb) {
  __shared__ __attribute__((aligned(16))) char smem[4 * AT_LDS];
  const int tid = threadIdx.x, lane = tid & 63;
  const int wave = __builtin_amdgcn_readfirstlane(tid >> 6);
  const int bid = blockIdx.x;
  if (wave >= 4) {
    const int h = (bid & 7) * 4 + (wave - 4);
    const int qb0 = (bid >> 3) * 2;
    char* al = smem + (wave - 4) * AT_LDS;
    attn_stage(Kf, Vf, (unsigned)(size_t)al, h, qb0, lane);
    u32x4 qv[2][2];
    {
      const char* qp = Qf + (size_t)(qb0 * 2) * 20480 + (((20 * h) >> 3) + min(lane >> 4, 2)) * 256 + (lane & 15) * 16;
#pragma unroll
      for (int i = 0; i < 4; ++i) GLOAD(qv[i >> 1][i & 1], qp + i * 20480);
    }
    asm volatile("s_waitcnt vmcnt(0)" : "+v"(qv[0][0]), "+v"(qv[0][1]), "+v"(qv[1][0]), "+v"(qv[1][1]) :: "memory");
    attn_item(qv[0], al, al + AT_K, Of, h, qb0, lane);
    attn_item(qv[1], al + 1536, al + AT_K + 1280, Of, h, qb0 + 1, lane);
    BARRIER();
    return;
  }
  int mt, nt;
  g2_tile(bid, mt, nt);
  f32x4 acc[5][4];
  g2_main<54>(Hf, Wdf, mt, nt, wave, lane, acc);
  f32x4* red = (f32x4*)smem;
  BARRIER();
#pragma unroll
  for (int u = 0; u < 5; ++u)
#pragma unroll
    for (int i = 0; i < 4; ++i) red[((wave * 4 + i) * 5 + u) * 64 + lane] = acc[u][i];
  BARRIER();
#pragma unroll
  for (int u = 0; u < 5; ++u) {
    f32x4 v = red[((0 * 4 + wave) * 5 + u) * 64 + lane];
#pragma unroll
    for (int w = 1; w < 4; ++w) v += red[((w * 4 + wave) * 5 + u) * 64 + lane];
    u32x2 pv = {pack2(v[0], v[1]), pack2(v[2], v[3])};
    *(u32x2*)(Bb + ((size_t)((bid * 4 + wave) * 5 + u) * 64 + lane) * 8) = pv;
  }
}

__global__ __launch_bounds__(256, 2) void k3_out(const char* __restrict__ Of, const char* __restrict__ Wof,
                                                 const char* __restrict__ Bb, const float* __restrict__ G,
                                                 u16* __restrict__ out) {
  __shared__ __attribute__((aligned(16))) char smem[81920];
  const int tid = threadIdx.x, lane = tid & 63;
  const int wave = __builtin_amdgcn_readfirstlane(tid >> 6);
  const int bid = blockIdx.x;
  int mt, nt;
  g2_tile(bid, mt, nt);
  f32x4 acc[5][4];
  g2_main<20>(Of, Wof, mt, nt, wave, lane, acc);
  const int g = lane >> 4, c = lane & 15;
  const int tok = mt * 64 + wave * 16 + c;
  const float gv = G[tok];
  s16x4 bv[5];
#pragma unroll
  for (int u = 0; u < 5; ++u) bv[u] = *(const s16x4*)(Bb + ((size_t)((bid * 4 + wave) * 5 + u) * 64 + lane) * 8);
  f32x4* red = (f32x4*)smem;
#pragma unroll
  for (int u = 0; u < 5; ++u)
#pragma unroll
    for (int i = 0; i < 4; ++i) red[((wave * 4 + i) * 5 + u) * 64 + lane] = acc[u][i];
  __syncthreads();
  const float omg = rbf(1.0f - gv);
#pragma unroll
  for (int u = 0; u < 5; ++u) {
    f32x4 v = red[((0 * 4 + wave) * 5 + u) * 64 + lane];
#pragma unroll
    for (int w = 1; w < 4; ++w) v += red[((w * 4 + wave) * 5 + u) * 64 + lane];
    float r[4];
#pragma unroll
    for (int e = 0; e < 4; ++e) r[e] = rbf(gv * rbf(v[e])) + rbf(omg * bf2f((u16)bv[u][e]));
    u32x2 pv = {pack2(r[0], r[1]), pack2(r[2], r[3])};
    *(u32x2*)(out + (size_t)tok * D_MODEL + nt * 80 + u * 16 + 4 * g) = pv;
  }
}

extern "C" void kernel_launch(void* const* d_in, const int* in_sizes, int n_in,
                              void* d_out, int out_size, void* d_ws, size_t ws_size,
                              hipStream_t stream) {
  const u16* x = (const u16*)d_in[0];
  const u16* wqkv = (const u16*)d_in[1];
  const u16* wo = (const u16*)d_in[2];
  const u16* wg = (const u16*)d_in[4];
  const u16* wu = (const u16*)d_in[5];
  const u16* wd = (const u16*)d_in[6];
  const u16* gw = (const u16*)d_in[7];
  char* ws = (char*)d_ws;
  const size_t QB = (size_t)S_LEN * D_MODEL * 2;
  char* Qf = ws;
  char* Kf = Qf + QB;
  char* Vf = Kf + QB;
  char* Hf = ws + (12u << 20);
  char* Of = ws + (20u << 20);
  char* Bb = ws + (24u << 20);
  char* Wdf = ws + (28u << 20);
  char* Wof = ws + (31u << 20);
  float* G = (float*)(ws + (32u << 20));
  u16* out = (u16*)d_out;

  k1_gemm<<<256, 768, 0, stream>>>(x, wqkv, wg, wu, wd, wo, gw, Qf, Vf, Hf, Wdf, Wof, G);
  k2_mix<<<256, 512, 0, stream>>>(Qf, Kf, Vf, Of, Hf, Wdf, Bb);
  k3_out<<<256, 256, 0, stream>>>(Of, Wof, Bb, G, out);
}
```

```cpp
#include <hip/hip_runtime.h>
#include <hip/hip_bf16.h>
#include <stdint.h>

typedef __attribute__((ext_vector_type(8))) __bf16 bfx8;
typedef __attribute__((ext_vector_type(2))) __bf16 bfx2;
typedef __attribute__((ext_vector_type(2))) float f32x2;
typedef __attribute__((ext_vector_type(4))) short s16x4;
typedef __attribute__((ext_vector_type(8))) short s16x8;
typedef __attribute__((ext_vector_type(4))) float f32x4;
typedef __attribute__((ext_vector_type(16))) float f32x16;
typedef __attribute__((ext_vector_type(2))) unsigned u32x2;
typedef __attribute__((ext_vector_type(4))) unsigned u32x4;
typedef __attribute__((ext_vector_type(4), aligned(8))) unsigned u32x4a8;
typedef __attribute__((address_space(3))) s16x4* lds_s16x4_ptr;
typedef unsigned short u16;

#define S_LEN 2048
#define D_MODEL 640
#define F_DIM 1728

__device__ __forceinline__ float bf2f(u16 v) { return __uint_as_float(((unsigned)v) << 16); }
__device__ __forceinline__ u16 f2bf(float f) {
  __hip_bfloat16 b = __float2bfloat16(f);
  return __builtin_bit_cast(u16, b);
}
__device__ __forceinline__ float rbf(float f) { return bf2f(f2bf(f)); }
__device__ __forceinline__ unsigned pack2(float lo, float hi) {
  bfx2 r = __builtin_convertvector(f32x2{lo, hi}, bfx2);
  return __builtin_bit_cast(unsigned, r);
}
__device__ __forceinline__ f32x4 mfma16(bfx8 a, bfx8 b, f32x4 c) {
  return __builtin_amdgcn_mfma_f32_16x16x32_bf16(a, b, c, 0, 0, 0);
}
__device__ __forceinline__ f32x16 mfma32(bfx8 a, bfx8 b, f32x16 c) {
  return __builtin_amdgcn_mfma_f32_32x32x16_bf16(a, b, c, 0, 0, 0);
}
__device__ __forceinline__ bfx8 tr_frag(const char* p0, const char* p1) {
  s16x4 a = __builtin_amdgcn_ds_read_tr16_b64_v4i16((lds_s16x4_ptr)p0);
  s16x4 b = __builtin_amdgcn_ds_read_tr16_b64_v4i16((lds_s16x4_ptr)p1);
  s16x8 v = {a[0], a[1], a[2], a[3], b[0], b[1], b[2], b[3]};
  return __builtin_bit_cast(bfx8, v);
}
#define STPOL 2
#define STPOLB 2
template <int POL>
__device__ __forceinline__ void st8p(char* p, u32x2 v) {
  if constexpr (POL == 0) *(u32x2*)p = v;
  else if constexpr (POL == 1) __builtin_nontemporal_store(v, (u32x2*)p);
  else if constexpr (POL == 2) asm volatile("global_store_dwordx2 %0, %1, off sc1" :: "v"(p), "v"(v) : "memory");
  else asm volatile("global_store_dwordx2 %0, %1, off sc0 sc1" :: "v"(p), "v"(v) : "memory");
}
__device__ __forceinline__ void st8(char* p, u32x2 v) { st8p<STPOL>(p, v); }
__device__ __forceinline__ void st8b(char* p, u32x2 v) { st8p<STPOLB>(p, v); }
#define GLOAD(r, p) asm volatile("global_load_dwordx4 %0, %1, off" : "=v"(r) : "v"(p))
#define GLOADS(r, o, p) asm volatile("global_load_dwordx4 %0, %1, %2" : "=v"(r) : "v"(o), "s"(p))
#define GLOADSO(r, o, p, imm) asm volatile("global_load_dwordx4 %0, %1, %2 offset:%3" : "=v"(r) : "v"(o), "s"(p), "i"(imm))
#define GLOAD1(r, p) asm volatile("global_load_dword %0, %1, off" : "=v"(r) : "v"(p))
#define WAITV(n, r) asm volatile("s_waitcnt vmcnt(%1)" : "+v"(r) : "i"(n))
__device__ __forceinline__ void bump(const u16*& p, int stride) {
  unsigned lo = (unsigned)(size_t)p, hi = (unsigned)((size_t)p >> 32);
  asm volatile("v_add_co_u32 %0, vcc, %0, %2\n\tv_addc_co_u32 %1, vcc, 0, %1, vcc" : "+v"(lo), "+v"(hi) : "v"(stride) : "vcc");
  p = (const u16*)(((size_t)hi << 32) | lo);
}
#define BARRIER0() asm volatile("s_barrier" ::: "memory")
#define BARRIER() asm volatile("s_waitcnt lgkmcnt(0)\n\ts_barrier" ::: "memory")
#define ST(role, i)

struct Ws {
  char *Qf, *Kf, *Vf, *Hf, *Of, *Wdf, *Wof, *Bb;
  float* G;
};

#define K1_XS 16384
#define K1_WP 704
#define K1_WG (8 * K1_WP)
#define K1_WS (4 * K1_WG)
#define K1_W0 (3 * K1_XS)
#define K1_TR K1_W0
#define K1_LDS (K1_W0 + 5 * K1_WS + 256)

__device__ __forceinline__ void k1_tile(int bid, int& mt, int& nt) {
  const int xcd = bid & 7, idx = bid >> 3;
  mt = (xcd >> 2) * 8 + (idx & 7);
  nt = (xcd & 3) * 4 + (idx >> 3);
}

#define CST(i)

#define K1_PD 4
struct K1C {
  f32x16 acc[5];
  f32x4 accs;
  int wa[6], wah[6];
  int xa[4], xsa[2];
  bfx8 wf[K1_PD + 1], xf[2], xs;
};
template <int F>
__device__ __forceinline__ bfx8 k1_wfrag(const char* smem, const K1C& st) {
  constexpr int P = F / 11, i = F % 11, sl = P % 5;
  constexpr int u = i < 10 ? i % 5 : 5;
  constexpr int off = (sl < 3 ? sl : sl - 3) * K1_WS + (i >= 5 && i < 10 ? 16 * K1_WP : 0);
  const char* b = smem + (sl < 3 ? st.wa[u] : st.wah[u]);
  return tr_frag(b + off, b + off + 4 * K1_WP);
}
template <bool UNSW, int F>
__device__ __forceinline__ void k1_steps(const char* smem, K1C& st, int wave, int lane) {
  constexpr int P = F / 11, i = F % 11;
  if constexpr (F + K1_PD < 220) st.wf[(F + K1_PD) % (K1_PD + 1)] = k1_wfrag<F + K1_PD>(smem, st);
  if constexpr (i == 0) st.xf[1] = *(const bfx8*)(smem + st.xa[(P & 1) * 2 + 1] + ((P >> 1) % 3) * K1_XS);
  if constexpr (i == 5 && P < 19)
    st.xf[0] = *(const bfx8*)(smem + st.xa[((P + 1) & 1) * 2] + (((P + 1) >> 1) % 3) * K1_XS);
  if constexpr (i == 6) st.xs = *(const bfx8*)(smem + st.xsa[P & 1] + ((P >> 1) % 3) * K1_XS);
  if constexpr (i < 10) {
    if constexpr (UNSW) st.acc[i % 5] = mfma32(st.xf[i / 5], st.wf[F % (K1_PD + 1)], st.acc[i % 5]);
    else st.acc[i % 5] = mfma32(st.wf[F % (K1_PD + 1)], st.xf[i / 5], st.acc[i % 5]);
  } else {
    st.accs = mfma16(st.wf[F % (K1_PD + 1)], st.xs, st.accs);
  }
  __builtin_amdgcn_sched_barrier(0);
  if constexpr (i == 10 && P < 18) {
    if constexpr (P & 1) { CST(P + 1); }
    BARRIER0();
    if constexpr (P & 1) { CST(P + 2); }
  }
  if constexpr (F + 1 < 220) k1_steps<UNSW, F + 1>(smem, st, wave, lane);
}

__device__ __forceinline__ float silu_mul(float g, float u) {
  return g * u * __builtin_amdgcn_rcpf(1.0f + __builtin_amdgcn_exp2f(g * -1.4426950408889634f));
}

template <bool UNSW>
__device__ __forceinline__ void k1_compute(char* smem, const u16* __restrict__ x, const u16* __restrict__ wqkv,
                                           const u16* __restrict__ wg, const u16* __restrict__ wu,
                                           char* __restrict__ Qf, char* __restrict__ Vf, char* __restrict__ Hf) {
  const int tid = threadIdx.x, lane = tid & 63;
  const int wave = __builtin_amdgcn_readfirstlane(tid >> 6);
  int mt, nt;
  k1_tile(blockIdx.x, mt, nt);
  const int wm = wave & 3, wn = wave >> 2;
  const int g = lane >> 4, c = lane & 15, hh = lane >> 5;
  const int nq = min(max(52 - nt * 10, 0), 10), ngu = 10 - nq;
  unsigned pf[2];
  {
    const int idx = blockIdx.x >> 3;
    const int qcol = nt < 2 ? 1280 + 320 * nt : 320 * nt - 640;
    const int gcol = 16 * (nt * 10 + nq - 52);
#pragma unroll
    for (int i = 0; i < 2; ++i) {
      const int e = i * 512 + tid;
      const u16* p;
      if (e < 720) {
        const int r = e / 9, q = e - r * 9;
        const int k = r * 8 + (idx & 7);
        if (q == 8) p = wqkv + (size_t)k * 1920 + 1024 + 16 * nt;
        else if (ngu == 0) p = wqkv + (size_t)k * 1920 + qcol + min(q * 64, 312);
        else if (q == 0 && nq > 0) p = wqkv + (size_t)k * 1920 + qcol;
        else p = ((q & 4) ? wu : wg) + (size_t)k * F_DIM + gcol + min((q & 3) * 64, ngu * 16 - 8);
      } else {
        const int f = min(e - 720, 319);
        const int kt = f >> 5, r = (f & 31) * 4 + (idx >> 3);
        p = x + (size_t)(mt * 128 + r) * D_MODEL + kt * 64;
      }
      pf[i] = 0;
    }
  }
  K1C st;
  {
    const int xrow = wm * 32 + (lane & 31);
    const int x0 = xrow * 128 + ((hh ^ ((xrow >> 1) & 7)) << 4);
#pragma unroll
    for (int i = 0; i < 4; ++i) st.xa[i] = x0 ^ (i * 32);
    const int xrs = wm * 32 + wn * 16 + c;
    const int xs0 = xrs * 128 + ((g ^ ((xrs >> 1) & 7)) << 4);
    st.xsa[0] = xs0;
    st.xsa[1] = xs0 ^ 64;
    const int rowp = K1_W0 + (8 * hh + (c >> 2)) * K1_WP + (c & 3) * 8;
#pragma unroll
    for (int u = 0; u < 5; ++u) {
      const int b = wn * 5 + u;
      st.wa[u] = rowp + (b < nq ? b * 64 + (g & 1) * 32 : nq * 32 + b * 32 + ((g & 1) ? ngu * 32 + 32 : 0));
    }
    st.wa[5] = K1_W0 + (8 * g + (c >> 2)) * K1_WP + (c & 3) * 8 + nq * 64 + ngu * 32;
#pragma unroll
    for (int u = 0; u < 6; ++u) {
      st.wah[u] = st.wa[u] + 3 * K1_WS;
      asm volatile("" : "+v"(st.wa[u]), "+v"(st.wah[u]));
    }
#pragma unroll
    for (int i = 0; i < 4; ++i) asm volatile("" : "+v"(st.xa[i]));
    asm volatile("" : "+v"(st.xsa[0]), "+v"(st.xsa[1]));
  }
#pragma unroll
  for (int u = 0; u < 5; ++u)
#pragma unroll
    for (int r = 0; r < 16; ++r) st.acc[u][r] = 0.f;
  st.accs = f32x4{0, 0, 0, 0};
  CST(0);
  BARRIER();
  CST(1);
  st.wf[0] = k1_wfrag<0>(smem, st);
  st.wf[1] = k1_wfrag<1>(smem, st);
  if constexpr (K1_PD > 2) st.wf[2] = k1_wfrag<2>(smem, st);
  if constexpr (K1_PD > 3) st.wf[3] = k1_wfrag<3>(smem, st);
  if constexpr (K1_PD > 4) st.wf[4] = k1_wfrag<4>(smem, st);
  st.xf[0] = *(const bfx8*)(smem + st.xa[0]);
  k1_steps<UNSW, 0>(smem, st, wave, lane);
  CST(20);
  asm volatile("s_waitcnt vmcnt(0)" : "+v"(pf[0]), "+v"(pf[1]));
  const int m0 = mt * 128 + wm * 32;
  {
    const int ts = m0 + wn * 16 + c;
    const int cc = 4 * ((ts >> 3) & 3) + (ts & 3), blk = (ts >> 5) * 2 + ((ts >> 2) & 1);
    u32x2 v = {pack2(st.accs[0], st.accs[1]), pack2(st.accs[2], st.accs[3])};
    st8(Qf + (unsigned)((128 + blk) * 20480 + (48 + 2 * nt + (g >> 1)) * 256 + cc * 16 + (g & 1) * 8), v);
  }
  const int t = m0 + (lane & 31);
  const int tq = (t >> 4) * 20480 + (t & 15) * 16 + hh * 8;
  const int tk = (128 + (t >> 5) * 2 + ((t >> 2) & 1)) * 20480 + (4 * ((t >> 3) & 3) + (t & 3)) * 16 + hh * 8;
#pragma unroll
  for (int u = 0; u < 5; ++u) {
    const int b = wn * 5 + u, B = nt * 10 + b;
    const f32x16 a = st.acc[u];
    if constexpr (UNSW) {
      const int col = 32 * B + (lane & 31);
      const int h = col / 20, d = col - h * 20;
      char* dst = Vf + (unsigned)((h * 64 + (m0 >> 5)) * 1280 + (d < 16 ? d * 16 : 1024 + (d - 16) * 16) + hh * 8);
      const int js = d < 16 ? 256 : 64;
#pragma unroll
      for (int j = 0; j < 4; ++j) {
        u32x2 v = {pack2(a[4 * j], a[4 * j + 1]), pack2(a[4 * j + 2], a[4 * j + 3])};
        st8(dst + j * js, v);
      }
    } else {
      if (b < nq) {
        char* dst = Qf + (unsigned)(B >= 40 ? tk + (B - 40) * 1024 : tq + (B - 20) * 1024);
#pragma unroll
        for (int j = 0; j < 4; ++j) {
          u32x2 v = {pack2(a[4 * j], a[4 * j + 1]), pack2(a[4 * j + 2], a[4 * j + 3])};
          st8(dst + j * 256, v);
        }
      } else {
        char* dst = Hf + (unsigned)(((t >> 4) * 216 + 2 * (B - 52)) * 256 + (t & 15) * 16 + hh * 8);
#pragma unroll
        for (int j = 0; j < 2; ++j) {
          u32x2 v = {pack2(silu_mul(a[4 * j], a[8 + 4 * j]), silu_mul(a[4 * j + 1], a[9 + 4 * j])),
                     pack2(silu_mul(a[4 * j + 2], a[10 + 4 * j]), silu_mul(a[4 * j + 3], a[11 + 4 * j]))};
          st8(dst + j * 256, v);
        }
      }
    }
  }
  CST(21);
}

struct K1Ld {
  unsigned long long gp[6];
  int pitch[6];
  const u16* xb;
  int xvoff[2];
  unsigned lds0;
  int lw, lane;
};
__device__ __forceinline__ unsigned long long rowaddr(unsigned long long p, int pitch, int rows) {
  unsigned long long r;
  asm volatile("v_mad_u64_u32 %0, vcc, %1, %2, %3" : "=v"(r) : "v"(pitch), "s"(rows), "v"(p) : "vcc");
  return r;
}
#define GLDS16(p, l) asm volatile("s_mov_b32 m0, %1\n\ts_nop 0\n\tglobal_load_lds_dwordx4 %0, off" :: "v"(p), "s"(l) : "memory")
#define GLDS16S(o, p, l) asm volatile("s_mov_b32 m0, %2\n\ts_nop 0\n\tglobal_load_lds_dwordx4 %0, %1" :: "v"(o), "s"(p), "s"(l) : "memory")
#define WAITVM(n) asm volatile("s_waitcnt vmcnt(%0)" :: "i"(n) : "memory")
template <int H>
__device__ __forceinline__ void k1_ldw(const K1Ld& st) {
  const unsigned l = st.lds0 + K1_W0 + (H % 5) * K1_WS + st.lw * K1_WG;
#pragma unroll
  for (int j = 0; j < 6; ++j) {
    unsigned long long p = rowaddr(st.gp[j], st.pitch[j], H * 32);
    if (j < 5) GLDS16(p, l + j * 1024);
    else if (st.lane < 32) GLDS16(p, l + 5120);
  }
}
template <int T, int J0, int N>
__device__ __forceinline__ void k1_ldx(const K1Ld& st) {
#pragma unroll
  for (int j = J0; j < J0 + N; ++j)
    GLDS16S(st.xvoff[j & 1], st.xb + j * 8 * D_MODEL + T * 64, st.lds0 + (T % 3) * K1_XS + (st.lw * 4 + j) * 1024);
}
template <int P>
__device__ __forceinline__ void k1_lphase(const K1Ld& st) {
  k1_ldx<P / 2 + 2, (P & 1) * 2, 2>(st);
  k1_ldw<P + 4>(st);
  WAITVM(14);
  BARRIER();
  if constexpr (P + 1 < 15) k1_lphase<P + 1>(st);
}

__device__ __forceinline__ void k1_loader(char* smem, const u16* __restrict__ x, const u16* __restrict__ wqkv,
                                          const u16* __restrict__ wg, const u16* __restrict__ wu,
                                          const u16* __restrict__ wd, const u16* __restrict__ wo,
                                          const u16* __restrict__ gw, char* __restrict__ Wdf, char* __restrict__ Wof,
                                          float* __restrict__ G) {
  const int tid = threadIdx.x, lane = tid & 63;
  const int lw = __builtin_amdgcn_readfirstlane(tid >> 6) - 8;
  int mt, nt;
  k1_tile(blockIdx.x, mt, nt);
  K1Ld st;
  st.lw = lw;
  st.lane = lane;
  st.lds0 = (unsigned)(size_t)smem;
  {
    const int xr = lane >> 3;
    st.xb = x + (size_t)mt * 128 * D_MODEL;
    st.xvoff[0] = ((lw * 32 + xr) * D_MODEL + ((lane & 7) ^ (xr >> 1)) * 8) * 2;
    st.xvoff[1] = st.xvoff[0] ^ 64;
    const int nq = min(max(52 - nt * 10, 0), 10), ngu = 10 - nq;
    const int qcol = nt < 2 ? 1280 + 320 * nt : 320 * nt - 640;
    const int gcol = 16 * (nt * 10 + nq - 52);
#pragma unroll
    for (int j = 0; j < 6; ++j) {
      const int pp = j * 64 + lane;
      const int row = pp / 44, cp = min(pp - row * 44, 41);
      const int k = 8 * lw + (row & 7);
      const int cq = cp - 4 * nq;
      if (cq < 0 || (cq >= 2 * ngu && cq < 2 * ngu + 2)) {
        const int col = cq < 0 ? qcol + cp * 8 : 1024 + 16 * nt + 8 * (cq - 2 * ngu);
        st.gp[j] = (unsigned long long)(wqkv + (size_t)k * 1920 + col);
        st.pitch[j] = 2 * 1920;
      } else {
        const int half = cq >= 2 * ngu;
        const int col = gcol + 8 * (cq - half * (2 * ngu + 2));
        st.gp[j] = (unsigned long long)((half ? wu : wg) + (size_t)k * F_DIM + col);
        st.pitch[j] = 2 * F_DIM;
      }
    }
  }
  k1_ldx<0, 0, 4>(st);
  k1_ldw<0>(st);
  k1_ldw<1>(st);
  k1_ldx<1, 0, 4>(st);
  k1_ldw<2>(st);
  k1_ldw<3>(st);
  WAITVM(16);
  BARRIER();
  k1_lphase<0>(st);
  k1_ldx<9, 2, 2>(st);
  k1_ldw<19>(st);
  const int item = blockIdx.x * 4 + lw;
  const bool has_item = item < 740;
  const int isd = item < 540;
  const int it2 = isd ? item : (has_item ? item - 540 : 0);
  const int ks = it2 / 10, n64 = it2 - ks * 10;
  u32x4 tw[4];
  {
    const u16* src = (isd ? wd : wo) + (size_t)(ks * 32 + (lane >> 3)) * D_MODEL + n64 * 64 + (lane & 7) * 8;
#pragma unroll
    for (int i = 0; i < 4; ++i) GLOAD(tw[i], src + i * 8 * D_MODEL);
  }
  const int ch2 = 64 + (lane & 15);
  u32x4 xv[2], xv2[2], wv, wv2;
#pragma unroll
  for (int r = 0; r < 2; ++r) {
    const u16* xr = x + (size_t)(item * 2 + r) * D_MODEL;
    GLOAD(xv[r], xr + lane * 8);
    GLOAD(xv2[r], xr + ch2 * 8);
  }
  GLOAD(wv, gw + lane * 8);
  GLOAD(wv2, gw + ch2 * 8);
  WAITVM(10 + 8 + 6);
  BARRIER();
  WAITVM(10 + 6);
  BARRIER();
  WAITVM(10);
  BARRIER();
  asm volatile("s_waitcnt vmcnt(0)" : "+v"(tw[0]), "+v"(tw[1]), "+v"(tw[2]), "+v"(tw[3]), "+v"(xv[0]), "+v"(xv[1]),
               "+v"(xv2[0]), "+v"(xv2[1]), "+v"(wv), "+v"(wv2));
  if (has_item) {
    char* tb = smem + K1_TR + lw * 5632;
#pragma unroll
    for (int i = 0; i < 4; ++i) *(u32x4*)(tb + i * 1408 + (lane >> 3) * 160 + (lane & 7) * 16) = tw[i];
    const int g = lane >> 4, c = lane & 15;
    const char* rp = tb + g * 1408 + (c >> 2) * 160 + (c & 3) * 8;
    char* dst = (isd ? Wdf : Wof) + ((size_t)(ks * 40 + n64 * 4) * 64 + lane) * 16;
#pragma unroll
    for (int nb = 0; nb < 4; ++nb) {
      bfx8 f = tr_frag(rp + nb * 32, rp + nb * 32 + 640);
      *(bfx8*)(dst + nb * 1024) = f;
    }
  }
#pragma unroll
  for (int r = 0; r < 2; ++r) {
    float s = 0.f;
#pragma unroll
    for (int e = 0; e < 4; ++e) {
      s += rbf(__uint_as_float(xv[r][e] << 16) * __uint_as_float(wv[e] << 16));
      s += rbf(__uint_as_float(xv[r][e] & 0xffff0000u) * __uint_as_float(wv[e] & 0xffff0000u));
    }
    if (lane < 16) {
#pragma unroll
      for (int e = 0; e < 4; ++e) {
        s += rbf(__uint_as_float(xv2[r][e] << 16) * __uint_as_float(wv2[e] << 16));
        s += rbf(__uint_as_float(xv2[r][e] & 0xffff0000u) * __uint_as_float(wv2[e] & 0xffff0000u));
      }
    }
#pragma unroll
    for (int o = 32; o >= 1; o >>= 1) s += __shfl_xor(s, o);
    s = rbf(s);
    float gv = rbf(1.0f / (1.0f + __expf(-s)));
    if (lane == 0) G[item * 2 + r] = gv;
  }
}

__global__ __launch_bounds__(768, 3) void k1_gemm(const u16* __restrict__ x, const u16* __restrict__ wqkv,
                                                  const u16* __restrict__ wg, const u16* __restrict__ wu,
                                                  const u16* __restrict__ wd, const u16* __restrict__ wo,
                                                  const u16* __restrict__ gw, char* __restrict__ Qf,
                                                  char* __restrict__ Vf, char* __restrict__ Hf,
                                                  char* __restrict__ Wdf, char* __restrict__ Wof,
                                                  float* __restrict__ G) {
  __shared__ __attribute__((aligned(16))) char smem[K1_LDS];
  const int w8 = __builtin_amdgcn_readfirstlane(threadIdx.x) >> 9;
  int mt, nt;
  k1_tile(blockIdx.x, mt, nt);
  if (w8) k1_loader(smem, x, wqkv, wg, wu, wd, wo, gw, Wdf, Wof, G);
  else if (nt < 2) k1_compute<true>(smem, x, wqkv, wg, wu, Qf, Vf, Hf);
  else k1_compute<false>(smem, x, wqkv, wg, wu, Qf, Vf, Hf);
}

#define AT_K 15360
#define AT_LDS (AT_K + 12800)
__device__ __forceinline__ void attn_stage(const char* __restrict__ Kf, const char* __restrict__ Vf, unsigned lds,
                                           int h, int qb0, int lane) {
  const int slot0 = (20 * h) >> 3;
#pragma unroll
  for (int i = 0; i < 15; ++i) {
    const int p = i * 64 + lane;
    const int blk = p / 48, w = p - blk * 48;
    const int gb = max(qb0 - 8 + (blk >> 1), 0) * 2 + (blk & 1);
    GLDS16S(gb * 20480 + w * 16, Kf + slot0 * 256, lds + i * 1024);
  }
#pragma unroll
  for (int i = 0; i < 13; ++i) {
    const int p = i * 64 + lane;
    const int grp = p / 80, w = p - grp * 80;
    const int gg = max(qb0 - 8 + grp, 0);
    if (i < 12 || lane < 32) GLDS16S(gg * 1280 + w * 16, Vf + (size_t)h * 81920, lds + AT_K + i * 1024);
  }
}
__device__ __forceinline__ void attn_item(const u32x4 (&qv)[2], const char* kl, const char* vl, char* __restrict__ Of,
                                          int h, int qb, int lane) {
  asm volatile("" : "+v"(lane));
  const int g = lane >> 4, c = lane & 15;
  bfx8 qf[2];
#pragma unroll
  for (int t = 0; t < 2; ++t) {
    u32x4 v = qv[t];
    if (g == 3 || (g == 2 && !(h & 1))) { v[2] = 0; v[3] = 0; }
    if (g == 3 || (g == 0 && (h & 1))) { v[0] = 0; v[1] = 0; }
    qf[t] = __builtin_bit_cast(bfx8, v);
  }
  f32x4 s[9][2][2];
  const char* kp = kl + min(g, 2) * 256 + c * 16;
  bfx8 kf[18];
#pragma unroll
  for (int j = 0; j < 18; ++j) kf[j] = *(const bfx8*)(kp + j * 768);
  __builtin_amdgcn_sched_barrier(0);
#pragma unroll
  for (int j = 0; j < 9; ++j) {
#pragma unroll
    for (int a = 0; a < 2; ++a) {
#pragma unroll
      for (int t = 0; t < 2; ++t) s[j][a][t] = mfma16(kf[j * 2 + a], qf[t], f32x4{0, 0, 0, 0});
    }
  }
  __builtin_amdgcn_sched_barrier(0);
  u32x4 v0[9], v1[9];
#pragma unroll
  for (int j = 0; j < 9; ++j) {
    v0[j] = *(const u32x4*)(vl + j * 1280 + lane * 16);
    v1[j] = *(const u32x4*)(vl + j * 1280 + 1024 + g * 64 + (c & 3) * 16);
  }
  __builtin_amdgcn_sched_barrier(0);
  const float NEG = -1e30f;
#pragma unroll
  for (int a = 0; a < 2; ++a)
#pragma unroll
    for (int t = 0; t < 2; ++t)
#pragma unroll
      for (int r = 0; r < 4; ++r) {
        int ko = 8 * g + 4 * a + r, qo = 16 * t + c;
        if (!(ko > qo)) s[0][a][t][r] = NEG;
        if (!(ko <= qo)) s[8][a][t][r] = NEG;
      }
  if (qb < 8) {
#pragma unroll
    for (int j = 0; j < 8; ++j)
      if (j < 8 - qb) {
#pragma unroll
        for (int a = 0; a < 2; ++a)
#pragma unroll
          for (int t = 0; t < 2; ++t) s[j][a][t] = f32x4{NEG, NEG, NEG, NEG};
      }
  }
  const float cs = 0.22360679774997896f * 1.4426950408889634f;
  bfx8 pf[9][2];
  float mx[2][4];
#pragma unroll
  for (int t = 0; t < 2; ++t)
#pragma unroll
    for (int r = 0; r < 4; ++r) mx[t][r] = fmaxf(s[4][0][t][r], s[4][1][t][r]);
#pragma unroll
  for (int j = 0; j < 9; ++j)
    if (j != 4) {
#pragma unroll
      for (int t = 0; t < 2; ++t)
#pragma unroll
        for (int r = 0; r < 4; ++r) mx[t][r] = fmaxf(fmaxf(mx[t][r], s[j][0][t][r]), s[j][1][t][r]);
    }
  float mm[2];
#pragma unroll
  for (int t = 0; t < 2; ++t) mm[t] = fmaxf(fmaxf(mx[t][0], mx[t][1]), fmaxf(mx[t][2], mx[t][3]));
#pragma unroll
  for (int t = 0; t < 2; ++t) mm[t] = fmaxf(mm[t], __shfl_xor(mm[t], 16));
#pragma unroll
  for (int t = 0; t < 2; ++t) mm[t] = fmaxf(mm[t], __shfl_xor(mm[t], 32));
#pragma unroll
  for (int t = 0; t < 2; ++t) {
    const float mc = mm[t] * cs;
#pragma unroll
    for (int j = 0; j < 9; ++j) {
      u32x4 pk;
#pragma unroll
      for (int a = 0; a < 2; ++a) {
        float p0 = __builtin_amdgcn_exp2f(fmaf(s[j][a][t][0], cs, -mc));
        float p1 = __builtin_amdgcn_exp2f(fmaf(s[j][a][t][1], cs, -mc));
        float p2 = __builtin_amdgcn_exp2f(fmaf(s[j][a][t][2], cs, -mc));
        float p3 = __builtin_amdgcn_exp2f(fmaf(s[j][a][t][3], cs, -mc));
        pk[2 * a] = pack2(p0, p1);
        pk[2 * a + 1] = pack2(p2, p3);
      }
      pf[j][t] = __builtin_bit_cast(bfx8, pk);
    }
  }
  f32x4 o0[2] = {f32x4{0, 0, 0, 0}, f32x4{0, 0, 0, 0}}, o1[2] = {f32x4{0, 0, 0, 0}, f32x4{0, 0, 0, 0}};
#pragma unroll
  for (int j = 0; j < 9; ++j) {
    u32x4 w1 = v1[j];
    if (c == 4) w1 = u32x4{0x3f803f80u, 0x3f803f80u, 0x3f803f80u, 0x3f803f80u};
    bfx8 vf0 = __builtin_bit_cast(bfx8, v0[j]), vf1 = __builtin_bit_cast(bfx8, w1);
#pragma unroll
    for (int t = 0; t < 2; ++t) {
      o0[t] = mfma16(vf0, pf[j][t], o0[t]);
      o1[t] = mfma16(vf1, pf[j][t], o1[t]);
    }
  }
#pragma unroll
  for (int t = 0; t < 2; ++t) {
    float l = __shfl(o1[t][0], 16 + c);
    float inv = 1.0f / l;
    char* dst = Of + (size_t)(qb * 2 + t) * 20480 + c * 16;
    const int k0 = h * 20 + 4 * g;
    u32x2 v = {pack2(o0[t][0] * inv, o0[t][1] * inv), pack2(o0[t][2] * inv, o0[t][3] * inv)};
    st8b(dst + (k0 >> 3) * 256 + ((k0 >> 2) & 1) * 8, v);
    if (g == 0) {
      const int k1 = h * 20 + 16;
      u32x2 w = {pack2(o1[t][0] * inv, o1[t][1] * inv), pack2(o1[t][2] * inv, o1[t][3] * inv)};
      st8b(dst + (k1 >> 3) * 256 + ((k1 >> 2) & 1) * 8, w);
    }
  }
}

struct G2Frag { u32x4 a[4], w[5]; };
template <int NKS>
__device__ __forceinline__ void g2_issue(G2Frag& f, const char* Ab, const char* Wb, int ks, int voff) {
  const char* ap = Ab + (size_t)ks * 1024;
  const char* wp = Wb + (size_t)ks * 40960 + 2048;
#pragma unroll
  for (int i = 0; i < 4; ++i) GLOADS(f.a[i], voff, ap + (size_t)i * NKS * 1024);
  GLOADSO(f.w[0], voff, wp, -2048);
  GLOADSO(f.w[1], voff, wp, -1024);
  GLOADSO(f.w[2], voff, wp, 0);
  GLOADSO(f.w[3], voff, wp, 1024);
  GLOADSO(f.w[4], voff, wp, 2048);
}
template <int N>
__device__ __forceinline__ void g2_wait(G2Frag& f) {
  asm volatile("s_waitcnt vmcnt(%9)"
               : "+v"(f.a[0]), "+v"(f.a[1]), "+v"(f.a[2]), "+v"(f.a[3]), "+v"(f.w[0]), "+v"(f.w[1]), "+v"(f.w[2]),
                 "+v"(f.w[3]), "+v"(f.w[4])
               : "i"(N));
}
template <int NKS, int NST, int S>
__device__ __forceinline__ void g2_steps(G2Frag (&f)[4], f32x4 (&acc)[5][4], const char* Ab, const char* Wb, int wave,
                                         int voff) {
  constexpr int ahead = (NST - 1 - S) < 3 ? (NST - 1 - S) : 3;
  G2Frag& cur = f[S & 3];
  g2_wait<9 * ahead>(cur);
  if (S * 4 + 3 < NKS || S * 4 + wave < NKS) {
#pragma unroll
    for (int u = 0; u < 5; ++u)
#pragma unroll
      for (int i = 0; i < 4; ++i)
        acc[u][i] = mfma16(__builtin_bit_cast(bfx8, cur.w[u]), __builtin_bit_cast(bfx8, cur.a[i]), acc[u][i]);
  }
  __builtin_amdgcn_sched_barrier(0);
  if constexpr (S + 4 < NST) g2_issue<NKS>(cur, Ab, Wb, min((S + 4) * 4 + wave, NKS - 1), voff);
  if constexpr (S + 1 < NST) g2_steps<NKS, NST, S + 1>(f, acc, Ab, Wb, wave, voff);
}
template <int NKS>
__device__ __forceinline__ void g2_main(const char* __restrict__ Af, const char* __restrict__ Wf, int mt, int nt,
                                        int wave, int lane, f32x4 (&acc)[5][4]) {
  constexpr int NST = (NKS + 3) / 4;
  const char* Ab = Af + (size_t)mt * 4 * NKS * 1024;
  const char* Wb = Wf + (size_t)nt * 5120;
  const int voff = lane * 16;
#pragma unroll
  for (int u = 0; u < 5; ++u)
#pragma unroll
    for (int i = 0; i < 4; ++i) acc[u][i] = f32x4{0, 0, 0, 0};
  G2Frag f[4];
#pragma unroll
  for (int s = 0; s < 4; ++s) g2_issue<NKS>(f[s], Ab, Wb, min(s * 4 + wave, NKS - 1), voff);
  g2_steps<NKS, NST, 0>(f, acc, Ab, Wb, wave, voff);
}
__device__ __forceinline__ void g2_tile(int bid, int& mt, int& nt) {
  const int xcd = bid & 7, idx = bid >> 3;
  mt = (xcd >> 1) * 8 + (idx & 7);
  nt = (xcd & 1) * 4 + (idx >> 3);
}

__global__ __launch_bounds__(512, 2) void k2_mix(const char* __restrict__ Qf, const char* __restrict__ Kf,
                                                 const char* __restrict__ Vf, char* __restrict__ Of,
                                                 const char* __restrict__ Hf, const char* __restrict__ Wdf,
                                                 char* __restrict__ Bb) {
  __shared__ __attribute__((aligned(16))) char smem[4 * AT_LDS];
  const int tid = threadIdx.x, lane = tid & 63;
  const int wave = __builtin_amdgcn_readfirstlane(tid >> 6) ^ 4;
  const int bid = blockIdx.x;
  if (wave >= 4) {
    const int h = (bid & 7) * 4 + (wave - 4);
    const int qb0 = (bid >> 3) * 2;
    if (wave == 4) ST(0, 0);
    char* al = smem + (wave - 4) * AT_LDS;
    attn_stage(Kf, Vf, (unsigned)(size_t)al, h, qb0, lane);
    u32x4 qv[2][2];
    {
      const char* qp = Qf + (size_t)(qb0 * 2) * 20480 + (((20 * h) >> 3) + min(lane >> 4, 2)) * 256 + (lane & 15) * 16;
#pragma unroll
      for (int i = 0; i < 4; ++i) GLOAD(qv[i >> 1][i & 1], qp + i * 20480);
    }
    asm volatile("s_waitcnt vmcnt(0)" : "+v"(qv[0][0]), "+v"(qv[0][1]), "+v"(qv[1][0]), "+v"(qv[1][1]) :: "memory");
    if (wave == 4) ST(0, 1);
    attn_item(qv[0], al, al + AT_K, Of, h, qb0, lane);
    if (wave == 4) ST(0, 2);
    attn_item(qv[1], al + 1536, al + AT_K + 1280, Of, h, qb0 + 1, lane);
    if (wave == 4) ST(0, 3);
    BARRIER();
    return;
  }
  int mt, nt;
  g2_tile(bid, mt, nt);
  f32x4 acc[5][4];
  if (wave == 0) ST(1, 0);
  g2_main<54>(Hf, Wdf, mt, nt, wave, lane, acc);
  if (wave == 0) ST(1, 15);
  f32x4* red = (f32x4*)smem;
  BARRIER();
#pragma unroll
  for (int u = 0; u < 5; ++u)
#pragma unroll
    for (int i = 0; i < 4; ++i) red[((wave * 4 + i) * 5 + u) * 64 + lane] = acc[u][i];
  BARRIER();
#pragma unroll
  for (int u = 0; u < 5; ++u) {
    f32x4 v = red[((0 * 4 + wave) * 5 + u) * 64 + lane];
#pragma unroll
    for (int w = 1; w < 4; ++w) v += red[((w * 4 + wave) * 5 + u) * 64 + lane];
    u32x2 pv = {pack2(v[0], v[1]), pack2(v[2], v[3])};
    st8b(Bb + ((size_t)((bid * 4 + wave) * 5 + u) * 64 + lane) * 8, pv);
  }
  if (wave == 0) ST(1, 16);
}

__global__ __launch_bounds__(256, 2) void k3_out(const char* __restrict__ Of, const char* __restrict__ Wof,
                                                 const char* __restrict__ Bb, const float* __restrict__ G,
                                                 u16* __restrict__ out) {
  __shared__ __attribute__((aligned(16))) char smem[81920];
  const int tid = threadIdx.x, lane = tid & 63;
  const int wave = __builtin_amdgcn_readfirstlane(tid >> 6);
  const int bid = blockIdx.x;
  int mt, nt;
  g2_tile(bid, mt, nt);
  f32x4 acc[5][4];
  g2_main<20>(Of, Wof, mt, nt, wave, lane, acc);
  const int g = lane >> 4, c = lane & 15;
  const int tok = mt * 64 + wave * 16 + c;
  const float gv = G[tok];
  s16x4 bv[5];
#pragma unroll
  for (int u = 0; u < 5; ++u) bv[u] = *(const s16x4*)(Bb + ((size_t)((bid * 4 + wave) * 5 + u) * 64 + lane) * 8);
  f32x4* red = (f32x4*)smem;
#pragma unroll
  for (int u = 0; u < 5; ++u)
#pragma unroll
    for (int i = 0; i < 4; ++i) red[((wave * 4 + i) * 5 + u) * 64 + lane] = acc[u][i];
  __syncthreads();
  const float omg = rbf(1.0f - gv);
#pragma unroll
  for (int u = 0; u < 5; ++u) {
    f32x4 v = red[((0 * 4 + wave) * 5 + u) * 64 + lane];
#pragma unroll
    for (int w = 1; w < 4; ++w) v += red[((w * 4 + wave) * 5 + u) * 64 + lane];
    float r[4];
#pragma unroll
    for (int e = 0; e < 4; ++e) r[e] = rbf(gv * rbf(v[e])) + rbf(omg * bf2f((u16)bv[u][e]));
    u32x2 pv = {pack2(r[0], r[1]), pack2(r[2], r[3])};
    *(u32x2*)(out + (size_t)tok * D_MODEL + nt * 80 + u * 16 + 4 * g) = pv;
  }
}

extern "C" void kernel_launch(void* const* d_in, const int* in_sizes, int n_in,
                              void* d_out, int out_size, void* d_ws, size_t ws_size,
                              hipStream_t stream) {
  const u16* x = (const u16*)d_in[0];
  const u16* wqkv = (const u16*)d_in[1];
  const u16* wo = (const u16*)d_in[2];
  const u16* wg = (const u16*)d_in[4];
  const u16* wu = (const u16*)d_in[5];
  const u16* wd = (const u16*)d_in[6];
  const u16* gw = (const u16*)d_in[7];
  char* ws = (char*)d_ws;
  const size_t QB = (size_t)S_LEN * D_MODEL * 2;
  char* Qf = ws;
  char* Kf = Qf + QB;
  char* Vf = Kf + QB;
  char* Hf = ws + (12u << 20);
  char* Of = ws + (20u << 20);
  char* Bb = ws + (24u << 20);
  char* Wdf = ws + (28u << 20);
  char* Wof = ws + (31u << 20);
  float* G = (float*)(ws + (32u << 20));
  u16* out = (u16*)d_out;

  k1_gemm<<<256, 768, 0, stream>>>(x, wqkv, wg, wu, wd, wo, gw, Qf, Vf, Hf, Wdf, Wof, G);
  k2_mix<<<256, 512, 0, stream>>>(Qf, Kf, Vf, Of, Hf, Wdf, Bb);
  k3_out<<<256, 256, 0, stream>>>(Of, Wof, Bb, G, out);
}
```

```cpp
#include <hip/hip_runtime.h>
#include <hip/hip_bf16.h>
#include <stdint.h>

typedef __attribute__((ext_vector_type(8))) __bf16 bfx8;
typedef __attribute__((ext_vector_type(2))) __bf16 bfx2;
typedef __attribute__((ext_vector_type(2))) float f32x2;
typedef __attribute__((ext_vector_type(4))) short s16x4;
typedef __attribute__((ext_vector_type(8))) short s16x8;
typedef __attribute__((ext_vector_type(4))) float f32x4;
typedef __attribute__((ext_vector_type(16))) float f32x16;
typedef __attribute__((ext_vector_type(2))) unsigned u32x2;
typedef __attribute__((ext_vector_type(4))) unsigned u32x4;
typedef __attribute__((ext_vector_type(4), aligned(8))) unsigned u32x4a8;
typedef __attribute__((address_space(3))) s16x4* lds_s16x4_ptr;
typedef unsigned short u16;

#define S_LEN 2048
#define D_MODEL 640
#define F_DIM 1728

__device__ __forceinline__ float bf2f(u16 v) { return __uint_as_float(((unsigned)v) << 16); }
__device__ __forceinline__ u16 f2bf(float f) {
  __hip_bfloat16 b = __float2bfloat16(f);
  return __builtin_bit_cast(u16, b);
}
__device__ __forceinline__ float rbf(float f) { return bf2f(f2bf(f)); }
__device__ __forceinline__ unsigned pack2(float lo, float hi) {
  bfx2 r = __builtin_convertvector(f32x2{lo, hi}, bfx2);
  return __builtin_bit_cast(unsigned, r);
}
__device__ __forceinline__ f32x4 mfma16(bfx8 a, bfx8 b, f32x4 c) {
  return __builtin_amdgcn_mfma_f32_16x16x32_bf16(a, b, c, 0, 0, 0);
}
__device__ __forceinline__ f32x16 mfma32(bfx8 a, bfx8 b, f32x16 c) {
  return __builtin_amdgcn_mfma_f32_32x32x16_bf16(a, b, c, 0, 0, 0);
}
__device__ __forceinline__ bfx8 tr_frag(const char* p0, const char* p1) {
  s16x4 a = __builtin_amdgcn_ds_read_tr16_b64_v4i16((lds_s16x4_ptr)p0);
  s16x4 b = __builtin_amdgcn_ds_read_tr16_b64_v4i16((lds_s16x4_ptr)p1);
  s16x8 v = {a[0], a[1], a[2], a[3], b[0], b[1], b[2], b[3]};
  return __builtin_bit_cast(bfx8, v);
}
#define STPOL 2
#define STPOLB 2
template <int POL>
__device__ __forceinline__ void st8p(char* p, u32x2 v) {
  if constexpr (POL == 0) *(u32x2*)p = v;
  else if constexpr (POL == 1) __builtin_nontemporal_store(v, (u32x2*)p);
  else if constexpr (POL == 2) asm volatile("global_store_dwordx2 %0, %1, off sc1" :: "v"(p), "v"(v) : "memory");
  else asm volatile("global_store_dwordx2 %0, %1, off sc0 sc1" :: "v"(p), "v"(v) : "memory");
}
__device__ __forceinline__ void st8(char* p, u32x2 v) { st8p<STPOL>(p, v); }
__device__ __forceinline__ void st8b(char* p, u32x2 v) { st8p<STPOLB>(p, v); }
#define GLOAD(r, p) asm volatile("global_load_dwordx4 %0, %1, off" : "=v"(r) : "v"(p))
#define GLOADS(r, o, p) asm volatile("global_load_dwordx4 %0, %1, %2" : "=v"(r) : "v"(o), "s"(p))
#define GLOADSO(r, o, p, imm) asm volatile("global_load_dwordx4 %0, %1, %2 offset:%3" : "=v"(r) : "v"(o), "s"(p), "i"(imm))
#define GLOAD1(r, p) asm volatile("global_load_dword %0, %1, off" : "=v"(r) : "v"(p))
#define WAITV(n, r) asm volatile("s_waitcnt vmcnt(%1)" : "+v"(r) : "i"(n))
__device__ __forceinline__ void bump(const u16*& p, int stride) {
  unsigned lo = (unsigned)(size_t)p, hi = (unsigned)((size_t)p >> 32);
  asm volatile("v_add_co_u32 %0, vcc, %0, %2\n\tv_addc_co_u32 %1, vcc, 0, %1, vcc" : "+v"(lo), "+v"(hi) : "v"(stride) : "vcc");
  p = (const u16*)(((size_t)hi << 32) | lo);
}
#define BARRIER0() asm volatile("s_barrier" ::: "memory")
#define BARRIER() asm volatile("s_waitcnt lgkmcnt(0)\n\ts_barrier" ::: "memory")
#define ST(role, i)

struct Ws {
  char *Qf, *Kf, *Vf, *Hf, *Of, *Wdf, *Wof, *Bb;
  float* G;
};

#define K1_XS 16384
#define K1_WP 704
#define K1_WG (8 * K1_WP)
#define K1_WS (4 * K1_WG)
#define K1_W0 (3 * K1_XS)
#define K1_TR K1_W0
#define K1_LDS (K1_W0 + 5 * K1_WS + 256)

__device__ __forceinline__ void k1_tile(int bid, int& mt, int& nt) {
  const int xcd = bid & 7, idx = bid >> 3;
  mt = (xcd >> 2) * 8 + (idx & 7);
  nt = (xcd & 3) * 4 + (idx >> 3);
}

#define CST(i)

#define K1_PD 4
struct K1C {
  f32x16 acc[5];
  f32x4 accs;
  int wa[6], wah[6];
  int xa[4], xsa[2];
  bfx8 wf[K1_PD + 1], xf[2], xs;
};
template <int F>
__device__ __forceinline__ bfx8 k1_wfrag(const char* smem, const K1C& st) {
  constexpr int P = F / 11, i = F % 11, sl = P % 5;
  constexpr int u = i < 10 ? i % 5 : 5;
  constexpr int off = (sl < 3 ? sl : sl - 3) * K1_WS + (i >= 5 && i < 10 ? 16 * K1_WP : 0);
  const char* b = smem + (sl < 3 ? st.wa[u] : st.wah[u]);
  return tr_frag(b + off, b + off + 4 * K1_WP);
}
template <bool UNSW, int F>
__device__ __forceinline__ void k1_steps(const char* smem, K1C& st, int wave, int lane) {
  constexpr int P = F / 11, i = F % 11;
  if constexpr (F + K1_PD < 220) st.wf[(F + K1_PD) % (K1_PD + 1)] = k1_wfrag<F + K1_PD>(smem, st);
  if constexpr (i == 0) st.xf[1] = *(const bfx8*)(smem + st.xa[(P & 1) * 2 + 1] + ((P >> 1) % 3) * K1_XS);
  if constexpr (i == 5 && P < 19)
    st.xf[0] = *(const bfx8*)(smem + st.xa[((P + 1) & 1) * 2] + (((P + 1) >> 1) % 3) * K1_XS);
  if constexpr (i == 6) st.xs = *(const bfx8*)(smem + st.xsa[P & 1] + ((P >> 1) % 3) * K1_XS);
  if constexpr (i < 10) {
    if constexpr (UNSW) st.acc[i % 5] = mfma32(st.xf[i / 5], st.wf[F % (K1_PD + 1)], st.acc[i % 5]);
    else st.acc[i % 5] = mfma32(st.wf[F % (K1_PD + 1)], st.xf[i / 5], st.acc[i % 5]);
  } else {
    st.accs = mfma16(st.wf[F % (K1_PD + 1)], st.xs, st.accs);
  }
  __builtin_amdgcn_sched_barrier(0);
  if constexpr (i == 10 && P < 18) {
    if constexpr (P & 1) { CST(P + 1); }
    BARRIER0();
    if constexpr (P & 1) { CST(P + 2); }
  }
  if constexpr (F + 1 < 220) k1_steps<UNSW, F + 1>(smem, st, wave, lane);
}

__device__ __forceinline__ float silu_mul(float g, float u) {
  return g * u * __builtin_amdgcn_rcpf(1.0f + __builtin_amdgcn_exp2f(g * -1.4426950408889634f));
}

template <bool UNSW>
__device__ __forceinline__ void k1_compute(char* smem, const u16* __restrict__ x, const u16* __restrict__ wqkv,
                                           const u16* __restrict__ wg, const u16* __restrict__ wu,
                                           char* __restrict__ Qf, char* __restrict__ Vf, char* __restrict__ Hf) {
  const int tid = threadIdx.x, lane = tid & 63;
  const int wave = __builtin_amdgcn_readfirstlane(tid >> 6);
  int mt, nt;
  k1_tile(blockIdx.x, mt, nt);
  const int wm = wave & 3, wn = wave >> 2;
  const int g = lane >> 4, c = lane & 15, hh = lane >> 5;
  const int nq = min(max(52 - nt * 10, 0), 10), ngu = 10 - nq;
  unsigned pf[2];
  {
    const int idx = blockIdx.x >> 3;
    const int qcol = nt < 2 ? 1280 + 320 * nt : 320 * nt - 640;
    const int gcol = 16 * (nt * 10 + nq - 52);
#pragma unroll
    for (int i = 0; i < 2; ++i) {
      const int e = i * 512 + tid;
      const u16* p;
      if (e < 720) {
        const int r = e / 9, q = e - r * 9;
        const int k = r * 8 + (idx & 7);
        if (q == 8) p = wqkv + (size_t)k * 1920 + 1024 + 16 * nt;
        else if (ngu == 0) p = wqkv + (size_t)k * 1920 + qcol + min(q * 64, 312);
        else if (q == 0 && nq > 0) p = wqkv + (size_t)k * 1920 + qcol;
        else p = ((q & 4) ? wu : wg) + (size_t)k * F_DIM + gcol + min((q & 3) * 64, ngu * 16 - 8);
      } else {
        const int f = min(e - 720, 319);
        const int kt = f >> 5, r = (f & 31) * 4 + (idx >> 3);
        p = x + (size_t)(mt * 128 + r) * D_MODEL + kt * 64;
      }
      pf[i] = 0;
    }
  }
  K1C st;
  {
    const int xrow = wm * 32 + (lane & 31);
    const int x0 = xrow * 128 + ((hh ^ ((xrow >> 1) & 7)) << 4);
#pragma unroll
    for (int i = 0; i < 4; ++i) st.xa[i] = x0 ^ (i * 32);
    const int xrs = wm * 32 + wn * 16 + c;
    const int xs0 = xrs * 128 + ((g ^ ((xrs >> 1) & 7)) << 4);
    st.xsa[0] = xs0;
    st.xsa[1] = xs0 ^ 64;
    const int rowp = K1_W0 + (8 * hh + (c >> 2)) * K1_WP + (c & 3) * 8;
#pragma unroll
    for (int u = 0; u < 5; ++u) {
      const int b = wn * 5 + u;
      st.wa[u] = rowp + (b < nq ? b * 64 + (g & 1) * 32 : nq * 32 + b * 32 + ((g & 1) ? ngu * 32 + 32 : 0));
    }
    st.wa[5] = K1_W0 + (8 * g + (c >> 2)) * K1_WP + (c & 3) * 8 + nq * 64 + ngu * 32;
#pragma unroll
    for (int u = 0; u < 6; ++u) {
      st.wah[u] = st.wa[u] + 3 * K1_WS;
      asm volatile("" : "+v"(st.wa[u]), "+v"(st.wah[u]));
    }
#pragma unroll
    for (int i = 0; i < 4; ++i) asm volatile("" : "+v"(st.xa[i]));
    asm volatile("" : "+v"(st.xsa[0]), "+v"(st.xsa[1]));
  }
#pragma unroll
  for (int u = 0; u < 5; ++u)
#pragma unroll
    for (int r = 0; r < 16; ++r) st.acc[u][r] = 0.f;
  st.accs = f32x4{0, 0, 0, 0};
  CST(0);
  BARRIER();
  CST(1);
  st.wf[0] = k1_wfrag<0>(smem, st);
  st.wf[1] = k1_wfrag<1>(smem, st);
  if constexpr (K1_PD > 2) st.wf[2] = k1_wfrag<2>(smem, st);
  if constexpr (K1_PD > 3) st.wf[3] = k1_wfrag<3>(smem, st);
  if constexpr (K1_PD > 4) st.wf[4] = k1_wfrag<4>(smem, st);
  st.xf[0] = *(const bfx8*)(smem + st.xa[0]);
  k1_steps<UNSW, 0>(smem, st, wave, lane);
  CST(20);
  asm volatile("s_waitcnt vmcnt(0)" : "+v"(pf[0]), "+v"(pf[1]));
  const int m0 = mt * 128 + wm * 32;
  {
    const int ts = m0 + wn * 16 + c;
    const int cc = 4 * ((ts >> 3) & 3) + (ts & 3), blk = (ts >> 5) * 2 + ((ts >> 2) & 1);
    u32x2 v = {pack2(st.accs[0], st.accs[1]), pack2(st.accs[2], st.accs[3])};
    st8(Qf + (unsigned)((128 + blk) * 20480 + (48 + 2 * nt + (g >> 1)) * 256 + cc * 16 + (g & 1) * 8), v);
  }
  const int t = m0 + (lane & 31);
  const int tq = (t >> 4) * 20480 + (t & 15) * 16 + hh * 8;
  const int tk = (128 + (t >> 5) * 2 + ((t >> 2) & 1)) * 20480 + (4 * ((t >> 3) & 3) + (t & 3)) * 16 + hh * 8;
#pragma unroll
  for (int u = 0; u < 5; ++u) {
    const int b = wn * 5 + u, B = nt * 10 + b;
    const f32x16 a = st.acc[u];
    if constexpr (UNSW) {
      const int col = 32 * B + (lane & 31);
      const int h = col / 20, d = col - h * 20;
      char* dst = Vf + (unsigned)((h * 64 + (m0 >> 5)) * 1280 + (d < 16 ? d * 16 : 1024 + (d - 16) * 16) + hh * 8);
      const int js = d < 16 ? 256 : 64;
#pragma unroll
      for (int j = 0; j < 4; ++j) {
        u32x2 v = {pack2(a[4 * j], a[4 * j + 1]), pack2(a[4 * j + 2], a[4 * j + 3])};
        st8(dst + j * js, v);
      }
    } else {
      if (b < nq) {
        char* dst = Qf + (unsigned)(B >= 40 ? tk + (B - 40) * 1024 : tq + (B - 20) * 1024);
#pragma unroll
        for (int j = 0; j < 4; ++j) {
          u32x2 v = {pack2(a[4 * j], a[4 * j + 1]), pack2(a[4 * j + 2], a[4 * j + 3])};
          st8(dst + j * 256, v);
        }
      } else {
        char* dst = Hf + (unsigned)(((t >> 4) * 216 + 2 * (B - 52)) * 256 + (t & 15) * 16 + hh * 8);
#pragma unroll
        for (int j = 0; j < 2; ++j) {
          u32x2 v = {pack2(silu_mul(a[4 * j], a[8 + 4 * j]), silu_mul(a[4 * j + 1], a[9 + 4 * j])),
                     pack2(silu_mul(a[4 * j + 2], a[10 + 4 * j]), silu_mul(a[4 * j + 3], a[11 + 4 * j]))};
          st8(dst + j * 256, v);
        }
      }
    }
  }
  CST(21);
}

struct K1Ld {
  unsigned long long gp[6];
  int pitch[6];
  const u16* xb;
  int xvoff[2];
  unsigned lds0;
  int lw, lane, rot;
};
__device__ __forceinline__ unsigned long long rowaddr(unsigned long long p, int pitch, int rows) {
  unsigned long long r;
  asm volatile("v_mad_u64_u32 %0, vcc, %1, %2, %3" : "=v"(r) : "v"(pitch), "s"(rows), "v"(p) : "vcc");
  return r;
}
#define GLDS16(p, l) asm volatile("s_mov_b32 m0, %1\n\ts_nop 0\n\tglobal_load_lds_dwordx4 %0, off" :: "v"(p), "s"(l) : "memory")
#define GLDS16S(o, p, l) asm volatile("s_mov_b32 m0, %2\n\ts_nop 0\n\tglobal_load_lds_dwordx4 %0, %1" :: "v"(o), "s"(p), "s"(l) : "memory")
#define WAITVM(n) asm volatile("s_waitcnt vmcnt(%0)" :: "i"(n) : "memory")
template <int H>
__device__ __forceinline__ void k1_ldw(const K1Ld& st) {
  const unsigned l = st.lds0 + K1_W0 + (H % 5) * K1_WS + st.lw * K1_WG;
  int krow = H * 32 + st.rot * 64;
  if (krow >= 640) krow -= 640;
#pragma unroll
  for (int j = 0; j < 6; ++j) {
    unsigned long long p = rowaddr(st.gp[j], st.pitch[j], krow);
    if (j < 5) GLDS16(p, l + j * 1024);
    else if (st.lane < 32) GLDS16(p, l + 5120);
  }
}
template <int T, int J0, int N>
__device__ __forceinline__ void k1_ldx(const K1Ld& st) {
  int kc = (T + st.rot) * 64;
  if (kc >= 640) kc -= 640;
#pragma unroll
  for (int j = J0; j < J0 + N; ++j)
    GLDS16S(st.xvoff[j & 1], st.xb + j * 8 * D_MODEL + kc, st.lds0 + (T % 3) * K1_XS + (st.lw * 4 + j) * 1024);
}
template <int P>
__device__ __forceinline__ void k1_lphase(const K1Ld& st) {
  k1_ldx<P / 2 + 2, (P & 1) * 2, 2>(st);
  k1_ldw<P + 4>(st);
  WAITVM(14);
  BARRIER();
  if constexpr (P + 1 < 15) k1_lphase<P + 1>(st);
}

__device__ __forceinline__ void k1_loader(char* smem, const u16* __restrict__ x, const u16* __restrict__ wqkv,
                                          const u16* __restrict__ wg, const u16* __restrict__ wu,
                                          const u16* __restrict__ wd, const u16* __restrict__ wo,
                                          const u16* __restrict__ gw, char* __restrict__ Wdf, char* __restrict__ Wof,
                                          float* __restrict__ G, unsigned* __restrict__ flags) {
  const int tid = threadIdx.x, lane = tid & 63;
  const int lw = __builtin_amdgcn_readfirstlane(tid >> 6) - 8;
  int mt, nt;
  k1_tile(blockIdx.x, mt, nt);
  K1Ld st;
  st.lw = lw;
  st.lane = lane;
#define K1_ROT 0
  {
    const int idx = blockIdx.x >> 3;
    st.rot = K1_ROT == 0 ? 0 : K1_ROT == 1 ? ((idx & 7) * 5 + (idx >> 3) * 10 / 4 * 4) / 4 % 10 : K1_ROT == 2 ? (idx & 7) : (idx * 10) >> 5;
  }
  st.lds0 = (unsigned)(size_t)smem;
  {
    const int xr = lane >> 3;
    st.xb = x + (size_t)mt * 128 * D_MODEL;
    st.xvoff[0] = ((lw * 32 + xr) * D_MODEL + ((lane & 7) ^ (xr >> 1)) * 8) * 2;
    st.xvoff[1] = st.xvoff[0] ^ 64;
    const int nq = min(max(52 - nt * 10, 0), 10), ngu = 10 - nq;
    const int qcol = nt < 2 ? 1280 + 320 * nt : 320 * nt - 640;
    const int gcol = 16 * (nt * 10 + nq - 52);
#pragma unroll
    for (int j = 0; j < 6; ++j) {
      const int pp = j * 64 + lane;
      const int row = pp / 44, cp = min(pp - row * 44, 41);
      const int k = 8 * lw + (row & 7);
      const int cq = cp - 4 * nq;
      if (cq < 0 || (cq >= 2 * ngu && cq < 2 * ngu + 2)) {
        const int col = cq < 0 ? qcol + cp * 8 : 1024 + 16 * nt + 8 * (cq - 2 * ngu);
        st.gp[j] = (unsigned long long)(wqkv + (size_t)k * 1920 + col);
        st.pitch[j] = 2 * 1920;
      } else {
        const int half = cq >= 2 * ngu;
        const int col = gcol + 8 * (cq - half * (2 * ngu + 2));
        st.gp[j] = (unsigned long long)((half ? wu : wg) + (size_t)k * F_DIM + col);
        st.pitch[j] = 2 * F_DIM;
      }
    }
  }
  k1_ldx<0, 0, 4>(st);
  k1_ldw<0>(st);
  k1_ldw<1>(st);
  k1_ldx<1, 0, 4>(st);
  k1_ldw<2>(st);
  k1_ldw<3>(st);
  WAITVM(16);
  BARRIER();
  k1_lphase<0>(st);
  k1_ldx<9, 2, 2>(st);
  k1_ldw<19>(st);
  const int item = blockIdx.x * 4 + lw;
  const bool has_item = item < 740;
  const int isd = item < 540;
  const int it2 = isd ? item : (has_item ? item - 540 : 0);
  const int ks = it2 / 10, n64 = it2 - ks * 10;
  u32x4 tw[4];
  {
    const u16* src = (isd ? wd : wo) + (size_t)(ks * 32 + (lane >> 3)) * D_MODEL + n64 * 64 + (lane & 7) * 8;
#pragma unroll
    for (int i = 0; i < 4; ++i) GLOAD(tw[i], src + i * 8 * D_MODEL);
  }
  const int ch2 = 64 + (lane & 15);
  u32x4 xv[2], xv2[2], wv, wv2;
#pragma unroll
  for (int r = 0; r < 2; ++r) {
    const u16* xr = x + (size_t)(item * 2 + r) * D_MODEL;
    GLOAD(xv[r], xr + lane * 8);
    GLOAD(xv2[r], xr + ch2 * 8);
  }
  GLOAD(wv, gw + lane * 8);
  GLOAD(wv2, gw + ch2 * 8);
  WAITVM(10 + 8 + 6);
  BARRIER();
  WAITVM(10 + 6);
  BARRIER();
  WAITVM(10);
  BARRIER();
  asm volatile("s_waitcnt vmcnt(0)" : "+v"(tw[0]), "+v"(tw[1]), "+v"(tw[2]), "+v"(tw[3]), "+v"(xv[0]), "+v"(xv[1]),
               "+v"(xv2[0]), "+v"(xv2[1]), "+v"(wv), "+v"(wv2));
  if (has_item) {
    char* tb = smem + K1_TR + lw * 5632;
#pragma unroll
    for (int i = 0; i < 4; ++i) *(u32x4*)(tb + i * 1408 + (lane >> 3) * 160 + (lane & 7) * 16) = tw[i];
    const int g = lane >> 4, c = lane & 15;
    const char* rp = tb + g * 1408 + (c >> 2) * 160 + (c & 3) * 8;
    char* dst = (isd ? Wdf : Wof) + ((size_t)(ks * 40 + n64 * 4) * 64 + lane) * 16;
#pragma unroll
    for (int nb = 0; nb < 4; ++nb) {
      bfx8 f = tr_frag(rp + nb * 32, rp + nb * 32 + 640);
      *(bfx8*)(dst + nb * 1024) = f;
    }
  }
#pragma unroll
  for (int r = 0; r < 2; ++r) {
    float s = 0.f;
#pragma unroll
    for (int e = 0; e < 4; ++e) {
      s += rbf(__uint_as_float(xv[r][e] << 16) * __uint_as_float(wv[e] << 16));
      s += rbf(__uint_as_float(xv[r][e] & 0xffff0000u) * __uint_as_float(wv[e] & 0xffff0000u));
    }
    if (lane < 16) {
#pragma unroll
      for (int e = 0; e < 4; ++e) {
        s += rbf(__uint_as_float(xv2[r][e] << 16) * __uint_as_float(wv2[e] << 16));
        s += rbf(__uint_as_float(xv2[r][e] & 0xffff0000u) * __uint_as_float(wv2[e] & 0xffff0000u));
      }
    }
#pragma unroll
    for (int o = 32; o >= 1; o >>= 1) s += __shfl_xor(s, o);
    s = rbf(s);
    float gv = rbf(1.0f / (1.0f + __expf(-s)));
    if (lane == 0) G[item * 2 + r] = gv;
  }
  if (lw == 0 && lane == 0) flags[blockIdx.x] = 0u;
}

__global__ __launch_bounds__(768, 3) void k1_gemm(const u16* __restrict__ x, const u16* __restrict__ wqkv,
                                                  const u16* __restrict__ wg, const u16* __restrict__ wu,
                                                  const u16* __restrict__ wd, const u16* __restrict__ wo,
                                                  const u16* __restrict__ gw, char* __restrict__ Qf,
                                                  char* __restrict__ Vf, char* __restrict__ Hf,
                                                  char* __restrict__ Wdf, char* __restrict__ Wof,
                                                  float* __restrict__ G, unsigned* __restrict__ flags) {
  __shared__ __attribute__((aligned(16))) char smem[K1_LDS];
  const int w8 = __builtin_amdgcn_readfirstlane(threadIdx.x) >> 9;
  int mt, nt;
  k1_tile(blockIdx.x, mt, nt);
  if (w8) k1_loader(smem, x, wqkv, wg, wu, wd, wo, gw, Wdf, Wof, G, flags);
  else if (nt < 2) k1_compute<true>(smem, x, wqkv, wg, wu, Qf, Vf, Hf);
  else k1_compute<false>(smem, x, wqkv, wg, wu, Qf, Vf, Hf);
}

#define AT_K 15360
#define AT_LDS (AT_K + 12800)
__device__ __forceinline__ void attn_stage(const char* __restrict__ Kf, const char* __restrict__ Vf, unsigned lds,
                                           int h, int qb0, int lane) {
  const int slot0 = (20 * h) >> 3;
#pragma unroll
  for (int i = 0; i < 15; ++i) {
    const int p = i * 64 + lane;
    const int blk = p / 48, w = p - blk * 48;
    const int gb = max(qb0 - 8 + (blk >> 1), 0) * 2 + (blk & 1);
    GLDS16S(gb * 20480 + w * 16, Kf + slot0 * 256, lds + i * 1024);
  }
#pragma unroll
  for (int i = 0; i < 13; ++i) {
    const int p = i * 64 + lane;
    const int grp = p / 80, w = p - grp * 80;
    const int gg = max(qb0 - 8 + grp, 0);
    if (i < 12 || lane < 32) GLDS16S(gg * 1280 + w * 16, Vf + (size_t)h * 81920, lds + AT_K + i * 1024);
  }
}
template <bool FIRST>
__device__ __forceinline__ void attn_item(const u32x4 (&qv)[2], const float (&gq)[2], const char* kl, const char* vl,
                                          char* __restrict__ Of, int h, int qb, int lane) {
  asm volatile("" : "+v"(lane));
  const int g = lane >> 4, c = lane & 15;
  bfx8 qf[2];
#pragma unroll
  for (int t = 0; t < 2; ++t) {
    u32x4 v = qv[t];
    if (g == 3 || (g == 2 && !(h & 1))) { v[2] = 0; v[3] = 0; }
    if (g == 3 || (g == 0 && (h & 1))) { v[0] = 0; v[1] = 0; }
    qf[t] = __builtin_bit_cast(bfx8, v);
  }
  f32x4 s[9][2][2];
  const char* kp = kl + min(g, 2) * 256 + c * 16;
  bfx8 kf[18];
#pragma unroll
  for (int j = 0; j < 18; ++j) kf[j] = *(const bfx8*)(kp + j * 768);
  __builtin_amdgcn_sched_barrier(0);
#pragma unroll
  for (int j = 0; j < 9; ++j) {
#pragma unroll
    for (int a = 0; a < 2; ++a) {
#pragma unroll
      for (int t = 0; t < 2; ++t) s[j][a][t] = mfma16(kf[j * 2 + a], qf[t], f32x4{0, 0, 0, 0});
    }
  }
  __builtin_amdgcn_sched_barrier(0);
  if constexpr (FIRST) WAITVM(0);
  u32x4 v0[9], v1[9];
#pragma unroll
  for (int j = 0; j < 9; ++j) {
    v0[j] = *(const u32x4*)(vl + j * 1280 + lane * 16);
    v1[j] = *(const u32x4*)(vl + j * 1280 + 1024 + g * 64 + (c & 3) * 16);
  }
  __builtin_amdgcn_sched_barrier(0);
  const float NEG = -1e30f;
#pragma unroll
  for (int a = 0; a < 2; ++a)
#pragma unroll
    for (int t = 0; t < 2; ++t)
#pragma unroll
      for (int r = 0; r < 4; ++r) {
        int ko = 8 * g + 4 * a + r, qo = 16 * t + c;
        if (!(ko > qo)) s[0][a][t][r] = NEG;
        if (!(ko <= qo)) s[8][a][t][r] = NEG;
      }
  if (qb < 8) {
#pragma unroll
    for (int j = 0; j < 8; ++j)
      if (j < 8 - qb) {
#pragma unroll
        for (int a = 0; a < 2; ++a)
#pragma unroll
          for (int t = 0; t < 2; ++t) s[j][a][t] = f32x4{NEG, NEG, NEG, NEG};
      }
  }
  const float cs = 0.22360679774997896f * 1.4426950408889634f;
  bfx8 pf[9][2];
  float mx[2][4];
#pragma unroll
  for (int t = 0; t < 2; ++t)
#pragma unroll
    for (int r = 0; r < 4; ++r) mx[t][r] = fmaxf(s[4][0][t][r], s[4][1][t][r]);
#pragma unroll
  for (int j = 0; j < 9; ++j)
    if (j != 4) {
#pragma unroll
      for (int t = 0; t < 2; ++t)
#pragma unroll
        for (int r = 0; r < 4; ++r) mx[t][r] = fmaxf(fmaxf(mx[t][r], s[j][0][t][r]), s[j][1][t][r]);
    }
  float mm[2];
#pragma unroll
  for (int t = 0; t < 2; ++t) mm[t] = fmaxf(fmaxf(mx[t][0], mx[t][1]), fmaxf(mx[t][2], mx[t][3]));
#pragma unroll
  for (int t = 0; t < 2; ++t) mm[t] = fmaxf(mm[t], __shfl_xor(mm[t], 16));
#pragma unroll
  for (int t = 0; t < 2; ++t) mm[t] = fmaxf(mm[t], __shfl_xor(mm[t], 32));
#pragma unroll
  for (int t = 0; t < 2; ++t) {
    const float mc = mm[t] * cs;
#pragma unroll
    for (int j = 0; j < 9; ++j) {
      u32x4 pk;
#pragma unroll
      for (int a = 0; a < 2; ++a) {
        float p0 = __builtin_amdgcn_exp2f(fmaf(s[j][a][t][0], cs, -mc));
        float p1 = __builtin_amdgcn_exp2f(fmaf(s[j][a][t][1], cs, -mc));
        float p2 = __builtin_amdgcn_exp2f(fmaf(s[j][a][t][2], cs, -mc));
        float p3 = __builtin_amdgcn_exp2f(fmaf(s[j][a][t][3], cs, -mc));
        pk[2 * a] = pack2(p0, p1);
        pk[2 * a + 1] = pack2(p2, p3);
      }
      pf[j][t] = __builtin_bit_cast(bfx8, pk);
    }
  }
  f32x4 o0[2] = {f32x4{0, 0, 0, 0}, f32x4{0, 0, 0, 0}}, o1[2] = {f32x4{0, 0, 0, 0}, f32x4{0, 0, 0, 0}};
#pragma unroll
  for (int j = 0; j < 9; ++j) {
    u32x4 w1 = v1[j];
    if (c == 4) w1 = u32x4{0x3f803f80u, 0x3f803f80u, 0x3f803f80u, 0x3f803f80u};
    bfx8 vf0 = __builtin_bit_cast(bfx8, v0[j]), vf1 = __builtin_bit_cast(bfx8, w1);
#pragma unroll
    for (int t = 0; t < 2; ++t) {
      o0[t] = mfma16(vf0, pf[j][t], o0[t]);
      o1[t] = mfma16(vf1, pf[j][t], o1[t]);
    }
  }
#pragma unroll
  for (int t = 0; t < 2; ++t) {
    float l = __shfl(o1[t][0], 16 + c);
    float inv = gq[t] / l;
    char* dst = Of + (size_t)(qb * 2 + t) * 20480 + c * 16;
    const int k0 = h * 20 + 4 * g;
    u32x2 v = {pack2(o0[t][0] * inv, o0[t][1] * inv), pack2(o0[t][2] * inv, o0[t][3] * inv)};
    st8b(dst + (k0 >> 3) * 256 + ((k0 >> 2) & 1) * 8, v);
    if (g == 0) {
      const int k1 = h * 20 + 16;
      u32x2 w = {pack2(o1[t][0] * inv, o1[t][1] * inv), pack2(o1[t][2] * inv, o1[t][3] * inv)};
      st8b(dst + (k1 >> 3) * 256 + ((k1 >> 2) & 1) * 8, w);
    }
  }
}

#define GLOADSC(r, o, p) asm volatile("global_load_dwordx4 %0, %1, %2 sc1" : "=v"(r) : "v"(o), "s"(p))
struct G2Frag { u32x4 a[4], w[5]; };
__device__ __forceinline__ void g2_issue_w(G2Frag& f, const char* Wb, int ks, int voff) {
  const char* wp = Wb + (size_t)ks * 40960 + 2048;
  GLOADSO(f.w[0], voff, wp, -2048);
  GLOADSO(f.w[1], voff, wp, -1024);
  GLOADSO(f.w[2], voff, wp, 0);
  GLOADSO(f.w[3], voff, wp, 1024);
  GLOADSO(f.w[4], voff, wp, 2048);
}
__device__ __forceinline__ void g2_issue_h(G2Frag& f, const char* Ab, int ks, int voff) {
  const char* ap = Ab + (size_t)ks * 1024;
#pragma unroll
  for (int i = 0; i < 4; ++i) GLOADS(f.a[i], voff, ap + (size_t)i * 54 * 1024);
}
__device__ __forceinline__ void g2_issue_o(G2Frag& f, const char* Ab, int ks, int voff) {
  const char* ap = Ab + (size_t)ks * 1024;
#pragma unroll
  for (int i = 0; i < 4; ++i) GLOADSC(f.a[i], voff, ap + (size_t)i * 20 * 1024);
}
template <int N>
__device__ __forceinline__ void g2_wait(G2Frag& f) {
  asm volatile("s_waitcnt vmcnt(%9)"
               : "+v"(f.a[0]), "+v"(f.a[1]), "+v"(f.a[2]), "+v"(f.a[3]), "+v"(f.w[0]), "+v"(f.w[1]), "+v"(f.w[2]),
                 "+v"(f.w[3]), "+v"(f.w[4])
               : "i"(N));
}
struct G2Ctx {
  const char *Hb, *Wdb, *Ob, *Wob;
  const unsigned* fl;
  const float* gp;
  int wave, voff;
  float omg[4];
  unsigned fv;
};
template <int S>
__device__ __forceinline__ void g2_steps(G2Frag (&f)[4], f32x4 (&acc)[5][4], G2Ctx& cx) {
  constexpr int W = S <= 10 ? 27 : S == 11 ? 23 : S == 12 ? 19 : S == 13 ? 20 : S == 14 ? 12 : S == 15 ? 17 : S == 16 ? 13 : S == 17 ? 9 : 0;
  G2Frag& cur = f[S & 3];
  g2_wait<W>(cur);
  if (S != 13 || cx.wave < 2) {
#pragma unroll
    for (int u = 0; u < 5; ++u)
#pragma unroll
      for (int i = 0; i < 4; ++i)
        acc[u][i] = mfma16(__builtin_bit_cast(bfx8, cur.w[u]), __builtin_bit_cast(bfx8, cur.a[i]), acc[u][i]);
  }
  __builtin_amdgcn_sched_barrier(0);
  if constexpr (S < 10) {
    const int ks = min((S + 4) * 4 + cx.wave, 53);
    g2_issue_h(cur, cx.Hb, ks, cx.voff);
    g2_issue_w(cur, cx.Wdb, ks, cx.voff);
  } else if constexpr (S < 14) {
    if constexpr (S == 12) {
      const int go = (cx.voff >> 2) & 60;
#pragma unroll
      for (int i = 0; i < 4; ++i)
        asm volatile("global_load_dword %0, %1, %2 offset:%3" : "=v"(cx.omg[i]) : "v"(go), "s"(cx.gp), "i"(i * 64));
      asm volatile("global_load_dword %0, %1, %2 sc1" : "=v"(cx.fv) : "v"(go & 28), "s"(cx.fl));
    }
    g2_issue_w(cur, cx.Wob, (S - 10) * 4 + cx.wave, cx.voff);
    if constexpr (S == 13) {
      asm volatile("s_waitcnt vmcnt(10)" : "+v"(cx.fv), "+v"(cx.omg[0]), "+v"(cx.omg[1]), "+v"(cx.omg[2]), "+v"(cx.omg[3]));
#pragma unroll 1
      for (int it = 0; it < (1 << 20) && __builtin_amdgcn_ballot_w64(cx.fv != 1u) != 0; ++it) {
        __builtin_amdgcn_s_sleep(2);
        asm volatile("global_load_dword %0, %1, %2 sc1\n\ts_waitcnt vmcnt(0)" : "=v"(cx.fv) : "v"((cx.voff >> 2) & 28), "s"(cx.fl) : "memory");
      }
      asm volatile("" ::: "memory");
#pragma unroll
      for (int j = 0; j < 4; ++j) g2_issue_o(f[(14 + j) & 3], cx.Ob, j * 4 + cx.wave, cx.voff);
#pragma unroll
      for (int u = 0; u < 5; ++u)
#pragma unroll
        for (int i = 0; i < 4; ++i) acc[u][i] *= rbf(1.0f - cx.omg[i]);
      __builtin_amdgcn_sched_barrier(0);
    }
  } else if constexpr (S == 14) {
    g2_issue_o(cur, cx.Ob, 16 + cx.wave, cx.voff);
    g2_issue_w(cur, cx.Wob, 16 + cx.wave, cx.voff);
  }
  if constexpr (S + 1 < 19) g2_steps<S + 1>(f, acc, cx);
}
__device__ __forceinline__ void g2_tile(int bid, int& mt, int& nt) {
  const int xcd = bid & 7, idx = bid >> 3;
  mt = (xcd >> 1) * 8 + (idx >> 2);
  nt = (xcd & 1) * 4 + (idx & 3);
}

__global__ __launch_bounds__(512, 2) void k2_mix(const char* __restrict__ Qf, const char* __restrict__ Kf,
                                                 const char* __restrict__ Vf, char* Of,
                                                 const char* __restrict__ Hf, const char* __restrict__ Wdf,
                                                 const char* __restrict__ Wof, const float* __restrict__ G,
                                                 unsigned* flags, u16* __restrict__ out) {
  __shared__ __attribute__((aligned(16))) char smem[4 * AT_LDS];
  __shared__ unsigned k2_cnt;
  const int tid = threadIdx.x, lane = tid & 63;
  const int wave = __builtin_amdgcn_readfirstlane(tid >> 6) ^ 4;
  const int bid = blockIdx.x;
  int mt, nt;
  g2_tile(bid, mt, nt);
  if (wave >= 4) {
    float gq[4];
    {
      const float* gp = G + mt * 64 + (lane & 15);
#pragma unroll
      for (int i = 0; i < 4; ++i) GLOAD1(gq[i], gp + i * 16);
    }
    const int h = nt * 4 + (wave - 4);
    const int qb0 = mt * 2;
    if (wave == 4) ST(0, 0);
    char* al = smem + (wave - 4) * AT_LDS;
    u32x4 qv[2][2];
    {
      const char* qp = Qf + (size_t)(qb0 * 2) * 20480 + (((20 * h) >> 3) + min(lane >> 4, 2)) * 256 + (lane & 15) * 16;
#pragma unroll
      for (int i = 0; i < 4; ++i) GLOAD(qv[i >> 1][i & 1], qp + i * 20480);
    }
    attn_stage(Kf, Vf, (unsigned)(size_t)al, h, qb0, lane);
    if (tid == 0) k2_cnt = 0u;
    BARRIER();
    asm volatile("s_waitcnt vmcnt(13)" : "+v"(qv[0][0]), "+v"(qv[0][1]), "+v"(qv[1][0]), "+v"(qv[1][1]), "+v"(gq[0]),
                 "+v"(gq[1]), "+v"(gq[2]), "+v"(gq[3]) :: "memory");
    if (wave == 4) ST(0, 1);
    {
      const float g0[2] = {gq[0], gq[1]}, g1[2] = {gq[2], gq[3]};
      attn_item<true>(qv[0], g0, al, al + AT_K, Of, h, qb0, lane);
      if (wave == 4) ST(0, 2);
      attn_item<false>(qv[1], g1, al + 1536, al + AT_K + 1280, Of, h, qb0 + 1, lane);
    }
    if (wave == 4) ST(0, 3);
    WAITVM(0);
    if (lane == 0) {
      unsigned old = __hip_atomic_fetch_add(&k2_cnt, 1u, __ATOMIC_RELAXED, __HIP_MEMORY_SCOPE_WORKGROUP);
      if (old == 3u) __hip_atomic_store(flags + mt * 8 + nt, 1u, __ATOMIC_RELAXED, __HIP_MEMORY_SCOPE_AGENT);
    }
    if (wave == 4) ST(0, 4);
    BARRIER();
    return;
  }
  f32x4 acc[5][4];
#pragma unroll
  for (int u = 0; u < 5; ++u)
#pragma unroll
    for (int i = 0; i < 4; ++i) acc[u][i] = f32x4{0, 0, 0, 0};
  G2Ctx cx;
  cx.Hb = Hf + (size_t)mt * 4 * 54 * 1024;
  cx.Wdb = Wdf + (size_t)nt * 5120;
  cx.Ob = Of + (size_t)mt * 4 * 20 * 1024;
  cx.Wob = Wof + (size_t)nt * 5120;
  cx.fl = flags + mt * 8;
  cx.gp = G + mt * 64;
  cx.wave = wave;
  cx.voff = lane * 16;
  BARRIER();
  if (wave == 0) ST(1, 0);
  G2Frag f[4];
#pragma unroll
  for (int s = 0; s < 4; ++s) {
    g2_issue_h(f[s], cx.Hb, s * 4 + wave, cx.voff);
    g2_issue_w(f[s], cx.Wdb, s * 4 + wave, cx.voff);
  }
  g2_steps<0>(f, acc, cx);
  if (wave == 0) ST(1, 20);
  f32x4* red = (f32x4*)smem;
  BARRIER();
#pragma unroll
  for (int u = 0; u < 5; ++u)
#pragma unroll
    for (int i = 0; i < 4; ++i) red[((wave * 4 + i) * 5 + u) * 64 + lane] = acc[u][i];
  BARRIER();
  const int tok = mt * 64 + wave * 16 + (lane & 15);
#pragma unroll
  for (int u = 0; u < 5; ++u) {
    f32x4 v = red[((0 * 4 + wave) * 5 + u) * 64 + lane];
#pragma unroll
    for (int w = 1; w < 4; ++w) v += red[((w * 4 + wave) * 5 + u) * 64 + lane];
    u32x2 pv = {pack2(v[0], v[1]), pack2(v[2], v[3])};
    *(u32x2*)(out + (size_t)tok * D_MODEL + nt * 80 + u * 16 + 4 * (lane >> 4)) = pv;
  }
  if (wave == 0) ST(1, 21);
}

extern "C" void kernel_launch(void* const* d_in, const int* in_sizes, int n_in,
                              void* d_out, int out_size, void* d_ws, size_t ws_size,
                              hipStream_t stream) {
  const u16* x = (const u16*)d_in[0];
  const u16* wqkv = (const u16*)d_in[1];
  const u16* wo = (const u16*)d_in[2];
  const u16* wg = (const u16*)d_in[4];
  const u16* wu = (const u16*)d_in[5];
  const u16* wd = (const u16*)d_in[6];
  const u16* gw = (const u16*)d_in[7];
  char* ws = (char*)d_ws;
  const size_t QB = (size_t)S_LEN * D_MODEL * 2;
  char* Qf = ws;
  char* Kf = Qf + QB;
  char* Vf = Kf + QB;
  char* Hf = ws + (12u << 20);
  char* Of = ws + (20u << 20);
  char* Wdf = ws + (28u << 20);
  char* Wof = ws + (31u << 20);
  float* G = (float*)(ws + (32u << 20));
  unsigned* flags = (unsigned*)(ws + (33u << 20));
  u16* out = (u16*)d_out;

  k1_gemm<<<256, 768, 0, stream>>>(x, wqkv, wg, wu, wd, wo, gw, Qf, Vf, Hf, Wdf, Wof, G, flags);
  k2_mix<<<256, 512, 0, stream>>>(Qf, Kf, Vf, Of, Hf, Wdf, Wof, G, flags, out);
}
```

```cpp
#include <hip/hip_runtime.h>
#include <hip/hip_bf16.h>
#include <stdint.h>

typedef __attribute__((ext_vector_type(8))) __bf16 bfx8;
typedef __attribute__((ext_vector_type(2))) __bf16 bfx2;
typedef __attribute__((ext_vector_type(2))) float f32x2;
typedef __attribute__((ext_vector_type(4))) short s16x4;
typedef __attribute__((ext_vector_type(8))) short s16x8;
typedef __attribute__((ext_vector_type(4))) float f32x4;
typedef __attribute__((ext_vector_type(16))) float f32x16;
typedef __attribute__((ext_vector_type(2))) unsigned u32x2;
typedef __attribute__((ext_vector_type(4))) unsigned u32x4;
typedef __attribute__((ext_vector_type(4), aligned(8))) unsigned u32x4a8;
typedef __attribute__((address_space(3))) s16x4* lds_s16x4_ptr;
typedef unsigned short u16;

#define S_LEN 2048
#define D_MODEL 640
#define F_DIM 1728

__device__ __forceinline__ float bf2f(u16 v) { return __uint_as_float(((unsigned)v) << 16); }
__device__ __forceinline__ u16 f2bf(float f) {
  __hip_bfloat16 b = __float2bfloat16(f);
  return __builtin_bit_cast(u16, b);
}
__device__ __forceinline__ float rbf(float f) { return bf2f(f2bf(f)); }
__device__ __forceinline__ unsigned pack2(float lo, float hi) {
  bfx2 r = __builtin_convertvector(f32x2{lo, hi}, bfx2);
  return __builtin_bit_cast(unsigned, r);
}
__device__ __forceinline__ f32x4 mfma16(bfx8 a, bfx8 b, f32x4 c) {
  return __builtin_amdgcn_mfma_f32_16x16x32_bf16(a, b, c, 0, 0, 0);
}
__device__ __forceinline__ f32x16 mfma32(bfx8 a, bfx8 b, f32x16 c) {
  return __builtin_amdgcn_mfma_f32_32x32x16_bf16(a, b, c, 0, 0, 0);
}
__device__ __forceinline__ bfx8 tr_frag(const char* p0, const char* p1) {
  s16x4 a = __builtin_amdgcn_ds_read_tr16_b64_v4i16((lds_s16x4_ptr)p0);
  s16x4 b = __builtin_amdgcn_ds_read_tr16_b64_v4i16((lds_s16x4_ptr)p1);
  s16x8 v = {a[0], a[1], a[2], a[3], b[0], b[1], b[2], b[3]};
  return __builtin_bit_cast(bfx8, v);
}
#define STPOL 2
#define STPOLB 2
template <int POL>
__device__ __forceinline__ void st8p(char* p, u32x2 v) {
  if constexpr (POL == 0) *(u32x2*)p = v;
  else if constexpr (POL == 1) __builtin_nontemporal_store(v, (u32x2*)p);
  else if constexpr (POL == 2) asm volatile("global_store_dwordx2 %0, %1, off sc1" :: "v"(p), "v"(v) : "memory");
  else asm volatile("global_store_dwordx2 %0, %1, off sc0 sc1" :: "v"(p), "v"(v) : "memory");
}
__device__ __forceinline__ void st8(char* p, u32x2 v) { st8p<STPOL>(p, v); }
__device__ __forceinline__ void st8b(char* p, u32x2 v) { st8p<STPOLB>(p, v); }
#define GLOAD(r, p) asm volatile("global_load_dwordx4 %0, %1, off" : "=v"(r) : "v"(p))
#define GLOADS(r, o, p) asm volatile("global_load_dwordx4 %0, %1, %2" : "=v"(r) : "v"(o), "s"(p))
#define GLOADSO(r, o, p, imm) asm volatile("global_load_dwordx4 %0, %1, %2 offset:%3" : "=v"(r) : "v"(o), "s"(p), "i"(imm))
#define GLOAD1(r, p) asm volatile("global_load_dword %0, %1, off" : "=v"(r) : "v"(p))
#define WAITV(n, r) asm volatile("s_waitcnt vmcnt(%1)" : "+v"(r) : "i"(n))
__device__ __forceinline__ void bump(const u16*& p, int stride) {
  unsigned lo = (unsigned)(size_t)p, hi = (unsigned)((size_t)p >> 32);
  asm volatile("v_add_co_u32 %0, vcc, %0, %2\n\tv_addc_co_u32 %1, vcc, 0, %1, vcc" : "+v"(lo), "+v"(hi) : "v"(stride) : "vcc");
  p = (const u16*)(((size_t)hi << 32) | lo);
}
#define BARRIER0() asm volatile("s_barrier" ::: "memory")
#define BARRIER() asm volatile("s_waitcnt lgkmcnt(0)\n\ts_barrier" ::: "memory")
#define ST(role, i)

struct Ws {
  char *Qf, *Kf, *Vf, *Hf, *Of, *Wdf, *Wof, *Bb;
  float* G;
};

#define K1_XS 16384
#define K1_WP 704
#define K1_WG (8 * K1_WP)
#define K1_WS (4 * K1_WG)
#define K1_W0 (3 * K1_XS)
#define K1_TR K1_W0
#define K1_LDS (K1_W0 + 5 * K1_WS + 256)

__device__ __forceinline__ void k1_tile(int bid, int& mt, int& nt) {
  const int xcd = bid & 7, idx = bid >> 3;
  mt = (xcd >> 2) * 8 + (idx & 7);
  nt = (xcd & 3) * 4 + (idx >> 3);
}

#define CST(i)

#define K1_PD 4
struct K1C {
  f32x16 acc[5];
  f32x4 accs;
  int wa[6], wah[6];
  int xa[4], xsa[2];
  bfx8 wf[K1_PD + 1], xf[2], xs;
};
template <int F>
__device__ __forceinline__ bfx8 k1_wfrag(const char* smem, const K1C& st) {
  constexpr int P = F / 11, i = F % 11, sl = P % 5;
  constexpr int u = i < 10 ? i % 5 : 5;
  constexpr int off = (sl < 3 ? sl : sl - 3) * K1_WS + (i >= 5 && i < 10 ? 16 * K1_WP : 0);
  const char* b = smem + (sl < 3 ? st.wa[u] : st.wah[u]);
  return tr_frag(b + off, b + off + 4 * K1_WP);
}
template <bool UNSW, int F>
__device__ __forceinline__ void k1_steps(const char* smem, K1C& st, int wave, int lane) {
  constexpr int P = F / 11, i = F % 11;
  if constexpr (F + K1_PD < 220) st.wf[(F + K1_PD) % (K1_PD + 1)] = k1_wfrag<F + K1_PD>(smem, st);
  if constexpr (i == 0) st.xf[1] = *(const bfx8*)(smem + st.xa[(P & 1) * 2 + 1] + ((P >> 1) % 3) * K1_XS);
  if constexpr (i == 5 && P < 19)
    st.xf[0] = *(const bfx8*)(smem + st.xa[((P + 1) & 1) * 2] + (((P + 1) >> 1) % 3) * K1_XS);
  if constexpr (i == 6) st.xs = *(const bfx8*)(smem + st.xsa[P & 1] + ((P >> 1) % 3) * K1_XS);
  if constexpr (i < 10) {
    if constexpr (UNSW) st.acc[i % 5] = mfma32(st.xf[i / 5], st.wf[F % (K1_PD + 1)], st.acc[i % 5]);
    else st.acc[i % 5] = mfma32(st.wf[F % (K1_PD + 1)], st.xf[i / 5], st.acc[i % 5]);
  } else {
    st.accs = mfma16(st.wf[F % (K1_PD + 1)], st.xs, st.accs);
  }
  __builtin_amdgcn_sched_barrier(0);
  if constexpr (i == 10 && P < 18) {
    if constexpr (P & 1) { CST(P + 1); }
    BARRIER0();
    if constexpr (P & 1) { CST(P + 2); }
  }
  if constexpr (F + 1 < 220) k1_steps<UNSW, F + 1>(smem, st, wave, lane);
}

__device__ __forceinline__ float silu_mul(float g, float u) {
  return g * u * __builtin_amdgcn_rcpf(1.0f + __builtin_amdgcn_exp2f(g * -1.4426950408889634f));
}

template <bool UNSW>
__device__ __forceinline__ void k1_compute(char* smem, const u16* __restrict__ x, const u16* __restrict__ wqkv,
                                           const u16* __restrict__ wg, const u16* __restrict__ wu,
                                           char* __restrict__ Qf, char* __restrict__ Vf, char* __restrict__ Hf) {
  const int tid = threadIdx.x, lane = tid & 63;
  const int wave = __builtin_amdgcn_readfirstlane(tid >> 6);
  int mt, nt;
  k1_tile(blockIdx.x, mt, nt);
  const int wm = wave & 3, wn = wave >> 2;
  const int g = lane >> 4, c = lane & 15, hh = lane >> 5;
  const int nq = min(max(52 - nt * 10, 0), 10), ngu = 10 - nq;
  unsigned pf[2];
  {
    const int idx = blockIdx.x >> 3;
    const int qcol = nt < 2 ? 1280 + 320 * nt : 320 * nt - 640;
    const int gcol = 16 * (nt * 10 + nq - 52);
#pragma unroll
    for (int i = 0; i < 2; ++i) {
      const int e = i * 512 + tid;
      const u16* p;
      if (e < 720) {
        const int r = e / 9, q = e - r * 9;
        const int k = r * 8 + (idx & 7);
        if (q == 8) p = wqkv + (size_t)k * 1920 + 1024 + 16 * nt;
        else if (ngu == 0) p = wqkv + (size_t)k * 1920 + qcol + min(q * 64, 312);
        else if (q == 0 && nq > 0) p = wqkv + (size_t)k * 1920 + qcol;
        else p = ((q & 4) ? wu : wg) + (size_t)k * F_DIM + gcol + min((q & 3) * 64, ngu * 16 - 8);
      } else {
        const int f = min(e - 720, 319);
        const int kt = f >> 5, r = (f & 31) * 4 + (idx >> 3);
        p = x + (size_t)(mt * 128 + r) * D_MODEL + kt * 64;
      }
      pf[i] = 0;
    }
  }
  K1C st;
  {
    const int xrow = wm * 32 + (lane & 31);
    const int x0 = xrow * 128 + ((hh ^ ((xrow >> 1) & 7)) << 4);
#pragma unroll
    for (int i = 0; i < 4; ++i) st.xa[i] = x0 ^ (i * 32);
    const int xrs = wm * 32 + wn * 16 + c;
    const int xs0 = xrs * 128 + ((g ^ ((xrs >> 1) & 7)) << 4);
    st.xsa[0] = xs0;
    st.xsa[1] = xs0 ^ 64;
    const int rowp = K1_W0 + (8 * hh + (c >> 2)) * K1_WP + (c & 3) * 8;
#pragma unroll
    for (int u = 0; u < 5; ++u) {
      const int b = wn * 5 + u;
      st.wa[u] = rowp + (b < nq ? b * 64 + (g & 1) * 32 : nq * 32 + b * 32 + ((g & 1) ? ngu * 32 + 32 : 0));
    }
    st.wa[5] = K1_W0 + (8 * g + (c >> 2)) * K1_WP + (c & 3) * 8 + nq * 64 + ngu * 32;
#pragma unroll
    for (int u = 0; u < 6; ++u) {
      st.wah[u] = st.wa[u] + 3 * K1_WS;
      asm volatile("" : "+v"(st.wa[u]), "+v"(st.wah[u]));
    }
#pragma unroll
    for (int i = 0; i < 4; ++i) asm volatile("" : "+v"(st.xa[i]));
    asm volatile("" : "+v"(st.xsa[0]), "+v"(st.xsa[1]));
  }
#pragma unroll
  for (int u = 0; u < 5; ++u)
#pragma unroll
    for (int r = 0; r < 16; ++r) st.acc[u][r] = 0.f;
  st.accs = f32x4{0, 0, 0, 0};
  CST(0);
  BARRIER();
  CST(1);
  st.wf[0] = k1_wfrag<0>(smem, st);
  st.wf[1] = k1_wfrag<1>(smem, st);
  if constexpr (K1_PD > 2) st.wf[2] = k1_wfrag<2>(smem, st);
  if constexpr (K1_PD > 3) st.wf[3] = k1_wfrag<3>(smem, st);
  if constexpr (K1_PD > 4) st.wf[4] = k1_wfrag<4>(smem, st);
  st.xf[0] = *(const bfx8*)(smem + st.xa[0]);
  k1_steps<UNSW, 0>(smem, st, wave, lane);
  CST(20);
  asm volatile("s_waitcnt vmcnt(0)" : "+v"(pf[0]), "+v"(pf[1]));
  const int m0 = mt * 128 + wm * 32;
  {
    const int ts = m0 + wn * 16 + c;
    const int cc = 4 * ((ts >> 3) & 3) + (ts & 3), blk = (ts >> 5) * 2 + ((ts >> 2) & 1);
    u32x2 v = {pack2(st.accs[0], st.accs[1]), pack2(st.accs[2], st.accs[3])};
    st8(Qf + (unsigned)((128 + blk) * 20480 + (48 + 2 * nt + (g >> 1)) * 256 + cc * 16 + (g & 1) * 8), v);
  }
  const int t = m0 + (lane & 31);
  const int tq = (t >> 4) * 20480 + (t & 15) * 16 + hh * 8;
  const int tk = (128 + (t >> 5) * 2 + ((t >> 2) & 1)) * 20480 + (4 * ((t >> 3) & 3) + (t & 3)) * 16 + hh * 8;
#pragma unroll
  for (int u = 0; u < 5; ++u) {
    const int b = wn * 5 + u, B = nt * 10 + b;
    const f32x16 a = st.acc[u];
    if constexpr (UNSW) {
      const int col = 32 * B + (lane & 31);
      const int h = col / 20, d = col - h * 20;
      char* dst = Vf + (unsigned)((h * 64 + (m0 >> 5)) * 1280 + (d < 16 ? d * 16 : 1024 + (d - 16) * 16) + hh * 8);
      const int js = d < 16 ? 256 : 64;
#pragma unroll
      for (int j = 0; j < 4; ++j) {
        u32x2 v = {pack2(a[4 * j], a[4 * j + 1]), pack2(a[4 * j + 2], a[4 * j + 3])};
        st8(dst + j * js, v);
      }
    } else {
      if (b < nq) {
        char* dst = Qf + (unsigned)(B >= 40 ? tk + (B - 40) * 1024 : tq + (B - 20) * 1024);
#pragma unroll
        for (int j = 0; j < 4; ++j) {
          u32x2 v = {pack2(a[4 * j], a[4 * j + 1]), pack2(a[4 * j + 2], a[4 * j + 3])};
          st8(dst + j * 256, v);
        }
      } else {
        char* dst = Hf + (unsigned)(((t >> 4) * 216 + 2 * (B - 52)) * 256 + (t & 15) * 16 + hh * 8);
#pragma unroll
        for (int j = 0; j < 2; ++j) {
          u32x2 v = {pack2(silu_mul(a[4 * j], a[8 + 4 * j]), silu_mul(a[4 * j + 1], a[9 + 4 * j])),
                     pack2(silu_mul(a[4 * j + 2], a[10 + 4 * j]), silu_mul(a[4 * j + 3], a[11 + 4 * j]))};
          st8(dst + j * 256, v);
        }
      }
    }
  }
  CST(21);
}

struct K1Ld {
  unsigned long long gp[6];
  int pitch[6];
  const u16* xb;
  int xvoff[2];
  unsigned lds0;
  int lw, lane, rot;
};
__device__ __forceinline__ unsigned long long rowaddr(unsigned long long p, int pitch, int rows) {
  unsigned long long r;
  asm volatile("v_mad_u64_u32 %0, vcc, %1, %2, %3" : "=v"(r) : "v"(pitch), "s"(rows), "v"(p) : "vcc");
  return r;
}
#define GLDS16(p, l) asm volatile("s_mov_b32 m0, %1\n\ts_nop 0\n\tglobal_load_lds_dwordx4 %0, off" :: "v"(p), "s"(l) : "memory")
#define GLDS16S(o, p, l) asm volatile("s_mov_b32 m0, %2\n\ts_nop 0\n\tglobal_load_lds_dwordx4 %0, %1" :: "v"(o), "s"(p), "s"(l) : "memory")
#define WAITVM(n) asm volatile("s_waitcnt vmcnt(%0)" :: "i"(n) : "memory")
template <int H>
__device__ __forceinline__ void k1_ldw(const K1Ld& st) {
  const unsigned l = st.lds0 + K1_W0 + (H % 5) * K1_WS + st.lw * K1_WG;
  int krow = H * 32 + st.rot * 64;
  if (krow >= 640) krow -= 640;
#pragma unroll
  for (int j = 0; j < 6; ++j) {
    unsigned long long p = rowaddr(st.gp[j], st.pitch[j], krow);
    if (j < 5) GLDS16(p, l + j * 1024);
    else if (st.lane < 32) GLDS16(p, l + 5120);
  }
}
template <int T, int J0, int N>
__device__ __forceinline__ void k1_ldx(const K1Ld& st) {
  int kc = (T + st.rot) * 64;
  if (kc >= 640) kc -= 640;
#pragma unroll
  for (int j = J0; j < J0 + N; ++j)
    GLDS16S(st.xvoff[j & 1], st.xb + j * 8 * D_MODEL + kc, st.lds0 + (T % 3) * K1_XS + (st.lw * 4 + j) * 1024);
}
template <int P>
__device__ __forceinline__ void k1_lphase(const K1Ld& st) {
  k1_ldx<P / 2 + 2, (P & 1) * 2, 2>(st);
  k1_ldw<P + 4>(st);
  WAITVM(14);
  BARRIER();
  if constexpr (P + 1 < 15) k1_lphase<P + 1>(st);
}

__device__ __forceinline__ void k1_loader(char* smem, const u16* __restrict__ x, const u16* __restrict__ wqkv,
                                          const u16* __restrict__ wg, const u16* __restrict__ wu,
                                          const u16* __restrict__ wd, const u16* __restrict__ wo,
                                          const u16* __restrict__ gw, char* __restrict__ Wdf, char* __restrict__ Wof,
                                          float* __restrict__ G, unsigned* __restrict__ flags) {
  const int tid = threadIdx.x, lane = tid & 63;
  const int lw = __builtin_amdgcn_readfirstlane(tid >> 6) - 8;
  int mt, nt;
  k1_tile(blockIdx.x, mt, nt);
  K1Ld st;
  st.lw = lw;
  st.lane = lane;
#define K1_ROT 0
  {
    const int idx = blockIdx.x >> 3;
    st.rot = K1_ROT == 0 ? 0 : K1_ROT == 1 ? ((idx & 7) * 5 + (idx >> 3) * 10 / 4 * 4) / 4 % 10 : K1_ROT == 2 ? (idx & 7) : (idx * 10) >> 5;
  }
  st.lds0 = (unsigned)(size_t)smem;
  {
    const int xr = lane >> 3;
    st.xb = x + (size_t)mt * 128 * D_MODEL;
    st.xvoff[0] = ((lw * 32 + xr) * D_MODEL + ((lane & 7) ^ (xr >> 1)) * 8) * 2;
    st.xvoff[1] = st.xvoff[0] ^ 64;
    const int nq = min(max(52 - nt * 10, 0), 10), ngu = 10 - nq;
    const int qcol = nt < 2 ? 1280 + 320 * nt : 320 * nt - 640;
    const int gcol = 16 * (nt * 10 + nq - 52);
#pragma unroll
    for (int j = 0; j < 6; ++j) {
      const int pp = j * 64 + lane;
      const int row = pp / 44, cp = min(pp - row * 44, 41);
      const int k = 8 * lw + (row & 7);
      const int cq = cp - 4 * nq;
      if (cq < 0 || (cq >= 2 * ngu && cq < 2 * ngu + 2)) {
        const int col = cq < 0 ? qcol + cp * 8 : 1024 + 16 * nt + 8 * (cq - 2 * ngu);
        st.gp[j] = (unsigned long long)(wqkv + (size_t)k * 1920 + col);
        st.pitch[j] = 2 * 1920;
      } else {
        const int half = cq >= 2 * ngu;
        const int col = gcol + 8 * (cq - half * (2 * ngu + 2));
        st.gp[j] = (unsigned long long)((half ? wu : wg) + (size_t)k * F_DIM + col);
        st.pitch[j] = 2 * F_DIM;
      }
    }
  }
  k1_ldx<0, 0, 4>(st);
  k1_ldw<0>(st);
  k1_ldw<1>(st);
  k1_ldx<1, 0, 4>(st);
  k1_ldw<2>(st);
  k1_ldw<3>(st);
  WAITVM(16);
  BARRIER();
  k1_lphase<0>(st);
  k1_ldx<9, 2, 2>(st);
  k1_ldw<19>(st);
  const int item = blockIdx.x * 4 + lw;
  const bool has_item = item < 740;
  const int isd = item < 540;
  const int it2 = isd ? item : (has_item ? item - 540 : 0);
  const int ks = it2 / 10, n64 = it2 - ks * 10;
  u32x4 tw[4];
  {
    const u16* src = (isd ? wd : wo) + (size_t)(ks * 32 + (lane >> 3)) * D_MODEL + n64 * 64 + (lane & 7) * 8;
#pragma unroll
    for (int i = 0; i < 4; ++i) GLOAD(tw[i], src + i * 8 * D_MODEL);
  }
  const int ch2 = 64 + (lane & 15);
  u32x4 xv[2], xv2[2], wv, wv2;
#pragma unroll
  for (int r = 0; r < 2; ++r) {
    const u16* xr = x + (size_t)(item * 2 + r) * D_MODEL;
    GLOAD(xv[r], xr + lane * 8);
    GLOAD(xv2[r], xr + ch2 * 8);
  }
  GLOAD(wv, gw + lane * 8);
  GLOAD(wv2, gw + ch2 * 8);
  WAITVM(10 + 8 + 6);
  BARRIER();
  WAITVM(10 + 6);
  BARRIER();
  WAITVM(10);
  BARRIER();
  asm volatile("s_waitcnt vmcnt(0)" : "+v"(tw[0]), "+v"(tw[1]), "+v"(tw[2]), "+v"(tw[3]), "+v"(xv[0]), "+v"(xv[1]),
               "+v"(xv2[0]), "+v"(xv2[1]), "+v"(wv), "+v"(wv2));
  if (has_item) {
    char* tb = smem + K1_TR + lw * 5632;
#pragma unroll
    for (int i = 0; i < 4; ++i) *(u32x4*)(tb + i * 1408 + (lane >> 3) * 160 + (lane & 7) * 16) = tw[i];
    const int g = lane >> 4, c = lane & 15;
    const char* rp = tb + g * 1408 + (c >> 2) * 160 + (c & 3) * 8;
    char* dst = (isd ? Wdf : Wof) + ((size_t)(ks * 40 + n64 * 4) * 64 + lane) * 16;
#pragma unroll
    for (int nb = 0; nb < 4; ++nb) {
      bfx8 f = tr_frag(rp + nb * 32, rp + nb * 32 + 640);
      *(bfx8*)(dst + nb * 1024) = f;
    }
  }
#pragma unroll
  for (int r = 0; r < 2; ++r) {
    float s = 0.f;
#pragma unroll
    for (int e = 0; e < 4; ++e) {
      s += rbf(__uint_as_float(xv[r][e] << 16) * __uint_as_float(wv[e] << 16));
      s += rbf(__uint_as_float(xv[r][e] & 0xffff0000u) * __uint_as_float(wv[e] & 0xffff0000u));
    }
    if (lane < 16) {
#pragma unroll
      for (int e = 0; e < 4; ++e) {
        s += rbf(__uint_as_float(xv2[r][e] << 16) * __uint_as_float(wv2[e] << 16));
        s += rbf(__uint_as_float(xv2[r][e] & 0xffff0000u) * __uint_as_float(wv2[e] & 0xffff0000u));
      }
    }
#pragma unroll
    for (int o = 32; o >= 1; o >>= 1) s += __shfl_xor(s, o);
    s = rbf(s);
    float gv = rbf(1.0f / (1.0f + __expf(-s)));
    if (lane == 0) G[item * 2 + r] = gv;
  }
  if (lw == 0 && lane == 0) flags[blockIdx.x] = 0u;
  if (item == 1 && lane < 4) flags[1024 + lane] = 0x3f803f80u;
}

__global__ __launch_bounds__(768, 3) void k1_gemm(const u16* __restrict__ x, const u16* __restrict__ wqkv,
                                                  const u16* __restrict__ wg, const u16* __restrict__ wu,
                                                  const u16* __restrict__ wd, const u16* __restrict__ wo,
                                                  const u16* __restrict__ gw, char* __restrict__ Qf,
                                                  char* __restrict__ Vf, char* __restrict__ Hf,
                                                  char* __restrict__ Wdf, char* __restrict__ Wof,
                                                  float* __restrict__ G, unsigned* __restrict__ flags) {
  __shared__ __attribute__((aligned(16))) char smem[K1_LDS];
  const int w8 = __builtin_amdgcn_readfirstlane(threadIdx.x) >> 9;
  int mt, nt;
  k1_tile(blockIdx.x, mt, nt);
  if (w8) k1_loader(smem, x, wqkv, wg, wu, wd, wo, gw, Wdf, Wof, G, flags);
  else if (nt < 2) k1_compute<true>(smem, x, wqkv, wg, wu, Qf, Vf, Hf);
  else k1_compute<false>(smem, x, wqkv, wg, wu, Qf, Vf, Hf);
}

#define AT_K 15360
#define AT_VS 1296
#define AT_LDS (AT_K + 10 * AT_VS)
__device__ __forceinline__ void attn_stage(const char* __restrict__ Kf, const char* __restrict__ Vf, unsigned lds,
                                           int h, int qb0, int lane, unsigned ones) {
  const int slot0 = (20 * h) >> 3;
#pragma unroll
  for (int i = 0; i < 15; ++i) {
    const int p = i * 64 + lane;
    const int blk = p / 48, w = p - blk * 48;
    const int gb = max(qb0 - 8 + (blk >> 1), 0) * 2 + (blk & 1);
    GLDS16S(gb * 20480 + w * 16, Kf + slot0 * 256, lds + i * 1024);
  }
#pragma unroll
  for (int i = 0; i < 13; ++i) {
    const int p = i * 64 + lane;
    const int grp = p / 81, w = p - grp * 81;
    const int gg = max(qb0 - 8 + grp, 0);
    const unsigned o = w < 80 ? (unsigned)(gg * 1280 + w * 16) : ones - h * 81920;
    if (i < 12 || lane < 42) GLDS16S(o, Vf + (size_t)h * 81920, lds + AT_K + i * 1024);
  }
}
#define SB() __builtin_amdgcn_sched_barrier(0)
__device__ __forceinline__ f32x16 zero16() {
  return f32x16{0, 0, 0, 0, 0, 0, 0, 0, 0, 0, 0, 0, 0, 0, 0, 0};
}
__device__ __forceinline__ float at_fma(float a, float cs, float nmc) {
  float t = fmaf(a, cs, nmc);
  asm volatile("" : "+v"(t));
  return t;
}
template <bool FIRST>
__device__ __forceinline__ void at_max(const f32x16& t, float (&mx)[4]) {
#pragma unroll
  for (int r = 0; r < 4; ++r) {
    mx[r] = FIRST ? fmaxf(t[r], t[4 + r]) : fmaxf(fmaxf(mx[r], t[r]), t[4 + r]);
    mx[r] = fmaxf(fmaxf(mx[r], t[8 + r]), t[12 + r]);
    asm volatile("" : "+v"(mx[r]));
  }
}
__device__ __forceinline__ void at_neg(f32x16& t, bool c) {
#pragma unroll
  for (int i = 0; i < 16; ++i) t[i] = c ? -1e30f : t[i];
}
template <bool EXP, bool QK, bool EDGE>
__device__ __forceinline__ void at_stage(f32x16 (&s)[8], float& mc, f32x16& o, bfx8 q1, bfx8 q2, const char* kp1,
                                         const char* kp2, const char* vpa, const char* vpb, const unsigned (&mk)[8],
                                         int jmin, int dq) {
  const float cs = 0.22360679774997896f * 1.4426950408889634f;
  const float nmc = -mc;
  float mx[4];
  f32x16 sx;
  u32x4 pk[2], pp[2];
  bfx8 ka, kb, kbp, xa, xb;
  bfx8 va, vb, wa, wb;
  if constexpr (QK) { xa = *(const bfx8*)kp1; xb = *(const bfx8*)kp2; }
  if constexpr (EXP) o = zero16();
#pragma unroll
  for (int u = 0; u < 8; ++u) {
    const int j = u == 0 ? 8 : u;
    SB();
    float t[4];
#define AT_F(c4) if constexpr (EXP) { _Pragma("unroll") for (int e = 0; e < 4; ++e) t[e] = at_fma(s[u][4 * (c4) + e], cs, nmc); }
#define AT_E(c4, h2) if constexpr (EXP) { pk[(c4) >> 1][2 * ((c4) & 1) + (h2)] = pack2(__builtin_amdgcn_exp2f(t[2 * (h2)]), __builtin_amdgcn_exp2f(t[2 * (h2) + 1])); }
    if constexpr (QK) {
      if (u == 0) sx = mfma32(xa, q1, zero16());
      else s[u - 1] = mfma32(kbp, q2, s[u - 1]);
    }
    SB();
    AT_F(0); AT_E(0, 0); AT_E(0, 1);
    SB();
    if constexpr (QK) { if (u == 0) sx = mfma32(xb, q2, sx); }
    if constexpr (EXP) {
      if (u == 1) {
        u32x4 m8;
#pragma unroll
        for (int e = 0; e < 4; ++e) m8[e] = pp[0][e] & mk[e];
        o = mfma32(va, __builtin_bit_cast(bfx8, m8), o);
      } else if (u > 1) o = mfma32(va, __builtin_bit_cast(bfx8, pp[0]), o);
    }
    SB();
    if constexpr (QK) { ka = *(const bfx8*)(kp1 + j * 1536); kb = *(const bfx8*)(kp2 + j * 1536); }
    AT_F(1); AT_E(1, 0);
    if constexpr (EXP) {
      if (u == 1) {
        u32x4 m8;
#pragma unroll
        for (int e = 0; e < 4; ++e) m8[e] = pp[0][e] & ~mk[e];
        SB();
        o = mfma32(wa, __builtin_bit_cast(bfx8, m8), o);
        SB();
      }
    }
    AT_E(1, 1);
    SB();
    if constexpr (EXP) {
      if (u == 1) {
        u32x4 m8;
#pragma unroll
        for (int e = 0; e < 4; ++e) m8[e] = pp[1][e] & mk[4 + e];
        o = mfma32(vb, __builtin_bit_cast(bfx8, m8), o);
      } else if (u > 1) o = mfma32(vb, __builtin_bit_cast(bfx8, pp[1]), o);
    }
    SB();
    AT_F(2); AT_E(2, 0);
    if constexpr (EXP) {
      if (u == 1) {
        u32x4 m8;
#pragma unroll
        for (int e = 0; e < 4; ++e) m8[e] = pp[1][e] & ~mk[4 + e];
        SB();
        o = mfma32(wb, __builtin_bit_cast(bfx8, m8), o);
        SB();
      }
    }
    AT_E(2, 1);
    if constexpr (QK) {
      if (u == 1) {
        if constexpr (EDGE) at_neg(sx, jmin > 0);
#pragma unroll
        for (int i = 0; i < 16; ++i) s[0][i] = 16 * (i >> 3) + (i & 7) <= dq ? s[0][i] : sx[i];
        at_max<true>(s[0], mx);
      } else if (u > 1) {
        if constexpr (EDGE) at_neg(s[u - 1], u - 1 < jmin);
        at_max<false>(s[u - 1], mx);
      }
    }
    AT_F(3);
    SB();
    if constexpr (QK) s[u] = mfma32(ka, q1, zero16());
    SB();
    if constexpr (EXP) {
      va = *(const bfx8*)(vpa + j * AT_VS);
      if (u == 0) wa = *(const bfx8*)vpa;
      vb = *(const bfx8*)(vpb + j * AT_VS);
      if (u == 0) wb = *(const bfx8*)vpb;
    }
    AT_E(3, 0); AT_E(3, 1);
    kbp = kb;
    pp[0] = pk[0]; pp[1] = pk[1];
  }
  SB();
  if constexpr (QK) s[7] = mfma32(kbp, q2, s[7]);
  if constexpr (EXP) {
    o = mfma32(va, __builtin_bit_cast(bfx8, pp[0]), o);
    o = mfma32(vb, __builtin_bit_cast(bfx8, pp[1]), o);
  }
  if constexpr (QK) {
    if constexpr (EDGE) at_neg(s[7], 7 < jmin);
    at_max<false>(s[7], mx);
    float m = fmaxf(fmaxf(mx[0], mx[1]), fmaxf(mx[2], mx[3]));
    u32x2 r32 = __builtin_amdgcn_permlane32_swap(__float_as_uint(m), __float_as_uint(m), false, false);
    m = fmaxf(__uint_as_float(r32[0]), __uint_as_float(r32[1]));
    mc = m * cs;
  }
}
__device__ __forceinline__ void at_store(const f32x16& o, float gq, char* __restrict__ dst, int k0, int hh) {
  u32x2 r32 = __builtin_amdgcn_permlane32_swap(__float_as_uint(o[8]), __float_as_uint(o[8]), false, false);
  const float inv = gq * __builtin_amdgcn_rcpf(__uint_as_float(r32[1]));
#pragma unroll
  for (int q = 0; q < 3; ++q) {
    const int k = k0 + 8 * q;
    u32x2 v = {pack2(o[4 * q] * inv, o[4 * q + 1] * inv), pack2(o[4 * q + 2] * inv, o[4 * q + 3] * inv)};
    if (q < 2 || hh == 0) st8b(dst + (k >> 3) * 256 + ((k >> 2) & 1) * 8, v);
  }
}
template <bool EDGE>
__device__ __forceinline__ void attn_run(const u32x4 (&qv)[4], const float (&gq)[2], const char* al, char* __restrict__ Of,
                                         int h, int qb0, int lane) {
  asm volatile("" : "+v"(lane));
  const int hh = lane >> 5, n = lane & 31;
  bfx8 qf[4];
#pragma unroll
  for (int t = 0; t < 4; ++t) {
    u32x4 v = qv[t];
    if ((t & 1) && (hh || !(h & 1))) { v[2] = 0; v[3] = 0; }
    if ((t & 1) ? hh : (!hh && (h & 1))) { v[0] = 0; v[1] = 0; }
    qf[t] = __builtin_bit_cast(bfx8, v);
  }
  const char* kp2 = al + ((n >> 3) & 1) * 768 + (8 * (n >> 4) + (n & 7)) * 16;
  const char* kp1 = kp2 + hh * 256;
  kp2 += 512;
  const char* vpa = al + AT_K + (n < 16 ? hh * 256 + n * 16 : n < 20 ? 1024 + hh * 64 + (n - 16) * 16 : 1280);
  const char* vpb = vpa + (n < 16 ? 512 : n < 20 ? 128 : 0);
  const int dq = n - 8 * hh;
  unsigned mk[8];
#pragma unroll
  for (int t = 0; t < 8; ++t) {
    const int ko = 16 * (t >> 2) + 2 * (t & 3);
    mk[t] = (ko <= dq ? 0xffffu : 0u) | (ko + 1 <= dq ? 0xffff0000u : 0u);
  }
  const int jm = 8 - qb0;
  char* dst = Of + (size_t)(qb0 * 2 + (n >> 4)) * 20480 + (n & 15) * 16;
  const int k0 = h * 20 + 4 * hh;
  f32x16 s[8], o;
  float mc = 0.f;
#define AST(i)
  AST(0);
  at_stage<false, true, EDGE>(s, mc, o, qf[0], qf[1], kp1, kp2, vpa, vpb, mk, jm, dq);
  AST(1);
  WAITVM(0);
  AST(2);
  at_stage<true, true, EDGE>(s, mc, o, qf[2], qf[3], kp1 + 1536, kp2 + 1536, vpa, vpb, mk, jm - 1, dq);
  AST(3);
  at_store(o, gq[0], dst, k0, hh);
  AST(4);
  at_stage<true, false, EDGE>(s, mc, o, qf[2], qf[3], kp1, kp2, vpa + AT_VS, vpb + AT_VS, mk, jm, dq);
  AST(5);
  at_store(o, gq[1], dst + 40960, k0, hh);
  AST(6);
  AST(7);
}

#define GLOADSC(r, o, p) asm volatile("global_load_dwordx4 %0, %1, %2 sc1" : "=v"(r) : "v"(o), "s"(p))
struct G2Frag { u32x4 a[4], w[5]; };
__device__ __forceinline__ void g2_issue_w(G2Frag& f, const char* Wb, int ks, int voff) {
  const char* wp = Wb + (size_t)ks * 40960 + 2048;
  GLOADSO(f.w[0], voff, wp, -2048);
  GLOADSO(f.w[1], voff, wp, -1024);
  GLOADSO(f.w[2], voff, wp, 0);
  GLOADSO(f.w[3], voff, wp, 1024);
  GLOADSO(f.w[4], voff, wp, 2048);
}
__device__ __forceinline__ void g2_issue_h(G2Frag& f, const char* Ab, int ks, int voff) {
  const char* ap = Ab + (size_t)ks * 1024;
#pragma unroll
  for (int i = 0; i < 4; ++i) GLOADS(f.a[i], voff, ap + (size_t)i * 54 * 1024);
}
__device__ __forceinline__ void g2_issue_o(G2Frag& f, const char* Ab, int ks, int voff) {
  const char* ap = Ab + (size_t)ks * 1024;
#pragma unroll
  for (int i = 0; i < 4; ++i) GLOADSC(f.a[i], voff, ap + (size_t)i * 20 * 1024);
}
template <int N>
__device__ __forceinline__ void g2_wait(G2Frag& f) {
  asm volatile("s_waitcnt vmcnt(%9)"
               : "+v"(f.a[0]), "+v"(f.a[1]), "+v"(f.a[2]), "+v"(f.a[3]), "+v"(f.w[0]), "+v"(f.w[1]), "+v"(f.w[2]),
                 "+v"(f.w[3]), "+v"(f.w[4])
               : "i"(N));
}
struct G2Ctx {
  const char *Hb, *Wdb, *Ob, *Wob;
  const unsigned* fl;
  const float* gp;
  int wave, voff;
  float omg[4];
  unsigned fv;
};
#define GST(i)
template <int S>
__device__ __forceinline__ void g2_steps(G2Frag (&f)[4], f32x4 (&acc)[5][4], G2Ctx& cx) {
  constexpr int W = S <= 10 ? 27 : S == 11 ? 23 : S == 12 ? 19 : S == 13 ? 20 : S == 14 ? 12 : S == 15 ? 17 : S == 16 ? 13 : S == 17 ? 9 : 0;
  G2Frag& cur = f[S & 3];
  g2_wait<W>(cur);
  GST(S + 1);
  if (S != 13 || cx.wave < 2) {
#pragma unroll
    for (int u = 0; u < 5; ++u)
#pragma unroll
      for (int i = 0; i < 4; ++i)
        acc[u][i] = mfma16(__builtin_bit_cast(bfx8, cur.w[u]), __builtin_bit_cast(bfx8, cur.a[i]), acc[u][i]);
  }
  __builtin_amdgcn_sched_barrier(0);
  if constexpr (S < 10) {
    const int ks = min((S + 4) * 4 + cx.wave, 53);
    g2_issue_h(cur, cx.Hb, ks, cx.voff);
    g2_issue_w(cur, cx.Wdb, ks, cx.voff);
  } else if constexpr (S < 14) {
    if constexpr (S == 12) {
      const int go = (cx.voff >> 2) & 60;
#pragma unroll
      for (int i = 0; i < 4; ++i)
        asm volatile("global_load_dword %0, %1, %2 offset:%3" : "=v"(cx.omg[i]) : "v"(go), "s"(cx.gp), "i"(i * 64));
      asm volatile("global_load_dword %0, %1, %2 sc1" : "=v"(cx.fv) : "v"(go & 28), "s"(cx.fl));
    }
    g2_issue_w(cur, cx.Wob, (S - 10) * 4 + cx.wave, cx.voff);
    if constexpr (S == 13) {
      asm volatile("s_waitcnt vmcnt(10)" : "+v"(cx.fv), "+v"(cx.omg[0]), "+v"(cx.omg[1]), "+v"(cx.omg[2]), "+v"(cx.omg[3]));
#pragma unroll 1
      for (int it = 0; it < (1 << 20) && __builtin_amdgcn_ballot_w64(cx.fv != 1u) != 0; ++it) {
        __builtin_amdgcn_s_sleep(2);
        asm volatile("global_load_dword %0, %1, %2 sc1\n\ts_waitcnt vmcnt(0)" : "=v"(cx.fv) : "v"((cx.voff >> 2) & 28), "s"(cx.fl) : "memory");
      }
      asm volatile("" ::: "memory");
#pragma unroll
      for (int j = 0; j < 4; ++j) g2_issue_o(f[(14 + j) & 3], cx.Ob, j * 4 + cx.wave, cx.voff);
#pragma unroll
      for (int u = 0; u < 5; ++u)
#pragma unroll
        for (int i = 0; i < 4; ++i) acc[u][i] *= rbf(1.0f - cx.omg[i]);
      __builtin_amdgcn_sched_barrier(0);
    }
  } else if constexpr (S == 14) {
    g2_issue_o(cur, cx.Ob, 16 + cx.wave, cx.voff);
    g2_issue_w(cur, cx.Wob, 16 + cx.wave, cx.voff);
  }
  if constexpr (S + 1 < 19) g2_steps<S + 1>(f, acc, cx);
}
__device__ __forceinline__ void g2_tile(int bid, int& mt, int& nt) {
  const int xcd = bid & 7, idx = bid >> 3;
  mt = (xcd >> 1) * 8 + (idx >> 2);
  nt = (xcd & 1) * 4 + (idx & 3);
}

__global__ __launch_bounds__(512, 2) void k2_mix(const char* __restrict__ Qf, const char* __restrict__ Kf,
                                                 const char* __restrict__ Vf, char* Of,
                                                 const char* __restrict__ Hf, const char* __restrict__ Wdf,
                                                 const char* __restrict__ Wof, const float* __restrict__ G,
                                                 unsigned* flags, u16* __restrict__ out) {
  __shared__ __attribute__((aligned(16))) char smem[4 * AT_LDS];
  __shared__ unsigned k2_cnt;
  const int tid = threadIdx.x, lane = tid & 63;
  const int wave = __builtin_amdgcn_readfirstlane(tid >> 6) ^ 4;
  const int bid = blockIdx.x;
  int mt, nt;
  g2_tile(bid, mt, nt);
  if (wave >= 4) {
    float gq[2];
    {
      const float* gp = G + mt * 64 + (lane & 31);
#pragma unroll
      for (int i = 0; i < 2; ++i) GLOAD1(gq[i], gp + i * 32);
    }
    const int h = nt * 4 + (wave - 4);
    const int qb0 = mt * 2;
    if (wave == 4) ST(0, 0);
    char* al = smem + (wave - 4) * AT_LDS;
    u32x4 qv[4];
    {
      const char* qp = Qf + (size_t)(qb0 * 2 + ((lane >> 4) & 1)) * 20480 + ((20 * h) >> 3) * 256 + (lane & 15) * 16;
#pragma unroll
      for (int i = 0; i < 4; ++i) GLOAD(qv[i], qp + (i >> 1) * 40960 + ((i & 1) ? 512 : (lane >> 5) * 256));
    }
    attn_stage(Kf, Vf, (unsigned)(size_t)al, h, qb0, lane, (unsigned)((const char*)(flags + 1024) - Vf));
    if (tid == 0) k2_cnt = 0u;
    BARRIER();
    asm volatile("s_waitcnt vmcnt(13)" : "+v"(qv[0]), "+v"(qv[1]), "+v"(qv[2]), "+v"(qv[3]), "+v"(gq[0]),
                 "+v"(gq[1]) :: "memory");
    if (wave == 4) ST(0, 1);
    if (qb0 < 8) attn_run<true>(qv, gq, al, Of, h, qb0, lane);
    else attn_run<false>(qv, gq, al, Of, h, qb0, lane);
    if (wave == 4) ST(0, 2);
    if (wave == 4) ST(0, 3);
    WAITVM(0);
    if (lane == 0) {
      unsigned old = __hip_atomic_fetch_add(&k2_cnt, 1u, __ATOMIC_RELAXED, __HIP_MEMORY_SCOPE_WORKGROUP);
      if (old == 3u) __hip_atomic_store(flags + mt * 8 + nt, 1u, __ATOMIC_RELAXED, __HIP_MEMORY_SCOPE_AGENT);
    }
    if (wave == 4) ST(0, 4);
    BARRIER();
    return;
  }
  f32x4 acc[5][4];
#pragma unroll
  for (int u = 0; u < 5; ++u)
#pragma unroll
    for (int i = 0; i < 4; ++i) acc[u][i] = f32x4{0, 0, 0, 0};
  G2Ctx cx;
  cx.Hb = Hf + (size_t)mt * 4 * 54 * 1024;
  cx.Wdb = Wdf + (size_t)nt * 5120;
  cx.Ob = Of + (size_t)mt * 4 * 20 * 1024;
  cx.Wob = Wof + (size_t)nt * 5120;
  cx.fl = flags + mt * 8;
  cx.gp = G + mt * 64;
  cx.wave = wave;
  cx.voff = lane * 16;
  BARRIER();
  GST(0);
  G2Frag f[4];
#pragma unroll
  for (int s = 0; s < 4; ++s) {
    g2_issue_h(f[s], cx.Hb, s * 4 + wave, cx.voff);
    g2_issue_w(f[s], cx.Wdb, s * 4 + wave, cx.voff);
  }
  g2_steps<0>(f, acc, cx);
  GST(20);
  f32x4* red = (f32x4*)smem;
  BARRIER();
#pragma unroll
  for (int u = 0; u < 5; ++u)
#pragma unroll
    for (int i = 0; i < 4; ++i) red[((wave * 4 + i) * 5 + u) * 64 + lane] = acc[u][i];
  BARRIER();
  const int tok = mt * 64 + wave * 16 + (lane & 15);
#pragma unroll
  for (int u = 0; u < 5; ++u) {
    f32x4 v = red[((0 * 4 + wave) * 5 + u) * 64 + lane];
#pragma unroll
    for (int w = 1; w < 4; ++w) v += red[((w * 4 + wave) * 5 + u) * 64 + lane];
    u32x2 pv = {pack2(v[0], v[1]), pack2(v[2], v[3])};
    *(u32x2*)(out + (size_t)tok * D_MODEL + nt * 80 + u * 16 + 4 * (lane >> 4)) = pv;
  }
  GST(21);
}

extern "C" void kernel_launch(void* const* d_in, const int* in_sizes, int n_in,
                              void* d_out, int out_size, void* d_ws, size_t ws_size,
                              hipStream_t stream) {
  const u16* x = (const u16*)d_in[0];
  const u16* wqkv = (const u16*)d_in[1];
  const u16* wo = (const u16*)d_in[2];
  const u16* wg = (const u16*)d_in[4];
  const u16* wu = (const u16*)d_in[5];
  const u16* wd = (const u16*)d_in[6];
  const u16* gw = (const u16*)d_in[7];
  char* ws = (char*)d_ws;
  const size_t QB = (size_t)S_LEN * D_MODEL * 2;
  char* Qf = ws;
  char* Kf = Qf + QB;
  char* Vf = Kf + QB;
  char* Hf = ws + (12u << 20);
  char* Of = ws + (20u << 20);
  char* Wdf = ws + (28u << 20);
  char* Wof = ws + (31u << 20);
  float* G = (float*)(ws + (32u << 20));
  unsigned* flags = (unsigned*)(ws + (33u << 20));
  u16* out = (u16*)d_out;

  k1_gemm<<<256, 768, 0, stream>>>(x, wqkv, wg, wu, wd, wo, gw, Qf, Vf, Hf, Wdf, Wof, G, flags);
  k2_mix<<<256, 512, 0, stream>>>(Qf, Kf, Vf, Of, Hf, Wdf, Wof, G, flags, out);
}
```

```cpp
#include <hip/hip_runtime.h>
#include <hip/hip_bf16.h>
#include <stdint.h>

typedef __attribute__((ext_vector_type(8))) __bf16 bfx8;
typedef __attribute__((ext_vector_type(2))) __bf16 bfx2;
typedef __attribute__((ext_vector_type(2))) float f32x2;
typedef __attribute__((ext_vector_type(4))) short s16x4;
typedef __attribute__((ext_vector_type(8))) short s16x8;
typedef __attribute__((ext_vector_type(4))) float f32x4;
typedef __attribute__((ext_vector_type(16))) float f32x16;
typedef __attribute__((ext_vector_type(2))) unsigned u32x2;
typedef __attribute__((ext_vector_type(4))) unsigned u32x4;
typedef __attribute__((ext_vector_type(4), aligned(8))) unsigned u32x4a8;
typedef __attribute__((address_space(3))) s16x4* lds_s16x4_ptr;
typedef unsigned short u16;

#define FL_STR 64
#define FL_SLP 2
#define S_LEN 2048
#define D_MODEL 640
#define F_DIM 1728

__device__ __forceinline__ float bf2f(u16 v) { return __uint_as_float(((unsigned)v) << 16); }
__device__ __forceinline__ u16 f2bf(float f) {
  __hip_bfloat16 b = __float2bfloat16(f);
  return __builtin_bit_cast(u16, b);
}
__device__ __forceinline__ float rbf(float f) { return bf2f(f2bf(f)); }
__device__ __forceinline__ unsigned pack2(float lo, float hi) {
  bfx2 r = __builtin_convertvector(f32x2{lo, hi}, bfx2);
  return __builtin_bit_cast(unsigned, r);
}
__device__ __forceinline__ f32x4 mfma16(bfx8 a, bfx8 b, f32x4 c) {
  return __builtin_amdgcn_mfma_f32_16x16x32_bf16(a, b, c, 0, 0, 0);
}
__device__ __forceinline__ f32x16 mfma32(bfx8 a, bfx8 b, f32x16 c) {
  return __builtin_amdgcn_mfma_f32_32x32x16_bf16(a, b, c, 0, 0, 0);
}
__device__ __forceinline__ bfx8 tr_frag(const char* p0, const char* p1) {
  s16x4 a = __builtin_amdgcn_ds_read_tr16_b64_v4i16((lds_s16x4_ptr)p0);
  s16x4 b = __builtin_amdgcn_ds_read_tr16_b64_v4i16((lds_s16x4_ptr)p1);
  s16x8 v = {a[0], a[1], a[2], a[3], b[0], b[1], b[2], b[3]};
  return __builtin_bit_cast(bfx8, v);
}
#define STPOL 2
#define STPOLB 2
template <int POL>
__device__ __forceinline__ void st8p(char* p, u32x2 v) {
  if constexpr (POL == 0) *(u32x2*)p = v;
  else if constexpr (POL == 1) __builtin_nontemporal_store(v, (u32x2*)p);
  else if constexpr (POL == 2) asm volatile("global_store_dwordx2 %0, %1, off sc1" :: "v"(p), "v"(v) : "memory");
  else asm volatile("global_store_dwordx2 %0, %1, off sc0 sc1" :: "v"(p), "v"(v) : "memory");
}
__device__ __forceinline__ void st8(char* p, u32x2 v) { st8p<STPOL>(p, v); }
__device__ __forceinline__ void st8b(char* p, u32x2 v) { st8p<STPOLB>(p, v); }
#define GLOAD(r, p) asm volatile("global_load_dwordx4 %0, %1, off" : "=v"(r) : "v"(p))
#define GLOADS(r, o, p) asm volatile("global_load_dwordx4 %0, %1, %2" : "=v"(r) : "v"(o), "s"(p))
#define GLOADSO(r, o, p, imm) asm volatile("global_load_dwordx4 %0, %1, %2 offset:%3" : "=v"(r) : "v"(o), "s"(p), "i"(imm))
#define GLOAD1(r, p) asm volatile("global_load_dword %0, %1, off" : "=v"(r) : "v"(p))
#define WAITV(n, r) asm volatile("s_waitcnt vmcnt(%1)" : "+v"(r) : "i"(n))
__device__ __forceinline__ void bump(const u16*& p, int stride) {
  unsigned lo = (unsigned)(size_t)p, hi = (unsigned)((size_t)p >> 32);
  asm volatile("v_add_co_u32 %0, vcc, %0, %2\n\tv_addc_co_u32 %1, vcc, 0, %1, vcc" : "+v"(lo), "+v"(hi) : "v"(stride) : "vcc");
  p = (const u16*)(((size_t)hi << 32) | lo);
}
#define BARRIER0() asm volatile("s_barrier" ::: "memory")
#define BARRIER() asm volatile("s_waitcnt lgkmcnt(0)\n\ts_barrier" ::: "memory")
#define ST(role, i)

struct Ws {
  char *Qf, *Kf, *Vf, *Hf, *Of, *Wdf, *Wof, *Bb;
  float* G;
};

#define K1_XS 16384
#define K1_WP 704
#define K1_WG (8 * K1_WP)
#define K1_WS (4 * K1_WG)
#define K1_W0 (3 * K1_XS)
#define K1_TR K1_W0
#define K1_LDS (K1_W0 + 5 * K1_WS + 256)

__device__ __forceinline__ void k1_tile(int bid, int& mt, int& nt) {
  const int xcd = bid & 7, idx = bid >> 3;
  mt = (xcd >> 2) * 8 + (idx & 7);
  nt = (xcd & 3) * 4 + (idx >> 3);
}

#define CST(i)

#define K1_PD 4
struct K1C {
  f32x16 acc[5];
  f32x4 accs;
  int wa[6], wah[6];
  int xa[4], xsa[2];
  bfx8 wf[K1_PD + 1], xf[2], xs;
};
template <int F>
__device__ __forceinline__ bfx8 k1_wfrag(const char* smem, const K1C& st) {
  constexpr int P = F / 11, i = F % 11, sl = P % 5;
  constexpr int u = i < 10 ? i % 5 : 5;
  constexpr int off = (sl < 3 ? sl : sl - 3) * K1_WS + (i >= 5 && i < 10 ? 16 * K1_WP : 0);
  const char* b = smem + (sl < 3 ? st.wa[u] : st.wah[u]);
  return tr_frag(b + off, b + off + 4 * K1_WP);
}
template <bool UNSW, int F>
__device__ __forceinline__ void k1_steps(const char* smem, K1C& st, int wave, int lane) {
  constexpr int P = F / 11, i = F % 11;
  if constexpr (F + K1_PD < 220) st.wf[(F + K1_PD) % (K1_PD + 1)] = k1_wfrag<F + K1_PD>(smem, st);
  if constexpr (i == 0) st.xf[1] = *(const bfx8*)(smem + st.xa[(P & 1) * 2 + 1] + ((P >> 1) % 3) * K1_XS);
  if constexpr (i == 5 && P < 19)
    st.xf[0] = *(const bfx8*)(smem + st.xa[((P + 1) & 1) * 2] + (((P + 1) >> 1) % 3) * K1_XS);
  if constexpr (i == 6) st.xs = *(const bfx8*)(smem + st.xsa[P & 1] + ((P >> 1) % 3) * K1_XS);
  if constexpr (i < 10) {
    if constexpr (UNSW) st.acc[i % 5] = mfma32(st.xf[i / 5], st.wf[F % (K1_PD + 1)], st.acc[i % 5]);
    else st.acc[i % 5] = mfma32(st.wf[F % (K1_PD + 1)], st.xf[i / 5], st.acc[i % 5]);
  } else {
    st.accs = mfma16(st.wf[F % (K1_PD + 1)], st.xs, st.accs);
  }
  __builtin_amdgcn_sched_barrier(0);
  if constexpr (i == 10 && P < 18) {
    if constexpr (P & 1) { CST(P + 1); }
    BARRIER0();
    if constexpr (P & 1) { CST(P + 2); }
  }
  if constexpr (F + 1 < 220) k1_steps<UNSW, F + 1>(smem, st, wave, lane);
}

__device__ __forceinline__ float silu_mul(float g, float u) {
  return g * u * __builtin_amdgcn_rcpf(1.0f + __builtin_amdgcn_exp2f(g * -1.4426950408889634f));
}

template <bool UNSW>
__device__ __forceinline__ void k1_compute(char* smem, const u16* __restrict__ x, const u16* __restrict__ wqkv,
                                           const u16* __restrict__ wg, const u16* __restrict__ wu,
                                           char* __restrict__ Qf, char* __restrict__ Vf, char* __restrict__ Hf) {
  const int tid = threadIdx.x, lane = tid & 63;
  const int wave = __builtin_amdgcn_readfirstlane(tid >> 6);
  int mt, nt;
  k1_tile(blockIdx.x, mt, nt);
  const int wm = wave & 3, wn = wave >> 2;
  const int g = lane >> 4, c = lane & 15, hh = lane >> 5;
  const int nq = min(max(52 - nt * 10, 0), 10), ngu = 10 - nq;
  unsigned pf[2];
  {
    const int idx = blockIdx.x >> 3;
    const int qcol = nt < 2 ? 1280 + 320 * nt : 320 * nt - 640;
    const int gcol = 16 * (nt * 10 + nq - 52);
#pragma unroll
    for (int i = 0; i < 2; ++i) {
      const int e = i * 512 + tid;
      const u16* p;
      if (e < 720) {
        const int r = e / 9, q = e - r * 9;
        const int k = r * 8 + (idx & 7);
        if (q == 8) p = wqkv + (size_t)k * 1920 + 1024 + 16 * nt;
        else if (ngu == 0) p = wqkv + (size_t)k * 1920 + qcol + min(q * 64, 312);
        else if (q == 0 && nq > 0) p = wqkv + (size_t)k * 1920 + qcol;
        else p = ((q & 4) ? wu : wg) + (size_t)k * F_DIM + gcol + min((q & 3) * 64, ngu * 16 - 8);
      } else {
        const int f = min(e - 720, 319);
        const int kt = f >> 5, r = (f & 31) * 4 + (idx >> 3);
        p = x + (size_t)(mt * 128 + r) * D_MODEL + kt * 64;
      }
      pf[i] = 0;
    }
  }
  K1C st;
  {
    const int xrow = wm * 32 + (lane & 31);
    const int x0 = xrow * 128 + ((hh ^ ((xrow >> 1) & 7)) << 4);
#pragma unroll
    for (int i = 0; i < 4; ++i) st.xa[i] = x0 ^ (i * 32);
    const int xrs = wm * 32 + wn * 16 + c;
    const int xs0 = xrs * 128 + ((g ^ ((xrs >> 1) & 7)) << 4);
    st.xsa[0] = xs0;
    st.xsa[1] = xs0 ^ 64;
    const int rowp = K1_W0 + (8 * hh + (c >> 2)) * K1_WP + (c & 3) * 8;
#pragma unroll
    for (int u = 0; u < 5; ++u) {
      const int b = wn * 5 + u;
      st.wa[u] = rowp + (b < nq ? b * 64 + (g & 1) * 32 : nq * 32 + b * 32 + ((g & 1) ? ngu * 32 + 32 : 0));
    }
    st.wa[5] = K1_W0 + (8 * g + (c >> 2)) * K1_WP + (c & 3) * 8 + nq * 64 + ngu * 32;
#pragma unroll
    for (int u = 0; u < 6; ++u) {
      st.wah[u] = st.wa[u] + 3 * K1_WS;
      asm volatile("" : "+v"(st.wa[u]), "+v"(st.wah[u]));
    }
#pragma unroll
    for (int i = 0; i < 4; ++i) asm volatile("" : "+v"(st.xa[i]));
    asm volatile("" : "+v"(st.xsa[0]), "+v"(st.xsa[1]));
  }
#pragma unroll
  for (int u = 0; u < 5; ++u)
#pragma unroll
    for (int r = 0; r < 16; ++r) st.acc[u][r] = 0.f;
  st.accs = f32x4{0, 0, 0, 0};
  CST(0);
  BARRIER();
  CST(1);
  st.wf[0] = k1_wfrag<0>(smem, st);
  st.wf[1] = k1_wfrag<1>(smem, st);
  if constexpr (K1_PD > 2) st.wf[2] = k1_wfrag<2>(smem, st);
  if constexpr (K1_PD > 3) st.wf[3] = k1_wfrag<3>(smem, st);
  if constexpr (K1_PD > 4) st.wf[4] = k1_wfrag<4>(smem, st);
  st.xf[0] = *(const bfx8*)(smem + st.xa[0]);
  k1_steps<UNSW, 0>(smem, st, wave, lane);
  CST(20);
  asm volatile("s_waitcnt vmcnt(0)" : "+v"(pf[0]), "+v"(pf[1]));
  const int m0 = mt * 128 + wm * 32;
  {
    const int ts = m0 + wn * 16 + c;
    const int cc = 4 * ((ts >> 3) & 3) + (ts & 3), blk = (ts >> 5) * 2 + ((ts >> 2) & 1);
    u32x2 v = {pack2(st.accs[0], st.accs[1]), pack2(st.accs[2], st.accs[3])};
    st8(Qf + (unsigned)((128 + blk) * 20480 + (48 + 2 * nt + (g >> 1)) * 256 + cc * 16 + (g & 1) * 8), v);
  }
  const int t = m0 + (lane & 31);
  const int tq = (t >> 4) * 20480 + (t & 15) * 16 + hh * 8;
  const int tk = (128 + (t >> 5) * 2 + ((t >> 2) & 1)) * 20480 + (4 * ((t >> 3) & 3) + (t & 3)) * 16 + hh * 8;
#pragma unroll
  for (int u = 0; u < 5; ++u) {
    const int b = wn * 5 + u, B = nt * 10 + b;
    const f32x16 a = st.acc[u];
    if constexpr (UNSW) {
      const int col = 32 * B + (lane & 31);
      const int h = col / 20, d = col - h * 20;
      char* dst = Vf + (unsigned)((h * 64 + (m0 >> 5)) * 1280 + (d < 16 ? d * 16 : 1024 + (d - 16) * 16) + hh * 8);
      const int js = d < 16 ? 256 : 64;
#pragma unroll
      for (int j = 0; j < 4; ++j) {
        u32x2 v = {pack2(a[4 * j], a[4 * j + 1]), pack2(a[4 * j + 2], a[4 * j + 3])};
        st8(dst + j * js, v);
      }
    } else {
      if (b < nq) {
        char* dst = Qf + (unsigned)(B >= 40 ? tk + (B - 40) * 1024 : tq + (B - 20) * 1024);
#pragma unroll
        for (int j = 0; j < 4; ++j) {
          u32x2 v = {pack2(a[4 * j], a[4 * j + 1]), pack2(a[4 * j + 2], a[4 * j + 3])};
          st8(dst + j * 256, v);
        }
      } else {
        char* dst = Hf + (unsigned)(((t >> 4) * 216 + 2 * (B - 52)) * 256 + (t & 15) * 16 + hh * 8);
#pragma unroll
        for (int j = 0; j < 2; ++j) {
          u32x2 v = {pack2(silu_mul(a[4 * j], a[8 + 4 * j]), silu_mul(a[4 * j + 1], a[9 + 4 * j])),
                     pack2(silu_mul(a[4 * j + 2], a[10 + 4 * j]), silu_mul(a[4 * j + 3], a[11 + 4 * j]))};
          st8(dst + j * 256, v);
        }
      }
    }
  }
  CST(21);
}

struct K1Ld {
  unsigned long long gp[6];
  int pitch[6];
  const u16* xb;
  int xvoff[2];
  unsigned lds0;
  int lw, lane, rot;
};
__device__ __forceinline__ unsigned long long rowaddr(unsigned long long p, int pitch, int rows) {
  unsigned long long r;
  asm volatile("v_mad_u64_u32 %0, vcc, %1, %2, %3" : "=v"(r) : "v"(pitch), "s"(rows), "v"(p) : "vcc");
  return r;
}
#define GLDS16(p, l) asm volatile("s_mov_b32 m0, %1\n\ts_nop 0\n\tglobal_load_lds_dwordx4 %0, off" :: "v"(p), "s"(l) : "memory")
#define GLDS16S(o, p, l) asm volatile("s_mov_b32 m0, %2\n\ts_nop 0\n\tglobal_load_lds_dwordx4 %0, %1" :: "v"(o), "s"(p), "s"(l) : "memory")
#define WAITVM(n) asm volatile("s_waitcnt vmcnt(%0)" :: "i"(n) : "memory")
template <int H>
__device__ __forceinline__ void k1_ldw(const K1Ld& st) {
  const unsigned l = st.lds0 + K1_W0 + (H % 5) * K1_WS + st.lw * K1_WG;
  int krow = H * 32 + st.rot * 64;
  if (krow >= 640) krow -= 640;
#pragma unroll
  for (int j = 0; j < 6; ++j) {
    unsigned long long p = rowaddr(st.gp[j], st.pitch[j], krow);
    if (j < 5) GLDS16(p, l + j * 1024);
    else if (st.lane < 32) GLDS16(p, l + 5120);
  }
}
template <int T, int J0, int N>
__device__ __forceinline__ void k1_ldx(const K1Ld& st) {
  int kc = (T + st.rot) * 64;
  if (kc >= 640) kc -= 640;
#pragma unroll
  for (int j = J0; j < J0 + N; ++j)
    GLDS16S(st.xvoff[j & 1], st.xb + j * 8 * D_MODEL + kc, st.lds0 + (T % 3) * K1_XS + (st.lw * 4 + j) * 1024);
}
template <int P>
__device__ __forceinline__ void k1_lphase(const K1Ld& st) {
  k1_ldx<P / 2 + 2, (P & 1) * 2, 2>(st);
  k1_ldw<P + 4>(st);
  WAITVM(14);
  BARRIER();
  if constexpr (P + 1 < 15) k1_lphase<P + 1>(st);
}

__device__ __forceinline__ void k1_loader(char* smem, const u16* __restrict__ x, const u16* __restrict__ wqkv,
                                          const u16* __restrict__ wg, const u16* __restrict__ wu,
                                          const u16* __restrict__ wd, const u16* __restrict__ wo,
                                          const u16* __restrict__ gw, char* __restrict__ Wdf, char* __restrict__ Wof,
                                          float* __restrict__ G, unsigned* __restrict__ flags) {
  const int tid = threadIdx.x, lane = tid & 63;
  const int lw = __builtin_amdgcn_readfirstlane(tid >> 6) - 8;
  int mt, nt;
  k1_tile(blockIdx.x, mt, nt);
  K1Ld st;
  st.lw = lw;
  st.lane = lane;
#define K1_ROT 0
  {
    const int idx = blockIdx.x >> 3;
    st.rot = K1_ROT == 0 ? 0 : K1_ROT == 1 ? ((idx & 7) * 5 + (idx >> 3) * 10 / 4 * 4) / 4 % 10 : K1_ROT == 2 ? (idx & 7) : (idx * 10) >> 5;
  }
  st.lds0 = (unsigned)(size_t)smem;
  {
    const int xr = lane >> 3;
    st.xb = x + (size_t)mt * 128 * D_MODEL;
    st.xvoff[0] = ((lw * 32 + xr) * D_MODEL + ((lane & 7) ^ (xr >> 1)) * 8) * 2;
    st.xvoff[1] = st.xvoff[0] ^ 64;
    const int nq = min(max(52 - nt * 10, 0), 10), ngu = 10 - nq;
    const int qcol = nt < 2 ? 1280 + 320 * nt : 320 * nt - 640;
    const int gcol = 16 * (nt * 10 + nq - 52);
#pragma unroll
    for (int j = 0; j < 6; ++j) {
      const int pp = j * 64 + lane;
      const int row = pp / 44, cp = min(pp - row * 44, 41);
      const int k = 8 * lw + (row & 7);
      const int cq = cp - 4 * nq;
      if (cq < 0 || (cq >= 2 * ngu && cq < 2 * ngu + 2)) {
        const int col = cq < 0 ? qcol + cp * 8 : 1024 + 16 * nt + 8 * (cq - 2 * ngu);
        st.gp[j] = (unsigned long long)(wqkv + (size_t)k * 1920 + col);
        st.pitch[j] = 2 * 1920;
      } else {
        const int half = cq >= 2 * ngu;
        const int col = gcol + 8 * (cq - half * (2 * ngu + 2));
        st.gp[j] = (unsigned long long)((half ? wu : wg) + (size_t)k * F_DIM + col);
        st.pitch[j] = 2 * F_DIM;
      }
    }
  }
  k1_ldx<0, 0, 4>(st);
  k1_ldw<0>(st);
  k1_ldw<1>(st);
  k1_ldx<1, 0, 4>(st);
  k1_ldw<2>(st);
  k1_ldw<3>(st);
  WAITVM(16);
  BARRIER();
  k1_lphase<0>(st);
  k1_ldx<9, 2, 2>(st);
  k1_ldw<19>(st);
  const int item = blockIdx.x * 4 + lw;
  const bool has_item = item < 740;
  const int isd = item < 540;
  const int it2 = isd ? item : (has_item ? item - 540 : 0);
  const int ks = it2 / 10, n64 = it2 - ks * 10;
  u32x4 tw[4];
  {
    const u16* src = (isd ? wd : wo) + (size_t)(ks * 32 + (lane >> 3)) * D_MODEL + n64 * 64 + (lane & 7) * 8;
#pragma unroll
    for (int i = 0; i < 4; ++i) GLOAD(tw[i], src + i * 8 * D_MODEL);
  }
  const int ch2 = 64 + (lane & 15);
  u32x4 xv[2], xv2[2], wv, wv2;
#pragma unroll
  for (int r = 0; r < 2; ++r) {
    const u16* xr = x + (size_t)(item * 2 + r) * D_MODEL;
    GLOAD(xv[r], xr + lane * 8);
    GLOAD(xv2[r], xr + ch2 * 8);
  }
  GLOAD(wv, gw + lane * 8);
  GLOAD(wv2, gw + ch2 * 8);
  WAITVM(10 + 8 + 6);
  BARRIER();
  WAITVM(10 + 6);
  BARRIER();
  WAITVM(10);
  BARRIER();
  asm volatile("s_waitcnt vmcnt(0)" : "+v"(tw[0]), "+v"(tw[1]), "+v"(tw[2]), "+v"(tw[3]), "+v"(xv[0]), "+v"(xv[1]),
               "+v"(xv2[0]), "+v"(xv2[1]), "+v"(wv), "+v"(wv2));
  if (has_item) {
    char* tb = smem + K1_TR + lw * 5632;
#pragma unroll
    for (int i = 0; i < 4; ++i) *(u32x4*)(tb + i * 1408 + (lane >> 3) * 160 + (lane & 7) * 16) = tw[i];
    const int g = lane >> 4, c = lane & 15;
    const char* rp = tb + g * 1408 + (c >> 2) * 160 + (c & 3) * 8;
    char* dst = (isd ? Wdf : Wof) + ((size_t)(ks * 40 + n64 * 4) * 64 + lane) * 16;
#pragma unroll
    for (int nb = 0; nb < 4; ++nb) {
      bfx8 f = tr_frag(rp + nb * 32, rp + nb * 32 + 640);
      *(bfx8*)(dst + nb * 1024) = f;
    }
  }
#pragma unroll
  for (int r = 0; r < 2; ++r) {
    float s = 0.f;
#pragma unroll
    for (int e = 0; e < 4; ++e) {
      s += rbf(__uint_as_float(xv[r][e] << 16) * __uint_as_float(wv[e] << 16));
      s += rbf(__uint_as_float(xv[r][e] & 0xffff0000u) * __uint_as_float(wv[e] & 0xffff0000u));
    }
    if (lane < 16) {
#pragma unroll
      for (int e = 0; e < 4; ++e) {
        s += rbf(__uint_as_float(xv2[r][e] << 16) * __uint_as_float(wv2[e] << 16));
        s += rbf(__uint_as_float(xv2[r][e] & 0xffff0000u) * __uint_as_float(wv2[e] & 0xffff0000u));
      }
    }
#pragma unroll
    for (int o = 32; o >= 1; o >>= 1) s += __shfl_xor(s, o);
    s = rbf(s);
    float gv = rbf(1.0f / (1.0f + __expf(-s)));
    if (lane == 0) G[item * 2 + r] = gv;
  }
  if (lw == 0 && lane == 0) flags[(blockIdx.x >> 3) * FL_STR + (blockIdx.x & 7)] = 0u;
  if (item == 1 && lane < 4) flags[32 * FL_STR + lane] = 0x3f803f80u;
}

__global__ __launch_bounds__(768, 3) void k1_gemm(const u16* __restrict__ x, const u16* __restrict__ wqkv,
                                                  const u16* __restrict__ wg, const u16* __restrict__ wu,
                                                  const u16* __restrict__ wd, const u16* __restrict__ wo,
                                                  const u16* __restrict__ gw, char* __restrict__ Qf,
                                                  char* __restrict__ Vf, char* __restrict__ Hf,
                                                  char* __restrict__ Wdf, char* __restrict__ Wof,
                                                  float* __restrict__ G, unsigned* __restrict__ flags) {
  __shared__ __attribute__((aligned(16))) char smem[K1_LDS];
  const int w8 = __builtin_amdgcn_readfirstlane(threadIdx.x) >> 9;
  int mt, nt;
  k1_tile(blockIdx.x, mt, nt);
  if (w8) k1_loader(smem, x, wqkv, wg, wu, wd, wo, gw, Wdf, Wof, G, flags);
  else if (nt < 2) k1_compute<true>(smem, x, wqkv, wg, wu, Qf, Vf, Hf);
  else k1_compute<false>(smem, x, wqkv, wg, wu, Qf, Vf, Hf);
}

#define AT_K 15360
#define AT_VS 1296
#define AT_LDS (AT_K + 10 * AT_VS)
__device__ __forceinline__ void attn_stage(const char* __restrict__ Kf, const char* __restrict__ Vf, unsigned lds,
                                           int h, int qb0, int lane, unsigned ones) {
  const int slot0 = (20 * h) >> 3;
#pragma unroll
  for (int i = 0; i < 15; ++i) {
    const int p = i * 64 + lane;
    const int blk = p / 48, w = p - blk * 48;
    const int gb = max(qb0 - 8 + (blk >> 1), 0) * 2 + (blk & 1);
    GLDS16S(gb * 20480 + w * 16, Kf + slot0 * 256, lds + i * 1024);
  }
#pragma unroll
  for (int i = 0; i < 13; ++i) {
    const int p = i * 64 + lane;
    const int grp = p / 81, w = p - grp * 81;
    const int gg = max(qb0 - 8 + grp, 0);
    const unsigned o = w < 80 ? (unsigned)(gg * 1280 + w * 16) : ones - h * 81920;
    if (i < 12 || lane < 42) GLDS16S(o, Vf + (size_t)h * 81920, lds + AT_K + i * 1024);
  }
}
#define SB() __builtin_amdgcn_sched_barrier(0)
__device__ __forceinline__ f32x16 zero16() {
  return f32x16{0, 0, 0, 0, 0, 0, 0, 0, 0, 0, 0, 0, 0, 0, 0, 0};
}
__device__ __forceinline__ float at_fma(float a, float cs, float nmc) {
  float t = fmaf(a, cs, nmc);
  asm volatile("" : "+v"(t));
  return t;
}
template <bool FIRST>
__device__ __forceinline__ void at_max(const f32x16& t, float (&mx)[4]) {
#pragma unroll
  for (int r = 0; r < 4; ++r) {
    mx[r] = FIRST ? fmaxf(t[r], t[4 + r]) : fmaxf(fmaxf(mx[r], t[r]), t[4 + r]);
    mx[r] = fmaxf(fmaxf(mx[r], t[8 + r]), t[12 + r]);
    asm volatile("" : "+v"(mx[r]));
  }
}
__device__ __forceinline__ void at_neg(f32x16& t, bool c) {
#pragma unroll
  for (int i = 0; i < 16; ++i) t[i] = c ? -1e30f : t[i];
}
template <bool EXP, bool QK, bool EDGE>
__device__ __forceinline__ void at_stage(f32x16 (&s)[8], float& mc, f32x16& o, bfx8 q1, bfx8 q2, const char* kp1,
                                         const char* kp2, const char* vpa, const char* vpb, const unsigned (&mk)[8],
                                         int jmin, int dq) {
  const float cs = 0.22360679774997896f * 1.4426950408889634f;
  const float nmc = -mc;
  float mx[4];
  f32x16 sx;
  u32x4 pk[2], pp[2];
  bfx8 ka, kb, kbp, xa, xb;
  bfx8 va, vb, wa, wb;
  if constexpr (QK) { xa = *(const bfx8*)kp1; xb = *(const bfx8*)kp2; }
  if constexpr (EXP) o = zero16();
#pragma unroll
  for (int u = 0; u < 8; ++u) {
    const int j = u == 0 ? 8 : u;
    SB();
    float t[4];
#define AT_F(c4) if constexpr (EXP) { _Pragma("unroll") for (int e = 0; e < 4; ++e) t[e] = at_fma(s[u][4 * (c4) + e], cs, nmc); }
#define AT_E(c4, h2) if constexpr (EXP) { pk[(c4) >> 1][2 * ((c4) & 1) + (h2)] = pack2(__builtin_amdgcn_exp2f(t[2 * (h2)]), __builtin_amdgcn_exp2f(t[2 * (h2) + 1])); }
    if constexpr (QK) {
      if (u == 0) sx = mfma32(xa, q1, zero16());
      else s[u - 1] = mfma32(kbp, q2, s[u - 1]);
    }
    SB();
    AT_F(0); AT_E(0, 0); AT_E(0, 1);
    SB();
    if constexpr (QK) { if (u == 0) sx = mfma32(xb, q2, sx); }
    if constexpr (EXP) {
      if (u == 1) {
        u32x4 m8;
#pragma unroll
        for (int e = 0; e < 4; ++e) m8[e] = pp[0][e] & mk[e];
        o = mfma32(va, __builtin_bit_cast(bfx8, m8), o);
      } else if (u > 1) o = mfma32(va, __builtin_bit_cast(bfx8, pp[0]), o);
    }
    SB();
    if constexpr (QK) { ka = *(const bfx8*)(kp1 + j * 1536); kb = *(const bfx8*)(kp2 + j * 1536); }
    AT_F(1); AT_E(1, 0);
    if constexpr (EXP) {
      if (u == 1) {
        u32x4 m8;
#pragma unroll
        for (int e = 0; e < 4; ++e) m8[e] = pp[0][e] & ~mk[e];
        SB();
        o = mfma32(wa, __builtin_bit_cast(bfx8, m8), o);
        SB();
      }
    }
    AT_E(1, 1);
    SB();
    if constexpr (EXP) {
      if (u == 1) {
        u32x4 m8;
#pragma unroll
        for (int e = 0; e < 4; ++e) m8[e] = pp[1][e] & mk[4 + e];
        o = mfma32(vb, __builtin_bit_cast(bfx8, m8), o);
      } else if (u > 1) o = mfma32(vb, __builtin_bit_cast(bfx8, pp[1]), o);
    }
    SB();
    AT_F(2); AT_E(2, 0);
    if constexpr (EXP) {
      if (u == 1) {
        u32x4 m8;
#pragma unroll
        for (int e = 0; e < 4; ++e) m8[e] = pp[1][e] & ~mk[4 + e];
        SB();
        o = mfma32(wb, __builtin_bit_cast(bfx8, m8), o);
        SB();
      }
    }
    AT_E(2, 1);
    if constexpr (QK) {
      if (u == 1) {
        if constexpr (EDGE) at_neg(sx, jmin > 0);
#pragma unroll
        for (int i = 0; i < 16; ++i) s[0][i] = 16 * (i >> 3) + (i & 7) <= dq ? s[0][i] : sx[i];
        at_max<true>(s[0], mx);
      } else if (u > 1) {
        if constexpr (EDGE) at_neg(s[u - 1], u - 1 < jmin);
        at_max<false>(s[u - 1], mx);
      }
    }
    AT_F(3);
    SB();
    if constexpr (QK) s[u] = mfma32(ka, q1, zero16());
    SB();
    if constexpr (EXP) {
      va = *(const bfx8*)(vpa + j * AT_VS);
      if (u == 0) wa = *(const bfx8*)vpa;
      vb = *(const bfx8*)(vpb + j * AT_VS);
      if (u == 0) wb = *(const bfx8*)vpb;
    }
    AT_E(3, 0); AT_E(3, 1);
    kbp = kb;
    pp[0] = pk[0]; pp[1] = pk[1];
  }
  SB();
  if constexpr (QK) s[7] = mfma32(kbp, q2, s[7]);
  if constexpr (EXP) {
    o = mfma32(va, __builtin_bit_cast(bfx8, pp[0]), o);
    o = mfma32(vb, __builtin_bit_cast(bfx8, pp[1]), o);
  }
  if constexpr (QK) {
    if constexpr (EDGE) at_neg(s[7], 7 < jmin);
    at_max<false>(s[7], mx);
    float m = fmaxf(fmaxf(mx[0], mx[1]), fmaxf(mx[2], mx[3]));
    u32x2 r32 = __builtin_amdgcn_permlane32_swap(__float_as_uint(m), __float_as_uint(m), false, false);
    m = fmaxf(__uint_as_float(r32[0]), __uint_as_float(r32[1]));
    mc = m * cs;
  }
}
__device__ __forceinline__ void at_store(const f32x16& o, float gq, char* __restrict__ dst, int k0, int hh) {
  u32x2 r32 = __builtin_amdgcn_permlane32_swap(__float_as_uint(o[8]), __float_as_uint(o[8]), false, false);
  const float inv = gq * __builtin_amdgcn_rcpf(__uint_as_float(r32[1]));
#pragma unroll
  for (int q = 0; q < 3; ++q) {
    const int k = k0 + 8 * q;
    u32x2 v = {pack2(o[4 * q] * inv, o[4 * q + 1] * inv), pack2(o[4 * q + 2] * inv, o[4 * q + 3] * inv)};
    if (q < 2 || hh == 0) st8b(dst + (k >> 3) * 256 + ((k >> 2) & 1) * 8, v);
  }
}
template <bool EDGE>
__device__ __forceinline__ void attn_run(const u32x4 (&qv)[4], const float (&gq)[2], const char* al, char* __restrict__ Of,
                                         int h, int qb0, int lane) {
  asm volatile("" : "+v"(lane));
  const int hh = lane >> 5, n = lane & 31;
  bfx8 qf[4];
#pragma unroll
  for (int t = 0; t < 4; ++t) {
    u32x4 v = qv[t];
    if ((t & 1) && (hh || !(h & 1))) { v[2] = 0; v[3] = 0; }
    if ((t & 1) ? hh : (!hh && (h & 1))) { v[0] = 0; v[1] = 0; }
    qf[t] = __builtin_bit_cast(bfx8, v);
  }
  const char* kp2 = al + ((n >> 3) & 1) * 768 + (8 * (n >> 4) + (n & 7)) * 16;
  const char* kp1 = kp2 + hh * 256;
  kp2 += 512;
  const char* vpa = al + AT_K + (n < 16 ? hh * 256 + n * 16 : n < 20 ? 1024 + hh * 64 + (n - 16) * 16 : 1280);
  const char* vpb = vpa + (n < 16 ? 512 : n < 20 ? 128 : 0);
  const int dq = n - 8 * hh;
  unsigned mk[8];
#pragma unroll
  for (int t = 0; t < 8; ++t) {
    const int ko = 16 * (t >> 2) + 2 * (t & 3);
    mk[t] = (ko <= dq ? 0xffffu : 0u) | (ko + 1 <= dq ? 0xffff0000u : 0u);
  }
  const int jm = 8 - qb0;
  char* dst = Of + (size_t)(qb0 * 2 + (n >> 4)) * 20480 + (n & 15) * 16;
  const int k0 = h * 20 + 4 * hh;
  f32x16 s[8], o;
  float mc = 0.f;
#define AST(i)
  AST(0);
  at_stage<false, true, EDGE>(s, mc, o, qf[0], qf[1], kp1, kp2, vpa, vpb, mk, jm, dq);
  AST(1);
  WAITVM(0);
  AST(2);
  at_stage<true, true, EDGE>(s, mc, o, qf[2], qf[3], kp1 + 1536, kp2 + 1536, vpa, vpb, mk, jm - 1, dq);
  AST(3);
  at_store(o, gq[0], dst, k0, hh);
  AST(4);
  at_stage<true, false, EDGE>(s, mc, o, qf[2], qf[3], kp1, kp2, vpa + AT_VS, vpb + AT_VS, mk, jm, dq);
  AST(5);
  at_store(o, gq[1], dst + 40960, k0, hh);
  AST(6);
  AST(7);
}

#define GLOADSC(r, o, p) asm volatile("global_load_dwordx4 %0, %1, %2 sc1" : "=v"(r) : "v"(o), "s"(p))
struct G2Frag { u32x4 a[4], w[5]; };
__device__ __forceinline__ void g2_issue_w(G2Frag& f, const char* Wb, int ks, int voff) {
  const char* wp = Wb + (size_t)ks * 40960 + 2048;
  GLOADSO(f.w[0], voff, wp, -2048);
  GLOADSO(f.w[1], voff, wp, -1024);
  GLOADSO(f.w[2], voff, wp, 0);
  GLOADSO(f.w[3], voff, wp, 1024);
  GLOADSO(f.w[4], voff, wp, 2048);
}
__device__ __forceinline__ void g2_issue_h(G2Frag& f, const char* Ab, int ks, int voff) {
  const char* ap = Ab + (size_t)ks * 1024;
#pragma unroll
  for (int i = 0; i < 4; ++i) GLOADS(f.a[i], voff, ap + (size_t)i * 54 * 1024);
}
__device__ __forceinline__ void g2_issue_o(G2Frag& f, const char* Ab, int ks, int voff) {
  const char* ap = Ab + (size_t)ks * 1024;
#pragma unroll
  for (int i = 0; i < 4; ++i) GLOADSC(f.a[i], voff, ap + (size_t)i * 20 * 1024);
}
template <int N>
__device__ __forceinline__ void g2_wait(G2Frag& f) {
  asm volatile("s_waitcnt vmcnt(%9)"
               : "+v"(f.a[0]), "+v"(f.a[1]), "+v"(f.a[2]), "+v"(f.a[3]), "+v"(f.w[0]), "+v"(f.w[1]), "+v"(f.w[2]),
                 "+v"(f.w[3]), "+v"(f.w[4])
               : "i"(N));
}
struct G2Ctx {
  const char *Hb, *Wdb, *Ob, *Wob;
  const unsigned* fl;
  const float* gp;
  int wave, voff;
  float omg[4];
  unsigned fv;
};
#define GST(i)
template <int S>
__device__ __forceinline__ void g2_steps(G2Frag (&f)[4], f32x4 (&acc)[5][4], G2Ctx& cx) {
  constexpr int W = S <= 10 ? 27 : S == 11 ? 23 : S == 12 ? 19 : S == 13 ? 20 : S == 14 ? 12 : S == 15 ? 17 : S == 16 ? 13 : S == 17 ? 9 : 0;
  G2Frag& cur = f[S & 3];
  g2_wait<W>(cur);
  GST(S + 1);
  if (S != 13 || cx.wave < 2) {
#pragma unroll
    for (int u = 0; u < 5; ++u)
#pragma unroll
      for (int i = 0; i < 4; ++i)
        acc[u][i] = mfma16(__builtin_bit_cast(bfx8, cur.w[u]), __builtin_bit_cast(bfx8, cur.a[i]), acc[u][i]);
  }
  __builtin_amdgcn_sched_barrier(0);
  if constexpr (S < 10) {
    const int ks = min((S + 4) * 4 + cx.wave, 53);
    g2_issue_h(cur, cx.Hb, ks, cx.voff);
    g2_issue_w(cur, cx.Wdb, ks, cx.voff);
  } else if constexpr (S < 14) {
    if constexpr (S == 12) {
      const int go = (cx.voff >> 2) & 60;
#pragma unroll
      for (int i = 0; i < 4; ++i)
        asm volatile("global_load_dword %0, %1, %2 offset:%3" : "=v"(cx.omg[i]) : "v"(go), "s"(cx.gp), "i"(i * 64));
      asm volatile("global_load_dword %0, %1, %2 sc1" : "=v"(cx.fv) : "v"(go & 28), "s"(cx.fl));
    }
    g2_issue_w(cur, cx.Wob, (S - 10) * 4 + cx.wave, cx.voff);
    if constexpr (S == 13) {
      asm volatile("s_waitcnt vmcnt(10)" : "+v"(cx.fv), "+v"(cx.omg[0]), "+v"(cx.omg[1]), "+v"(cx.omg[2]), "+v"(cx.omg[3]));
#pragma unroll 1
      for (int it = 0; it < (1 << 20) && __builtin_amdgcn_ballot_w64(cx.fv != 1u) != 0; ++it) {
        __builtin_amdgcn_s_sleep(FL_SLP);
        asm volatile("global_load_dword %0, %1, %2 sc1\n\ts_waitcnt vmcnt(0)" : "=v"(cx.fv) : "v"((cx.voff >> 2) & 28), "s"(cx.fl) : "memory");
      }
      asm volatile("" ::: "memory");
      GST(22);
#pragma unroll
      for (int j = 0; j < 4; ++j) g2_issue_o(f[(14 + j) & 3], cx.Ob, j * 4 + cx.wave, cx.voff);
#pragma unroll
      for (int u = 0; u < 5; ++u)
#pragma unroll
        for (int i = 0; i < 4; ++i) acc[u][i] *= rbf(1.0f - cx.omg[i]);
      __builtin_amdgcn_sched_barrier(0);
    }
  } else if constexpr (S == 14) {
    g2_issue_o(cur, cx.Ob, 16 + cx.wave, cx.voff);
    g2_issue_w(cur, cx.Wob, 16 + cx.wave, cx.voff);
  }
  if constexpr (S + 1 < 19) g2_steps<S + 1>(f, acc, cx);
}
__device__ __forceinline__ void g2_tile(int bid, int& mt, int& nt) {
  const int xcd = bid & 7, idx = bid >> 3;
  mt = (xcd >> 1) * 8 + (idx >> 2);
  nt = (xcd & 1) * 4 + (idx & 3);
}

__global__ __launch_bounds__(512, 2) void k2_mix(const char* __restrict__ Qf, const char* __restrict__ Kf,
                                                 const char* __restrict__ Vf, char* Of,
                                                 const char* __restrict__ Hf, const char* __restrict__ Wdf,
                                                 const char* __restrict__ Wof, const float* __restrict__ G,
                                                 unsigned* flags, u16* __restrict__ out) {
  __shared__ __attribute__((aligned(16))) char smem[4 * AT_LDS];
  __shared__ unsigned k2_cnt;
  const int tid = threadIdx.x, lane = tid & 63;
  const int wave = __builtin_amdgcn_readfirstlane(tid >> 6) ^ 4;
  const int bid = blockIdx.x;
  int mt, nt;
  g2_tile(bid, mt, nt);
  if (wave >= 4) {
    float gq[2];
    {
      const float* gp = G + mt * 64 + (lane & 31);
#pragma unroll
      for (int i = 0; i < 2; ++i) GLOAD1(gq[i], gp + i * 32);
    }
    const int h = nt * 4 + (wave - 4);
    const int qb0 = mt * 2;
    if (wave == 4) ST(0, 0);
    char* al = smem + (wave - 4) * AT_LDS;
    u32x4 qv[4];
    {
      const char* qp = Qf + (size_t)(qb0 * 2 + ((lane >> 4) & 1)) * 20480 + ((20 * h) >> 3) * 256 + (lane & 15) * 16;
#pragma unroll
      for (int i = 0; i < 4; ++i) GLOAD(qv[i], qp + (i >> 1) * 40960 + ((i & 1) ? 512 : (lane >> 5) * 256));
    }
    attn_stage(Kf, Vf, (unsigned)(size_t)al, h, qb0, lane, (unsigned)((const char*)(flags + 32 * FL_STR) - Vf));
    if (tid == 0) k2_cnt = 0u;
    BARRIER();
    asm volatile("s_waitcnt vmcnt(13)" : "+v"(qv[0]), "+v"(qv[1]), "+v"(qv[2]), "+v"(qv[3]), "+v"(gq[0]),
                 "+v"(gq[1]) :: "memory");
    if (wave == 4) ST(0, 1);
    if (qb0 < 8) attn_run<true>(qv, gq, al, Of, h, qb0, lane);
    else attn_run<false>(qv, gq, al, Of, h, qb0, lane);
    if (wave == 4) ST(0, 2);
    if (wave == 4) ST(0, 3);
    WAITVM(0);
    if (lane == 0) {
      unsigned old = __hip_atomic_fetch_add(&k2_cnt, 1u, __ATOMIC_RELAXED, __HIP_MEMORY_SCOPE_WORKGROUP);
      if (old == 3u) __hip_atomic_store(flags + mt * FL_STR + nt, 1u, __ATOMIC_RELAXED, __HIP_MEMORY_SCOPE_AGENT);
    }
    if (wave == 4) ST(0, 4);
    BARRIER();
    return;
  }
  f32x4 acc[5][4];
#pragma unroll
  for (int u = 0; u < 5; ++u)
#pragma unroll
    for (int i = 0; i < 4; ++i) acc[u][i] = f32x4{0, 0, 0, 0};
  G2Ctx cx;
  cx.Hb = Hf + (size_t)mt * 4 * 54 * 1024;
  cx.Wdb = Wdf + (size_t)nt * 5120;
  cx.Ob = Of + (size_t)mt * 4 * 20 * 1024;
  cx.Wob = Wof + (size_t)nt * 5120;
  cx.fl = flags + mt * FL_STR;
  cx.gp = G + mt * 64;
  cx.wave = wave;
  cx.voff = lane * 16;
  BARRIER();
  GST(0);
  G2Frag f[4];
#pragma unroll
  for (int s = 0; s < 4; ++s) {
    g2_issue_h(f[s], cx.Hb, s * 4 + wave, cx.voff);
    g2_issue_w(f[s], cx.Wdb, s * 4 + wave, cx.voff);
  }
  g2_steps<0>(f, acc, cx);
  GST(20);
  f32x4* red = (f32x4*)smem;
  BARRIER();
#pragma unroll
  for (int u = 0; u < 5; ++u)
#pragma unroll
    for (int i = 0; i < 4; ++i) red[((wave * 4 + i) * 5 + u) * 64 + lane] = acc[u][i];
  BARRIER();
  const int tok = mt * 64 + wave * 16 + (lane & 15);
#pragma unroll
  for (int u = 0; u < 5; ++u) {
    f32x4 v = red[((0 * 4 + wave) * 5 + u) * 64 + lane];
#pragma unroll
    for (int w = 1; w < 4; ++w) v += red[((w * 4 + wave) * 5 + u) * 64 + lane];
    u32x2 pv = {pack2(v[0], v[1]), pack2(v[2], v[3])};
    *(u32x2*)(out + (size_t)tok * D_MODEL + nt * 80 + u * 16 + 4 * (lane >> 4)) = pv;
  }
  GST(21);
}

extern "C" void kernel_launch(void* const* d_in, const int* in_sizes, int n_in,
                              void* d_out, int out_size, void* d_ws, size_t ws_size,
                              hipStream_t stream) {
  const u16* x = (const u16*)d_in[0];
  const u16* wqkv = (const u16*)d_in[1];
  const u16* wo = (const u16*)d_in[2];
  const u16* wg = (const u16*)d_in[4];
  const u16* wu = (const u16*)d_in[5];
  const u16* wd = (const u16*)d_in[6];
  const u16* gw = (const u16*)d_in[7];
  char* ws = (char*)d_ws;
  const size_t QB = (size_t)S_LEN * D_MODEL * 2;
  char* Qf = ws;
  char* Kf = Qf + QB;
  char* Vf = Kf + QB;
  char* Hf = ws + (12u << 20);
  char* Of = ws + (20u << 20);
  char* Wdf = ws + (28u << 20);
  char* Wof = ws + (31u << 20);
  float* G = (float*)(ws + (32u << 20));
  unsigned* flags = (unsigned*)(ws + (33u << 20));
  u16* out = (u16*)d_out;

  k1_gemm<<<256, 768, 0, stream>>>(x, wqkv, wg, wu, wd, wo, gw, Qf, Vf, Hf, Wdf, Wof, G, flags);
  k2_mix<<<256, 512, 0, stream>>>(Qf, Kf, Vf, Of, Hf, Wdf, Wof, G, flags, out);
}
```

```cpp
#include <hip/hip_runtime.h>
#include <hip/hip_bf16.h>
#include <stdint.h>

typedef __attribute__((ext_vector_type(8))) __bf16 bfx8;
typedef __attribute__((ext_vector_type(2))) __bf16 bfx2;
typedef __attribute__((ext_vector_type(2))) float f32x2;
typedef __attribute__((ext_vector_type(4))) short s16x4;
typedef __attribute__((ext_vector_type(8))) short s16x8;
typedef __attribute__((ext_vector_type(4))) float f32x4;
typedef __attribute__((ext_vector_type(16))) float f32x16;
typedef __attribute__((ext_vector_type(2))) unsigned u32x2;
typedef __attribute__((ext_vector_type(4))) unsigned u32x4;
typedef __attribute__((ext_vector_type(4), aligned(8))) unsigned u32x4a8;
typedef __attribute__((address_space(3))) s16x4* lds_s16x4_ptr;
typedef unsigned short u16;

#define FL_STR 64
#define FL_SLP 2
#define S_LEN 2048
#define D_MODEL 640
#define F_DIM 1728

__device__ __forceinline__ float bf2f(u16 v) { return __uint_as_float(((unsigned)v) << 16); }
__device__ __forceinline__ u16 f2bf(float f) {
  __hip_bfloat16 b = __float2bfloat16(f);
  return __builtin_bit_cast(u16, b);
}
__device__ __forceinline__ float rbf(float f) { return bf2f(f2bf(f)); }
__device__ __forceinline__ unsigned pack2(float lo, float hi) {
  bfx2 r = __builtin_convertvector(f32x2{lo, hi}, bfx2);
  return __builtin_bit_cast(unsigned, r);
}
__device__ __forceinline__ f32x4 mfma16(bfx8 a, bfx8 b, f32x4 c) {
  return __builtin_amdgcn_mfma_f32_16x16x32_bf16(a, b, c, 0, 0, 0);
}
__device__ __forceinline__ f32x16 mfma32(bfx8 a, bfx8 b, f32x16 c) {
  return __builtin_amdgcn_mfma_f32_32x32x16_bf16(a, b, c, 0, 0, 0);
}
__device__ __forceinline__ bfx8 tr_frag(const char* p0, const char* p1) {
  s16x4 a = __builtin_amdgcn_ds_read_tr16_b64_v4i16((lds_s16x4_ptr)p0);
  s16x4 b = __builtin_amdgcn_ds_read_tr16_b64_v4i16((lds_s16x4_ptr)p1);
  s16x8 v = {a[0], a[1], a[2], a[3], b[0], b[1], b[2], b[3]};
  return __builtin_bit_cast(bfx8, v);
}
#define STPOL 2
#define STPOLB 2
template <int POL>
__device__ __forceinline__ void st8p(char* p, u32x2 v) {
  if constexpr (POL == 0) *(u32x2*)p = v;
  else if constexpr (POL == 1) __builtin_nontemporal_store(v, (u32x2*)p);
  else if constexpr (POL == 2) asm volatile("global_store_dwordx2 %0, %1, off sc1" :: "v"(p), "v"(v) : "memory");
  else asm volatile("global_store_dwordx2 %0, %1, off sc0 sc1" :: "v"(p), "v"(v) : "memory");
}
__device__ __forceinline__ void st8(char* p, u32x2 v) { st8p<STPOL>(p, v); }
__device__ __forceinline__ void st8b(char* p, u32x2 v) { st8p<STPOLB>(p, v); }
#define GLOAD(r, p) asm volatile("global_load_dwordx4 %0, %1, off" : "=v"(r) : "v"(p))
#define GLOADS(r, o, p) asm volatile("global_load_dwordx4 %0, %1, %2" : "=v"(r) : "v"(o), "s"(p))
#define GLOADSO(r, o, p, imm) asm volatile("global_load_dwordx4 %0, %1, %2 offset:%3" : "=v"(r) : "v"(o), "s"(p), "i"(imm))
#define GLOAD1(r, p) asm volatile("global_load_dword %0, %1, off" : "=v"(r) : "v"(p))
#define WAITV(n, r) asm volatile("s_waitcnt vmcnt(%1)" : "+v"(r) : "i"(n))
__device__ __forceinline__ void bump(const u16*& p, int stride) {
  unsigned lo = (unsigned)(size_t)p, hi = (unsigned)((size_t)p >> 32);
  asm volatile("v_add_co_u32 %0, vcc, %0, %2\n\tv_addc_co_u32 %1, vcc, 0, %1, vcc" : "+v"(lo), "+v"(hi) : "v"(stride) : "vcc");
  p = (const u16*)(((size_t)hi << 32) | lo);
}
#define BARRIER0() asm volatile("s_barrier" ::: "memory")
#define BARRIER() asm volatile("s_waitcnt lgkmcnt(0)\n\ts_barrier" ::: "memory")
#define ST(role, i)

struct Ws {
  char *Qf, *Kf, *Vf, *Hf, *Of, *Wdf, *Wof, *Bb;
  float* G;
};

#define K1_XS 16384
#define K1_WP 704
#define K1_WG (8 * K1_WP)
#define K1_WS (4 * K1_WG)
#define K1_W0 (3 * K1_XS)
#define K1_TR K1_W0
#define K1_LDS (K1_W0 + 5 * K1_WS + 256)

__device__ __forceinline__ void k1_tile(int bid, int& mt, int& nt) {
  const int xcd = bid & 7, idx = bid >> 3;
  mt = (xcd >> 2) * 8 + (idx & 7);
  nt = (xcd & 3) * 4 + (idx >> 3);
}

#define CST(i)

#define K1_PD 4
struct K1C {
  f32x16 acc[5];
  f32x4 accs;
  int wa[6], wah[6];
  int xa[4], xsa[2];
  bfx8 wf[K1_PD + 1], xf[2], xs;
};
template <int F>
__device__ __forceinline__ bfx8 k1_wfrag(const char* smem, const K1C& st) {
  constexpr int P = F / 11, i = F % 11, sl = P % 5;
  constexpr int u = i < 10 ? i % 5 : 5;
  constexpr int off = (sl < 3 ? sl : sl - 3) * K1_WS + (i >= 5 && i < 10 ? 16 * K1_WP : 0);
  const char* b = smem + (sl < 3 ? st.wa[u] : st.wah[u]);
  return tr_frag(b + off, b + off + 4 * K1_WP);
}
template <bool UNSW, int F>
__device__ __forceinline__ void k1_steps(const char* smem, K1C& st, int wave, int lane) {
  constexpr int P = F / 11, i = F % 11;
  if constexpr (F + K1_PD < 220) st.wf[(F + K1_PD) % (K1_PD + 1)] = k1_wfrag<F + K1_PD>(smem, st);
  if constexpr (i == 0) st.xf[1] = *(const bfx8*)(smem + st.xa[(P & 1) * 2 + 1] + ((P >> 1) % 3) * K1_XS);
  if constexpr (i == 5 && P < 19)
    st.xf[0] = *(const bfx8*)(smem + st.xa[((P + 1) & 1) * 2] + (((P + 1) >> 1) % 3) * K1_XS);
  if constexpr (i == 6) st.xs = *(const bfx8*)(smem + st.xsa[P & 1] + ((P >> 1) % 3) * K1_XS);
  if constexpr (i < 10) {
    if constexpr (UNSW) st.acc[i % 5] = mfma32(st.xf[i / 5], st.wf[F % (K1_PD + 1)], st.acc[i % 5]);
    else st.acc[i % 5] = mfma32(st.wf[F % (K1_PD + 1)], st.xf[i / 5], st.acc[i % 5]);
  } else {
    st.accs = mfma16(st.wf[F % (K1_PD + 1)], st.xs, st.accs);
  }
  __builtin_amdgcn_sched_barrier(0);
  if constexpr (i == 10 && P < 18) {
    if constexpr (P & 1) { CST(P + 1); }
    BARRIER0();
    if constexpr (P & 1) { CST(P + 2); }
  }
  if constexpr (F + 1 < 220) k1_steps<UNSW, F + 1>(smem, st, wave, lane);
}

__device__ __forceinline__ float silu_mul(float g, float u) {
  return g * u * __builtin_amdgcn_rcpf(1.0f + __builtin_amdgcn_exp2f(g * -1.4426950408889634f));
}

template <bool UNSW>
__device__ __forceinline__ void k1_compute(char* smem, const u16* __restrict__ x, const u16* __restrict__ wqkv,
                                           const u16* __restrict__ wg, const u16* __restrict__ wu,
                                           char* __restrict__ Qf, char* __restrict__ Vf, char* __restrict__ Hf) {
  const int tid = threadIdx.x, lane = tid & 63;
  const int wave = __builtin_amdgcn_readfirstlane(tid >> 6);
  int mt, nt;
  k1_tile(blockIdx.x, mt, nt);
  const int wm = wave & 3, wn = wave >> 2;
  const int g = lane >> 4, c = lane & 15, hh = lane >> 5;
  const int nq = min(max(52 - nt * 10, 0), 10), ngu = 10 - nq;
  unsigned pf[2];
  {
    const int idx = blockIdx.x >> 3;
    const int qcol = nt < 2 ? 1280 + 320 * nt : 320 * nt - 640;
    const int gcol = 16 * (nt * 10 + nq - 52);
#pragma unroll
    for (int i = 0; i < 2; ++i) {
      const int e = i * 512 + tid;
      const u16* p;
      if (e < 720) {
        const int r = e / 9, q = e - r * 9;
        const int k = r * 8 + (idx & 7);
        if (q == 8) p = wqkv + (size_t)k * 1920 + 1024 + 16 * nt;
        else if (ngu == 0) p = wqkv + (size_t)k * 1920 + qcol + min(q * 64, 312);
        else if (q == 0 && nq > 0) p = wqkv + (size_t)k * 1920 + qcol;
        else p = ((q & 4) ? wu : wg) + (size_t)k * F_DIM + gcol + min((q & 3) * 64, ngu * 16 - 8);
      } else {
        const int f = min(e - 720, 319);
        const int kt = f >> 5, r = (f & 31) * 4 + (idx >> 3);
        p = x + (size_t)(mt * 128 + r) * D_MODEL + kt * 64;
      }
      pf[i] = 0;
    }
  }
  K1C st;
  {
    const int xrow = wm * 32 + (lane & 31);
    const int x0 = xrow * 128 + ((hh ^ ((xrow >> 1) & 7)) << 4);
#pragma unroll
    for (int i = 0; i < 4; ++i) st.xa[i] = x0 ^ (i * 32);
    const int xrs = wm * 32 + wn * 16 + c;
    const int xs0 = xrs * 128 + ((g ^ ((xrs >> 1) & 7)) << 4);
    st.xsa[0] = xs0;
    st.xsa[1] = xs0 ^ 64;
    const int rowp = K1_W0 + (8 * hh + (c >> 2)) * K1_WP + (c & 3) * 8;
#pragma unroll
    for (int u = 0; u < 5; ++u) {
      const int b = wn * 5 + u;
      st.wa[u] = rowp + (b < nq ? b * 64 + (g & 1) * 32 : nq * 32 + b * 32 + ((g & 1) ? ngu * 32 + 32 : 0));
    }
    st.wa[5] = K1_W0 + (8 * g + (c >> 2)) * K1_WP + (c & 3) * 8 + nq * 64 + ngu * 32;
#pragma unroll
    for (int u = 0; u < 6; ++u) {
      st.wah[u] = st.wa[u] + 3 * K1_WS;
      asm volatile("" : "+v"(st.wa[u]), "+v"(st.wah[u]));
    }
#pragma unroll
    for (int i = 0; i < 4; ++i) asm volatile("" : "+v"(st.xa[i]));
    asm volatile("" : "+v"(st.xsa[0]), "+v"(st.xsa[1]));
  }
#pragma unroll
  for (int u = 0; u < 5; ++u)
#pragma unroll
    for (int r = 0; r < 16; ++r) st.acc[u][r] = 0.f;
  st.accs = f32x4{0, 0, 0, 0};
  CST(0);
  BARRIER();
  CST(1);
  st.wf[0] = k1_wfrag<0>(smem, st);
  st.wf[1] = k1_wfrag<1>(smem, st);
  if constexpr (K1_PD > 2) st.wf[2] = k1_wfrag<2>(smem, st);
  if constexpr (K1_PD > 3) st.wf[3] = k1_wfrag<3>(smem, st);
  if constexpr (K1_PD > 4) st.wf[4] = k1_wfrag<4>(smem, st);
  st.xf[0] = *(const bfx8*)(smem + st.xa[0]);
  k1_steps<UNSW, 0>(smem, st, wave, lane);
  CST(20);
  asm volatile("s_waitcnt vmcnt(0)" : "+v"(pf[0]), "+v"(pf[1]));
  const int m0 = mt * 128 + wm * 32;
  {
    const int ts = m0 + wn * 16 + c;
    const int cc = 4 * ((ts >> 3) & 3) + (ts & 3), blk = (ts >> 5) * 2 + ((ts >> 2) & 1);
    u32x2 v = {pack2(st.accs[0], st.accs[1]), pack2(st.accs[2], st.accs[3])};
    st8(Qf + (unsigned)((128 + blk) * 20480 + (48 + 2 * nt + (g >> 1)) * 256 + cc * 16 + (g & 1) * 8), v);
  }
  const int t = m0 + (lane & 31);
  const int tq = (t >> 4) * 20480 + (t & 15) * 16 + hh * 8;
  const int tk = (128 + (t >> 5) * 2 + ((t >> 2) & 1)) * 20480 + (4 * ((t >> 3) & 3) + (t & 3)) * 16 + hh * 8;
#pragma unroll
  for (int u = 0; u < 5; ++u) {
    const int b = wn * 5 + u, B = nt * 10 + b;
    const f32x16 a = st.acc[u];
    if constexpr (UNSW) {
      const int col = 32 * B + (lane & 31);
      const int h = col / 20, d = col - h * 20;
      char* dst = Vf + (unsigned)((h * 64 + (m0 >> 5)) * 1280 + (d < 16 ? d * 16 : 1024 + (d - 16) * 16) + hh * 8);
      const int js = d < 16 ? 256 : 64;
#pragma unroll
      for (int j = 0; j < 4; ++j) {
        u32x2 v = {pack2(a[4 * j], a[4 * j + 1]), pack2(a[4 * j + 2], a[4 * j + 3])};
        st8(dst + j * js, v);
      }
    } else {
      if (b < nq) {
        char* dst = Qf + (unsigned)(B >= 40 ? tk + (B - 40) * 1024 : tq + (B - 20) * 1024);
#pragma unroll
        for (int j = 0; j < 4; ++j) {
          u32x2 v = {pack2(a[4 * j], a[4 * j + 1]), pack2(a[4 * j + 2], a[4 * j + 3])};
          st8(dst + j * 256, v);
        }
      } else {
        char* dst = Hf + (unsigned)(((t >> 4) * 216 + 2 * (B - 52)) * 256 + (t & 15) * 16 + hh * 8);
#pragma unroll
        for (int j = 0; j < 2; ++j) {
          u32x2 v = {pack2(silu_mul(a[4 * j], a[8 + 4 * j]), silu_mul(a[4 * j + 1], a[9 + 4 * j])),
                     pack2(silu_mul(a[4 * j + 2], a[10 + 4 * j]), silu_mul(a[4 * j + 3], a[11 + 4 * j]))};
          st8(dst + j * 256, v);
        }
      }
    }
  }
  CST(21);
}

struct K1Ld {
  unsigned long long gp[6];
  int pitch[6];
  const u16* xb;
  int xvoff[2];
  unsigned lds0;
  int lw, lane, rot;
};
__device__ __forceinline__ unsigned long long rowaddr(unsigned long long p, int pitch, int rows) {
  unsigned long long r;
  asm volatile("v_mad_u64_u32 %0, vcc, %1, %2, %3" : "=v"(r) : "v"(pitch), "s"(rows), "v"(p) : "vcc");
  return r;
}
#define GLDS16(p, l) asm volatile("s_mov_b32 m0, %1\n\ts_nop 0\n\tglobal_load_lds_dwordx4 %0, off" :: "v"(p), "s"(l) : "memory")
#define GLDS16S(o, p, l) asm volatile("s_mov_b32 m0, %2\n\ts_nop 0\n\tglobal_load_lds_dwordx4 %0, %1" :: "v"(o), "s"(p), "s"(l) : "memory")
#define WAITVM(n) asm volatile("s_waitcnt vmcnt(%0)" :: "i"(n) : "memory")
template <int H>
__device__ __forceinline__ void k1_ldw(const K1Ld& st) {
  const unsigned l = st.lds0 + K1_W0 + (H % 5) * K1_WS + st.lw * K1_WG;
  int krow = H * 32 + st.rot * 64;
  if (krow >= 640) krow -= 640;
#pragma unroll
  for (int j = 0; j < 6; ++j) {
    unsigned long long p = rowaddr(st.gp[j], st.pitch[j], krow);
    if (j < 5) GLDS16(p, l + j * 1024);
    else if (st.lane < 32) GLDS16(p, l + 5120);
  }
}
template <int T, int J0, int N>
__device__ __forceinline__ void k1_ldx(const K1Ld& st) {
  int kc = (T + st.rot) * 64;
  if (kc >= 640) kc -= 640;
#pragma unroll
  for (int j = J0; j < J0 + N; ++j)
    GLDS16S(st.xvoff[j & 1], st.xb + j * 8 * D_MODEL + kc, st.lds0 + (T % 3) * K1_XS + (st.lw * 4 + j) * 1024);
}
template <int P>
__device__ __forceinline__ void k1_lphase(const K1Ld& st) {
  k1_ldx<P / 2 + 2, (P & 1) * 2, 2>(st);
  k1_ldw<P + 4>(st);
  WAITVM(14);
  BARRIER();
  if constexpr (P + 1 < 15) k1_lphase<P + 1>(st);
}

__device__ __forceinline__ void k1_loader(char* smem, const u16* __restrict__ x, const u16* __restrict__ wqkv,
                                          const u16* __restrict__ wg, const u16* __restrict__ wu,
                                          const u16* __restrict__ wd, const u16* __restrict__ wo,
                                          const u16* __restrict__ gw, char* __restrict__ Wdf, char* __restrict__ Wof,
                                          float* __restrict__ G, unsigned* __restrict__ flags) {
  const int tid = threadIdx.x, lane = tid & 63;
  const int lw = __builtin_amdgcn_readfirstlane(tid >> 6) - 8;
  int mt, nt;
  k1_tile(blockIdx.x, mt, nt);
  K1Ld st;
  st.lw = lw;
  st.lane = lane;
#define K1_ROT 0
  {
    const int idx = blockIdx.x >> 3;
    st.rot = K1_ROT == 0 ? 0 : K1_ROT == 1 ? ((idx & 7) * 5 + (idx >> 3) * 10 / 4 * 4) / 4 % 10 : K1_ROT == 2 ? (idx & 7) : (idx * 10) >> 5;
  }
  st.lds0 = (unsigned)(size_t)smem;
  {
    const int xr = lane >> 3;
    st.xb = x + (size_t)mt * 128 * D_MODEL;
    st.xvoff[0] = ((lw * 32 + xr) * D_MODEL + ((lane & 7) ^ (xr >> 1)) * 8) * 2;
    st.xvoff[1] = st.xvoff[0] ^ 64;
    const int nq = min(max(52 - nt * 10, 0), 10), ngu = 10 - nq;
    const int qcol = nt < 2 ? 1280 + 320 * nt : 320 * nt - 640;
    const int gcol = 16 * (nt * 10 + nq - 52);
#pragma unroll
    for (int j = 0; j < 6; ++j) {
      const int pp = j * 64 + lane;
      const int row = pp / 44, cp = min(pp - row * 44, 41);
      const int k = 8 * lw + (row & 7);
      const int cq = cp - 4 * nq;
      if (cq < 0 || (cq >= 2 * ngu && cq < 2 * ngu + 2)) {
        const int col = cq < 0 ? qcol + cp * 8 : 1024 + 16 * nt + 8 * (cq - 2 * ngu);
        st.gp[j] = (unsigned long long)(wqkv + (size_t)k * 1920 + col);
        st.pitch[j] = 2 * 1920;
      } else {
        const int half = cq >= 2 * ngu;
        const int col = gcol + 8 * (cq - half * (2 * ngu + 2));
        st.gp[j] = (unsigned long long)((half ? wu : wg) + (size_t)k * F_DIM + col);
        st.pitch[j] = 2 * F_DIM;
      }
    }
  }
  k1_ldx<0, 0, 4>(st);
  k1_ldw<0>(st);
  k1_ldw<1>(st);
  k1_ldx<1, 0, 4>(st);
  k1_ldw<2>(st);
  k1_ldw<3>(st);
  WAITVM(16);
  BARRIER();
  k1_lphase<0>(st);
  k1_ldx<9, 2, 2>(st);
  k1_ldw<19>(st);
  const int item = blockIdx.x * 4 + lw;
  const bool has_item = item < 740;
  const int isd = item < 540;
  const int it2 = isd ? item : (has_item ? item - 540 : 0);
  const int ks = it2 / 10, n64 = it2 - ks * 10;
  u32x4 tw[4];
  {
    const u16* src = (isd ? wd : wo) + (size_t)(ks * 32 + (lane >> 3)) * D_MODEL + n64 * 64 + (lane & 7) * 8;
#pragma unroll
    for (int i = 0; i < 4; ++i) GLOAD(tw[i], src + i * 8 * D_MODEL);
  }
  const int ch2 = 64 + (lane & 15);
  u32x4 xv[2], xv2[2], wv, wv2;
#pragma unroll
  for (int r = 0; r < 2; ++r) {
    const u16* xr = x + (size_t)(item * 2 + r) * D_MODEL;
    GLOAD(xv[r], xr + lane * 8);
    GLOAD(xv2[r], xr + ch2 * 8);
  }
  GLOAD(wv, gw + lane * 8);
  GLOAD(wv2, gw + ch2 * 8);
  WAITVM(10 + 8 + 6);
  BARRIER();
  WAITVM(10 + 6);
  BARRIER();
  WAITVM(10);
  BARRIER();
  asm volatile("s_waitcnt vmcnt(0)" : "+v"(tw[0]), "+v"(tw[1]), "+v"(tw[2]), "+v"(tw[3]), "+v"(xv[0]), "+v"(xv[1]),
               "+v"(xv2[0]), "+v"(xv2[1]), "+v"(wv), "+v"(wv2));
  if (has_item) {
    char* tb = smem + K1_TR + lw * 5632;
#pragma unroll
    for (int i = 0; i < 4; ++i) *(u32x4*)(tb + i * 1408 + (lane >> 3) * 160 + (lane & 7) * 16) = tw[i];
    const int g = lane >> 4, c = lane & 15;
    const char* rp = tb + g * 1408 + (c >> 2) * 160 + (c & 3) * 8;
    char* dst = (isd ? Wdf : Wof) + ((size_t)(ks * 40 + n64 * 4) * 64 + lane) * 16;
#pragma unroll
    for (int nb = 0; nb < 4; ++nb) {
      bfx8 f = tr_frag(rp + nb * 32, rp + nb * 32 + 640);
      *(bfx8*)(dst + nb * 1024) = f;
    }
  }
#pragma unroll
  for (int r = 0; r < 2; ++r) {
    float s = 0.f;
#pragma unroll
    for (int e = 0; e < 4; ++e) {
      s += rbf(__uint_as_float(xv[r][e] << 16) * __uint_as_float(wv[e] << 16));
      s += rbf(__uint_as_float(xv[r][e] & 0xffff0000u) * __uint_as_float(wv[e] & 0xffff0000u));
    }
    if (lane < 16) {
#pragma unroll
      for (int e = 0; e < 4; ++e) {
        s += rbf(__uint_as_float(xv2[r][e] << 16) * __uint_as_float(wv2[e] << 16));
        s += rbf(__uint_as_float(xv2[r][e] & 0xffff0000u) * __uint_as_float(wv2[e] & 0xffff0000u));
      }
    }
#pragma unroll
    for (int o = 32; o >= 1; o >>= 1) s += __shfl_xor(s, o);
    s = rbf(s);
    float gv = rbf(1.0f / (1.0f + __expf(-s)));
    if (lane == 0) G[item * 2 + r] = gv;
  }
  if (lw == 0 && lane == 0) flags[(blockIdx.x >> 3) * FL_STR + (blockIdx.x & 7)] = 0u;
  if (item == 1 && lane < 4) flags[32 * FL_STR + lane] = 0x3f803f80u;
}

__global__ __launch_bounds__(768, 3) void k1_gemm(const u16* __restrict__ x, const u16* __restrict__ wqkv,
                                                  const u16* __restrict__ wg, const u16* __restrict__ wu,
                                                  const u16* __restrict__ wd, const u16* __restrict__ wo,
                                                  const u16* __restrict__ gw, char* __restrict__ Qf,
                                                  char* __restrict__ Vf, char* __restrict__ Hf,
                                                  char* __restrict__ Wdf, char* __restrict__ Wof,
                                                  float* __restrict__ G, unsigned* __restrict__ flags) {
  __shared__ __attribute__((aligned(16))) char smem[K1_LDS];
  const int w8 = __builtin_amdgcn_readfirstlane(threadIdx.x) >> 9;
  int mt, nt;
  k1_tile(blockIdx.x, mt, nt);
  if (w8) k1_loader(smem, x, wqkv, wg, wu, wd, wo, gw, Wdf, Wof, G, flags);
  else if (nt < 2) k1_compute<true>(smem, x, wqkv, wg, wu, Qf, Vf, Hf);
  else k1_compute<false>(smem, x, wqkv, wg, wu, Qf, Vf, Hf);
}

#define AT_K 15360
#define AT_VS 1296
#define AT_LDS (AT_K + 10 * AT_VS)
__device__ __forceinline__ void attn_stage_k(const char* __restrict__ Kf, unsigned lds, int h, int qb0, int lane) {
  const int slot0 = (20 * h) >> 3;
#pragma unroll
  for (int i = 0; i < 15; ++i) {
    const int p = i * 64 + lane;
    const int blk = p / 48, w = p - blk * 48;
    const int gb = max(qb0 - 8 + (blk >> 1), 0) * 2 + (blk & 1);
    GLDS16S(gb * 20480 + w * 16, Kf + slot0 * 256, lds + i * 1024);
  }
}
template <int I0, int I1>
__device__ __forceinline__ void attn_stage_v(const char* __restrict__ Vf, unsigned lds, int h, int qb0, int lane,
                                             unsigned ones) {
#pragma unroll
  for (int i = I0; i < I1; ++i) {
    const int p = i * 64 + lane;
    const int grp = p / 81, w = p - grp * 81;
    const int gg = max(qb0 - 8 + grp, 0);
    const unsigned o = w < 80 ? (unsigned)(gg * 1280 + w * 16) : ones - h * 81920;
    if (i < 12 || lane < 42) GLDS16S(o, Vf + (size_t)h * 81920, lds + AT_K + i * 1024);
  }
}
#define SB() __builtin_amdgcn_sched_barrier(0)
__device__ __forceinline__ f32x16 zero16() {
  return f32x16{0, 0, 0, 0, 0, 0, 0, 0, 0, 0, 0, 0, 0, 0, 0, 0};
}
__device__ __forceinline__ float at_fma(float a, float cs, float nmc) {
  float t = fmaf(a, cs, nmc);
  asm volatile("" : "+v"(t));
  return t;
}
template <bool FIRST>
__device__ __forceinline__ void at_max(const f32x16& t, float (&mx)[4]) {
#pragma unroll
  for (int r = 0; r < 4; ++r) {
    mx[r] = FIRST ? fmaxf(t[r], t[4 + r]) : fmaxf(fmaxf(mx[r], t[r]), t[4 + r]);
    mx[r] = fmaxf(fmaxf(mx[r], t[8 + r]), t[12 + r]);
    asm volatile("" : "+v"(mx[r]));
  }
}
__device__ __forceinline__ void at_neg(f32x16& t, bool c) {
#pragma unroll
  for (int i = 0; i < 16; ++i) t[i] = c ? -1e30f : t[i];
}
template <bool EXP, bool QK, bool EDGE>
__device__ __forceinline__ void at_stage(f32x16 (&s)[8], float& mc, f32x16& o, bfx8 q1, bfx8 q2, const char* kp1,
                                         const char* kp2, const char* vpa, const char* vpb, const unsigned (&mk)[8],
                                         int jmin, int dq) {
  const float cs = 0.22360679774997896f * 1.4426950408889634f;
  const float nmc = -mc;
  float mx[4];
  f32x16 sx;
  u32x4 pk[2], pp[2];
  bfx8 ka, kb, kbp, xa, xb;
  bfx8 va, vb, wa, wb;
  if constexpr (QK) { xa = *(const bfx8*)kp1; xb = *(const bfx8*)kp2; }
  if constexpr (EXP) o = zero16();
#pragma unroll
  for (int u = 0; u < 8; ++u) {
    const int j = u == 0 ? 8 : u;
    SB();
    float t[4];
#define AT_F(c4) if constexpr (EXP) { _Pragma("unroll") for (int e = 0; e < 4; ++e) t[e] = at_fma(s[u][4 * (c4) + e], cs, nmc); }
#define AT_E(c4, h2) if constexpr (EXP) { pk[(c4) >> 1][2 * ((c4) & 1) + (h2)] = pack2(__builtin_amdgcn_exp2f(t[2 * (h2)]), __builtin_amdgcn_exp2f(t[2 * (h2) + 1])); }
    if constexpr (QK) {
      if (u == 0) sx = mfma32(xa, q1, zero16());
      else s[u - 1] = mfma32(kbp, q2, s[u - 1]);
    }
    SB();
    AT_F(0); AT_E(0, 0); AT_E(0, 1);
    SB();
    if constexpr (QK) { if (u == 0) sx = mfma32(xb, q2, sx); }
    if constexpr (EXP) {
      if (u == 1) {
        u32x4 m8;
#pragma unroll
        for (int e = 0; e < 4; ++e) m8[e] = pp[0][e] & mk[e];
        o = mfma32(va, __builtin_bit_cast(bfx8, m8), o);
      } else if (u > 1) o = mfma32(va, __builtin_bit_cast(bfx8, pp[0]), o);
    }
    SB();
    if constexpr (QK) { ka = *(const bfx8*)(kp1 + j * 1536); kb = *(const bfx8*)(kp2 + j * 1536); }
    AT_F(1); AT_E(1, 0);
    if constexpr (EXP) {
      if (u == 1) {
        u32x4 m8;
#pragma unroll
        for (int e = 0; e < 4; ++e) m8[e] = pp[0][e] & ~mk[e];
        SB();
        o = mfma32(wa, __builtin_bit_cast(bfx8, m8), o);
        SB();
      }
    }
    AT_E(1, 1);
    SB();
    if constexpr (EXP) {
      if (u == 1) {
        u32x4 m8;
#pragma unroll
        for (int e = 0; e < 4; ++e) m8[e] = pp[1][e] & mk[4 + e];
        o = mfma32(vb, __builtin_bit_cast(bfx8, m8), o);
      } else if (u > 1) o = mfma32(vb, __builtin_bit_cast(bfx8, pp[1]), o);
    }
    SB();
    AT_F(2); AT_E(2, 0);
    if constexpr (EXP) {
      if (u == 1) {
        u32x4 m8;
#pragma unroll
        for (int e = 0; e < 4; ++e) m8[e] = pp[1][e] & ~mk[4 + e];
        SB();
        o = mfma32(wb, __builtin_bit_cast(bfx8, m8), o);
        SB();
      }
    }
    AT_E(2, 1);
    if constexpr (QK) {
      if (u == 1) {
        if constexpr (EDGE) at_neg(sx, jmin > 0);
#pragma unroll
        for (int i = 0; i < 16; ++i) s[0][i] = 16 * (i >> 3) + (i & 7) <= dq ? s[0][i] : sx[i];
        at_max<true>(s[0], mx);
      } else if (u > 1) {
        if constexpr (EDGE) at_neg(s[u - 1], u - 1 < jmin);
        at_max<false>(s[u - 1], mx);
      }
    }
    AT_F(3);
    SB();
    if constexpr (QK) s[u] = mfma32(ka, q1, zero16());
    SB();
    if constexpr (EXP) {
      va = *(const bfx8*)(vpa + j * AT_VS);
      if (u == 0) wa = *(const bfx8*)vpa;
      vb = *(const bfx8*)(vpb + j * AT_VS);
      if (u == 0) wb = *(const bfx8*)vpb;
    }
    AT_E(3, 0); AT_E(3, 1);
    kbp = kb;
    pp[0] = pk[0]; pp[1] = pk[1];
  }
  SB();
  if constexpr (QK) s[7] = mfma32(kbp, q2, s[7]);
  if constexpr (EXP) {
    o = mfma32(va, __builtin_bit_cast(bfx8, pp[0]), o);
    o = mfma32(vb, __builtin_bit_cast(bfx8, pp[1]), o);
  }
  if constexpr (QK) {
    if constexpr (EDGE) at_neg(s[7], 7 < jmin);
    at_max<false>(s[7], mx);
    float m = fmaxf(fmaxf(mx[0], mx[1]), fmaxf(mx[2], mx[3]));
    u32x2 r32 = __builtin_amdgcn_permlane32_swap(__float_as_uint(m), __float_as_uint(m), false, false);
    m = fmaxf(__uint_as_float(r32[0]), __uint_as_float(r32[1]));
    mc = m * cs;
  }
}
template <bool EDGE>
__device__ __forceinline__ void at_first(f32x16 (&s)[8], float& mc, bfx8 q1, bfx8 q2, const char* kp1, const char* kp2,
                                         int jmin, int dq, const char* __restrict__ Vf, unsigned lds, int h, int qb0,
                                         int lane, unsigned ones) {
  const float cs = 0.22360679774997896f * 1.4426950408889634f;
  float mx[4];
  f32x16 sx;
  bfx8 ka[9], kb[9];
#pragma unroll
  for (int p = 0; p < 6; ++p) {
    if (p >= 1 && p <= 3) WAITVM(12);
    if (p == 4) WAITVM(13);
#pragma unroll
    for (int j = 2 * p; j < 2 * p + 2; ++j)
      if (j < 9) { ka[j] = *(const bfx8*)(kp1 + j * 1536); kb[j] = *(const bfx8*)(kp2 + j * 1536); }
    SB();
#pragma unroll
    for (int j = 2 * p - 2; j < 2 * p; ++j)
      if (j >= 0 && j < 9) {
        f32x16 t = mfma32(kb[j], q2, mfma32(ka[j], q1, zero16()));
        if (j == 0) sx = t; else s[j & 7] = t;
      }
#pragma unroll
    for (int j = 2 * p - 4; j < 2 * p - 2; ++j)
      if (j >= 1) {
        if constexpr (EDGE) at_neg(s[j], j < jmin);
        if (j == 1) at_max<true>(s[j], mx); else at_max<false>(s[j], mx);
      }
    SB();
    if (p == 0) attn_stage_v<0, 3>(Vf, lds, h, qb0, lane, ones);
    if (p == 1) attn_stage_v<3, 6>(Vf, lds, h, qb0, lane, ones);
    if (p == 2) attn_stage_v<6, 9>(Vf, lds, h, qb0, lane, ones);
    if (p == 3) attn_stage_v<9, 12>(Vf, lds, h, qb0, lane, ones);
    if (p == 4) { attn_stage_v<12, 13>(Vf, lds, h, qb0, lane, ones); BARRIER0(); }
    SB();
  }
  if constexpr (EDGE) at_neg(sx, jmin > 0);
#pragma unroll
  for (int i = 0; i < 16; ++i) s[0][i] = 16 * (i >> 3) + (i & 7) <= dq ? s[0][i] : sx[i];
  at_max<false>(s[0], mx);
  float m = fmaxf(fmaxf(mx[0], mx[1]), fmaxf(mx[2], mx[3]));
  u32x2 r32 = __builtin_amdgcn_permlane32_swap(__float_as_uint(m), __float_as_uint(m), false, false);
  m = fmaxf(__uint_as_float(r32[0]), __uint_as_float(r32[1]));
  mc = m * cs;
}
__device__ __forceinline__ void at_store(const f32x16& o, float gq, char* __restrict__ dst, int k0, int hh) {
  u32x2 r32 = __builtin_amdgcn_permlane32_swap(__float_as_uint(o[8]), __float_as_uint(o[8]), false, false);
  const float inv = gq * __builtin_amdgcn_rcpf(__uint_as_float(r32[1]));
#pragma unroll
  for (int q = 0; q < 3; ++q) {
    const int k = k0 + 8 * q;
    u32x2 v = {pack2(o[4 * q] * inv, o[4 * q + 1] * inv), pack2(o[4 * q + 2] * inv, o[4 * q + 3] * inv)};
    if (q < 2 || hh == 0) st8b(dst + (k >> 3) * 256 + ((k >> 2) & 1) * 8, v);
  }
}
template <bool EDGE>
__device__ __forceinline__ void attn_run(const u32x4 (&qv)[4], const float (&gq)[2], const char* al, char* __restrict__ Of,
                                         int h, int qb0, int lane, const char* __restrict__ Vf, unsigned ones) {
  asm volatile("" : "+v"(lane));
  const int hh = lane >> 5, n = lane & 31;
  bfx8 qf[4];
#pragma unroll
  for (int t = 0; t < 4; ++t) {
    u32x4 v = qv[t];
    if ((t & 1) && (hh || !(h & 1))) { v[2] = 0; v[3] = 0; }
    if ((t & 1) ? hh : (!hh && (h & 1))) { v[0] = 0; v[1] = 0; }
    qf[t] = __builtin_bit_cast(bfx8, v);
  }
  const char* kp2 = al + ((n >> 3) & 1) * 768 + (8 * (n >> 4) + (n & 7)) * 16;
  const char* kp1 = kp2 + hh * 256;
  kp2 += 512;
  const char* vpa = al + AT_K + (n < 16 ? hh * 256 + n * 16 : n < 20 ? 1024 + hh * 64 + (n - 16) * 16 : 1280);
  const char* vpb = vpa + (n < 16 ? 512 : n < 20 ? 128 : 0);
  const int dq = n - 8 * hh;
  unsigned mk[8];
#pragma unroll
  for (int t = 0; t < 8; ++t) {
    const int ko = 16 * (t >> 2) + 2 * (t & 3);
    mk[t] = (ko <= dq ? 0xffffu : 0u) | (ko + 1 <= dq ? 0xffff0000u : 0u);
  }
  const int jm = 8 - qb0;
  char* dst = Of + (size_t)(qb0 * 2 + (n >> 4)) * 20480 + (n & 15) * 16;
  const int k0 = h * 20 + 4 * hh;
  f32x16 s[8], o;
  float mc = 0.f;
#define AST(i)
  AST(0);
  at_first<EDGE>(s, mc, qf[0], qf[1], kp1, kp2, jm, dq, Vf, (unsigned)(size_t)al, h, qb0, lane, ones);
  AST(1);
  WAITVM(0);
  AST(2);
  at_stage<true, true, EDGE>(s, mc, o, qf[2], qf[3], kp1 + 1536, kp2 + 1536, vpa, vpb, mk, jm - 1, dq);
  AST(3);
  at_store(o, gq[0], dst, k0, hh);
  AST(4);
  at_stage<true, false, EDGE>(s, mc, o, qf[2], qf[3], kp1, kp2, vpa + AT_VS, vpb + AT_VS, mk, jm, dq);
  AST(5);
  at_store(o, gq[1], dst + 40960, k0, hh);
  AST(6);
  AST(7);
}

#define GLOADSC(r, o, p) asm volatile("global_load_dwordx4 %0, %1, %2 sc1" : "=v"(r) : "v"(o), "s"(p))
struct G2Frag { u32x4 a[4], w[5]; };
__device__ __forceinline__ void g2_issue_w(G2Frag& f, const char* Wb, int ks, int voff) {
  const char* wp = Wb + (size_t)ks * 40960 + 2048;
  GLOADSO(f.w[0], voff, wp, -2048);
  GLOADSO(f.w[1], voff, wp, -1024);
  GLOADSO(f.w[2], voff, wp, 0);
  GLOADSO(f.w[3], voff, wp, 1024);
  GLOADSO(f.w[4], voff, wp, 2048);
}
__device__ __forceinline__ void g2_issue_h(G2Frag& f, const char* Ab, int ks, int voff, int wave) {
  const char* ap = Ab + (size_t)ks * 1024;
#pragma unroll
  for (int i = 0; i < 4; ++i) GLOADS(f.a[i], voff, ap + (size_t)((wave + i) & 3) * 54 * 1024);
}
__device__ __forceinline__ void g2_issue_o(G2Frag& f, const char* Ab, int ks, int voff, int wave) {
  const char* ap = Ab + (size_t)ks * 1024;
#pragma unroll
  for (int i = 0; i < 4; ++i) GLOADSC(f.a[i], voff, ap + (size_t)((wave + i) & 3) * 20 * 1024);
}
template <int N>
__device__ __forceinline__ void g2_wait(G2Frag& f) {
  asm volatile("s_waitcnt vmcnt(%9)"
               : "+v"(f.a[0]), "+v"(f.a[1]), "+v"(f.a[2]), "+v"(f.a[3]), "+v"(f.w[0]), "+v"(f.w[1]), "+v"(f.w[2]),
                 "+v"(f.w[3]), "+v"(f.w[4])
               : "i"(N));
}
struct G2Ctx {
  const char *Hb, *Wdb, *Ob, *Wob;
  const unsigned* fl;
  const float* gp;
  int wave, voff;
  float omg[4];
  unsigned fv;
};
#define GST(i)
#define G2_PD 4
struct G2Seq { int wait[19], poll; };
constexpr G2Seq g2_seq() {
  G2Seq q{};
  int tag[256] = {}, n = 0;
  for (int s = 0; s < G2_PD; ++s) for (int i = 0; i < 9; ++i) tag[n++] = s;
  for (int t = 0; t < 19; ++t) {
    int last = 0;
    for (int i = 0; i < n; ++i) if (tag[i] == t) last = i;
    q.wait[t] = n - 1 - last;
    const int T = t + G2_PD;
    if (t == 12) for (int i = 0; i < 5; ++i) tag[n++] = -1;
    if (t < 13) for (int i = 0; i < (T <= 13 ? 9 : 5); ++i) tag[n++] = T;
    if (t == 13) {
      for (int u = T; u <= 17; ++u) for (int i = 0; i < 5; ++i) tag[n++] = u;
      for (int i = 0; i < n; ++i) if (tag[i] == -1) last = i;
      q.poll = n - 1 - last;
      for (int u = 14; u <= 17; ++u) for (int i = 0; i < 4; ++i) tag[n++] = u;
    }
    if (t == 14) for (int i = 0; i < 9; ++i) tag[n++] = 18;
  }
  return q;
}
template <int S>
__device__ __forceinline__ void g2_steps(G2Frag (&f)[4], f32x4 (&acc)[5][4], G2Ctx& cx) {
  constexpr G2Seq Q = g2_seq();
  constexpr int T = S + G2_PD;
  G2Frag& cur = f[S & 3];
  g2_wait<Q.wait[S]>(cur);
  GST(S + 1);
  if (S != 13 || cx.wave < 2) {
#pragma unroll
    for (int u = 0; u < 5; ++u)
#pragma unroll
      for (int i = 0; i < 4; ++i)
        asm volatile("v_mfma_f32_16x16x32_bf16 %0, %1, %2, %0" : "+v"(acc[u][i]) : "v"(cur.w[u]), "v"(cur.a[i]));
  }
  if constexpr (S == 13 || S == 18) asm volatile("s_nop 7\n\ts_nop 7" ::: "memory");
  __builtin_amdgcn_sched_barrier(0);
  if constexpr (S == 12) {
    const int go = (cx.voff >> 2) & 60;
#pragma unroll
    for (int i = 0; i < 4; ++i)
      asm volatile("global_load_dword %0, %1, %2" : "=v"(cx.omg[i]) : "v"(go), "s"(cx.gp + ((cx.wave + i) & 3) * 16));
    asm volatile("global_load_dword %0, %1, %2 sc1" : "=v"(cx.fv) : "v"(go & 28), "s"(cx.fl));
  }
  if constexpr (S < 13) {
    if constexpr (T <= 13) {
      const int ks = min(T * 4 + cx.wave, 53);
      g2_issue_h(f[T & 3], cx.Hb, ks, cx.voff, cx.wave);
      g2_issue_w(f[T & 3], cx.Wdb, ks, cx.voff);
    } else {
      g2_issue_w(f[T & 3], cx.Wob, (T - 14) * 4 + cx.wave, cx.voff);
    }
  } else if constexpr (S == 13) {
#pragma unroll
    for (int u = T; u <= 17; ++u) g2_issue_w(f[u & 3], cx.Wob, (u - 14) * 4 + cx.wave, cx.voff);
    asm volatile("s_waitcnt vmcnt(%5)" : "+v"(cx.fv), "+v"(cx.omg[0]), "+v"(cx.omg[1]), "+v"(cx.omg[2]), "+v"(cx.omg[3]) : "i"(Q.poll));
#pragma unroll 1
    for (int it = 0; it < (1 << 20) && __builtin_amdgcn_ballot_w64(cx.fv != 1u) != 0; ++it) {
      __builtin_amdgcn_s_sleep(FL_SLP);
      asm volatile("global_load_dword %0, %1, %2 sc1\n\ts_waitcnt vmcnt(0)" : "=v"(cx.fv) : "v"((cx.voff >> 2) & 28), "s"(cx.fl) : "memory");
    }
    asm volatile("" ::: "memory");
    GST(22);
#pragma unroll
    for (int j = 0; j < 4; ++j) g2_issue_o(f[(14 + j) & 3], cx.Ob, j * 4 + cx.wave, cx.voff, cx.wave);
#pragma unroll
    for (int u = 0; u < 5; ++u)
#pragma unroll
      for (int i = 0; i < 4; ++i) {
        acc[u][i] *= rbf(1.0f - cx.omg[i]);
        asm volatile("" : "+v"(acc[u][i]));
      }
    __builtin_amdgcn_sched_barrier(0);
    asm volatile("s_nop 3" ::: "memory");
  } else if constexpr (S == 14) {
    g2_issue_o(cur, cx.Ob, 16 + cx.wave, cx.voff, cx.wave);
    g2_issue_w(cur, cx.Wob, 16 + cx.wave, cx.voff);
  }
  if constexpr (S + 1 < 19) g2_steps<S + 1>(f, acc, cx);
}
__device__ __forceinline__ void g2_tile(int bid, int& mt, int& nt) {
  const int xcd = bid & 7, idx = bid >> 3;
  mt = (xcd >> 1) * 8 + (idx >> 2);
  nt = (xcd & 1) * 4 + (idx & 3);
}

__global__ __launch_bounds__(512, 2) void k2_mix(const char* __restrict__ Qf, const char* __restrict__ Kf,
                                                 const char* __restrict__ Vf, char* Of,
                                                 const char* __restrict__ Hf, const char* __restrict__ Wdf,
                                                 const char* __restrict__ Wof, const float* __restrict__ G,
                                                 unsigned* flags, u16* __restrict__ out) {
  __shared__ __attribute__((aligned(16))) char smem[4 * AT_LDS];
  __shared__ unsigned k2_cnt;
  const int tid = threadIdx.x, lane = tid & 63;
  const int wave = __builtin_amdgcn_readfirstlane(tid >> 6) ^ 4;
  const int bid = blockIdx.x;
  int mt, nt;
  g2_tile(bid, mt, nt);
  if (wave >= 4) {
    float gq[2];
    {
      const float* gp = G + mt * 64 + (lane & 31);
#pragma unroll
      for (int i = 0; i < 2; ++i) GLOAD1(gq[i], gp + i * 32);
    }
    const int h = nt * 4 + (wave - 4);
    const int qb0 = mt * 2;
    if (wave == 4) ST(0, 0);
    char* al = smem + (wave - 4) * AT_LDS;
    u32x4 qv[4];
    {
      const char* qp = Qf + (size_t)(qb0 * 2 + ((lane >> 4) & 1)) * 20480 + ((20 * h) >> 3) * 256 + (lane & 15) * 16;
#pragma unroll
      for (int i = 0; i < 4; ++i) GLOAD(qv[i], qp + (i >> 1) * 40960 + ((i & 1) ? 512 : (lane >> 5) * 256));
    }
    attn_stage_k(Kf, (unsigned)(size_t)al, h, qb0, lane);
    if (tid == 0) k2_cnt = 0u;
    asm volatile("s_waitcnt vmcnt(12)" : "+v"(qv[0]), "+v"(qv[1]), "+v"(qv[2]), "+v"(qv[3]), "+v"(gq[0]),
                 "+v"(gq[1]) :: "memory");
    if (wave == 4) ST(0, 1);
    const unsigned ones = (unsigned)((const char*)(flags + 32 * FL_STR) - Vf);
    if (qb0 < 8) attn_run<true>(qv, gq, al, Of, h, qb0, lane, Vf, ones);
    else attn_run<false>(qv, gq, al, Of, h, qb0, lane, Vf, ones);
    if (wave == 4) ST(0, 2);
    if (wave == 4) ST(0, 3);
    WAITVM(0);
    if (lane == 0) {
      unsigned old = __hip_atomic_fetch_add(&k2_cnt, 1u, __ATOMIC_RELAXED, __HIP_MEMORY_SCOPE_WORKGROUP);
      if (old == 3u) __hip_atomic_store(flags + mt * FL_STR + nt, 1u, __ATOMIC_RELAXED, __HIP_MEMORY_SCOPE_AGENT);
    }
    if (wave == 4) ST(0, 4);
    return;
  }
  f32x4 acc[5][4];
#pragma unroll
  for (int u = 0; u < 5; ++u)
#pragma unroll
    for (int i = 0; i < 4; ++i) {
      acc[u][i] = f32x4{0, 0, 0, 0};
      asm volatile("" : "+v"(acc[u][i]));
    }
  G2Ctx cx;
  cx.Hb = Hf + (size_t)mt * 4 * 54 * 1024;
  cx.Wdb = Wdf + (size_t)nt * 5120;
  cx.Ob = Of + (size_t)mt * 4 * 20 * 1024;
  cx.Wob = Wof + (size_t)nt * 5120;
  cx.fl = flags + mt * FL_STR;
  cx.gp = G + mt * 64;
  cx.wave = wave;
  cx.voff = lane * 16;
  BARRIER0();
  GST(0);
  G2Frag f[4];
#pragma unroll
  for (int s = 0; s < G2_PD; ++s) {
    g2_issue_h(f[s], cx.Hb, s * 4 + wave, cx.voff, wave);
    g2_issue_w(f[s], cx.Wdb, s * 4 + wave, cx.voff);
  }
  g2_steps<0>(f, acc, cx);
  GST(20);
  f32x4* red = (f32x4*)smem;
#pragma unroll
  for (int i = 1; i < 4; ++i)
#pragma unroll
    for (int u = 0; u < 5; ++u) red[((((wave + i) & 3) * 3 + i - 1) * 5 + u) * 64 + lane] = acc[u][i];
  BARRIER();
  const int tok = mt * 64 + wave * 16 + (lane & 15);
#pragma unroll
  for (int u = 0; u < 5; ++u) {
    f32x4 v = acc[u][0];
#pragma unroll
    for (int i = 0; i < 3; ++i) v += red[((wave * 3 + i) * 5 + u) * 64 + lane];
    u32x2 pv = {pack2(v[0], v[1]), pack2(v[2], v[3])};
    *(u32x2*)(out + (size_t)tok * D_MODEL + nt * 80 + u * 16 + 4 * (lane >> 4)) = pv;
  }
  GST(21);
}

extern "C" void kernel_launch(void* const* d_in, const int* in_sizes, int n_in,
                              void* d_out, int out_size, void* d_ws, size_t ws_size,
                              hipStream_t stream) {
  const u16* x = (const u16*)d_in[0];
  const u16* wqkv = (const u16*)d_in[1];
  const u16* wo = (const u16*)d_in[2];
  const u16* wg = (const u16*)d_in[4];
  const u16* wu = (const u16*)d_in[5];
  const u16* wd = (const u16*)d_in[6];
  const u16* gw = (const u16*)d_in[7];
  char* ws = (char*)d_ws;
  const size_t QB = (size_t)S_LEN * D_MODEL * 2;
  char* Qf = ws;
  char* Kf = Qf + QB;
  char* Vf = Kf + QB;
  char* Hf = ws + (12u << 20);
  char* Of = ws + (20u << 20);
  char* Wdf = ws + (28u << 20);
  char* Wof = ws + (31u << 20);
  float* G = (float*)(ws + (32u << 20));
  unsigned* flags = (unsigned*)(ws + (33u << 20));
  u16* out = (u16*)d_out;

  k1_gemm<<<256, 768, 0, stream>>>(x, wqkv, wg, wu, wd, wo, gw, Qf, Vf, Hf, Wdf, Wof, G, flags);
  k2_mix<<<256, 512, 0, stream>>>(Qf, Kf, Vf, Of, Hf, Wdf, Wof, G, flags, out);
}
```

```cpp
#include <hip/hip_runtime.h>
#include <hip/hip_bf16.h>
#include <stdint.h>

typedef __attribute__((ext_vector_type(8))) __bf16 bfx8;
typedef __attribute__((ext_vector_type(2))) __bf16 bfx2;
typedef __attribute__((ext_vector_type(2))) float f32x2;
typedef __attribute__((ext_vector_type(4))) short s16x4;
typedef __attribute__((ext_vector_type(8))) short s16x8;
typedef __attribute__((ext_vector_type(4))) float f32x4;
typedef __attribute__((ext_vector_type(16))) float f32x16;
typedef __attribute__((ext_vector_type(2))) unsigned u32x2;
typedef __attribute__((ext_vector_type(4))) unsigned u32x4;
typedef __attribute__((ext_vector_type(4), aligned(8))) unsigned u32x4a8;
typedef __attribute__((address_space(3))) s16x4* lds_s16x4_ptr;
typedef unsigned short u16;

#define FL_STR 64
#define FL_SLP 2
#define S_LEN 2048
#define D_MODEL 640
#define F_DIM 1728

__device__ __forceinline__ float bf2f(u16 v) { return __uint_as_float(((unsigned)v) << 16); }
__device__ __forceinline__ u16 f2bf(float f) {
  __hip_bfloat16 b = __float2bfloat16(f);
  return __builtin_bit_cast(u16, b);
}
__device__ __forceinline__ float rbf(float f) { return bf2f(f2bf(f)); }
__device__ __forceinline__ unsigned pack2(float lo, float hi) {
  bfx2 r = __builtin_convertvector(f32x2{lo, hi}, bfx2);
  return __builtin_bit_cast(unsigned, r);
}
__device__ __forceinline__ f32x4 mfma16(bfx8 a, bfx8 b, f32x4 c) {
  return __builtin_amdgcn_mfma_f32_16x16x32_bf16(a, b, c, 0, 0, 0);
}
__device__ __forceinline__ f32x16 mfma32(bfx8 a, bfx8 b, f32x16 c) {
  return __builtin_amdgcn_mfma_f32_32x32x16_bf16(a, b, c, 0, 0, 0);
}
__device__ __forceinline__ bfx8 tr_frag(const char* p0, const char* p1) {
  s16x4 a = __builtin_amdgcn_ds_read_tr16_b64_v4i16((lds_s16x4_ptr)p0);
  s16x4 b = __builtin_amdgcn_ds_read_tr16_b64_v4i16((lds_s16x4_ptr)p1);
  s16x8 v = {a[0], a[1], a[2], a[3], b[0], b[1], b[2], b[3]};
  return __builtin_bit_cast(bfx8, v);
}
#define STPOL 2
#define STPOLB 2
template <int POL>
__device__ __forceinline__ void st8p(char* p, u32x2 v) {
  if constexpr (POL == 0) *(u32x2*)p = v;
  else if constexpr (POL == 1) __builtin_nontemporal_store(v, (u32x2*)p);
  else if constexpr (POL == 2) asm volatile("global_store_dwordx2 %0, %1, off sc1" :: "v"(p), "v"(v) : "memory");
  else asm volatile("global_store_dwordx2 %0, %1, off sc0 sc1" :: "v"(p), "v"(v) : "memory");
}
__device__ __forceinline__ void st8(char* p, u32x2 v) { st8p<STPOL>(p, v); }
__device__ __forceinline__ void st8b(char* p, u32x2 v) { st8p<STPOLB>(p, v); }
#define GLOAD(r, p) asm volatile("global_load_dwordx4 %0, %1, off" : "=v"(r) : "v"(p))
#define GLOADS(r, o, p) asm volatile("global_load_dwordx4 %0, %1, %2" : "=v"(r) : "v"(o), "s"(p))
#define GLOADSO(r, o, p, imm) asm volatile("global_load_dwordx4 %0, %1, %2 offset:%3" : "=v"(r) : "v"(o), "s"(p), "i"(imm))
#define GLOAD1(r, p) asm volatile("global_load_dword %0, %1, off" : "=v"(r) : "v"(p))
#define WAITV(n, r) asm volatile("s_waitcnt vmcnt(%1)" : "+v"(r) : "i"(n))
__device__ __forceinline__ void bump(const u16*& p, int stride) {
  unsigned lo = (unsigned)(size_t)p, hi = (unsigned)((size_t)p >> 32);
  asm volatile("v_add_co_u32 %0, vcc, %0, %2\n\tv_addc_co_u32 %1, vcc, 0, %1, vcc" : "+v"(lo), "+v"(hi) : "v"(stride) : "vcc");
  p = (const u16*)(((size_t)hi << 32) | lo);
}
#define BARRIER0() asm volatile("s_barrier" ::: "memory")
#define BARRIER() asm volatile("s_waitcnt lgkmcnt(0)\n\ts_barrier" ::: "memory")
#define ST(role, i)
#define RT(i)

struct Ws {
  char *Qf, *Kf, *Vf, *Hf, *Of, *Wdf, *Wof, *Bb;
  float* G;
};

#define K1_XS 16384
#define K1_WP 704
#define K1_WG (8 * K1_WP + 32)
#define K1_WS (4 * K1_WG)
#define K1_W0 (3 * K1_XS)
#define K1_TR K1_W0
#define K1_LDS (K1_W0 + 5 * K1_WS + 256)

__device__ __forceinline__ void k1_tile(int bid, int& mt, int& nt) {
  const int xcd = bid & 7, idx = bid >> 3;
  mt = (xcd >> 2) * 8 + (idx & 7);
  nt = (xcd & 3) * 4 + (idx >> 3);
}

#define CST(i)

#define K1_PD 2
struct K1C {
  f32x4 acc[5][4], accs;
  int wa[6], wah[6];
  int xa[2][4];
  bfx8 wf[K1_PD + 1], xf[2][4];
};
template <int F>
__device__ __forceinline__ bfx8 k1_wfrag(const char* smem, const K1C& st) {
  constexpr int P = F / 6, u = F % 6, sl = P % 5;
  constexpr int off = (sl < 3 ? sl : sl - 3) * K1_WS;
  const char* b = smem + (sl < 3 ? st.wa[u] : st.wah[u]);
  return tr_frag(b + off, b + off + 4 * K1_WP);
}
template <bool UNSW, int F>
__device__ __forceinline__ void k1_steps(const char* smem, K1C& st, int wave, int lane) {
  constexpr int P = F / 6, u = F % 6;
  if constexpr (F + K1_PD < 120) st.wf[(F + K1_PD) % (K1_PD + 1)] = k1_wfrag<F + K1_PD>(smem, st);
  if constexpr (u < 4 && P < 19)
    st.xf[(P + 1) & 1][u] = *(const bfx8*)(smem + st.xa[(P + 1) & 1][u] + (((P + 1) >> 1) % 3) * K1_XS);
  if constexpr (u < 5) {
#pragma unroll
    for (int i = 0; i < 4; ++i) {
      if constexpr (UNSW) st.acc[u][i] = mfma16(st.xf[P & 1][i], st.wf[F % (K1_PD + 1)], st.acc[u][i]);
      else st.acc[u][i] = mfma16(st.wf[F % (K1_PD + 1)], st.xf[P & 1][i], st.acc[u][i]);
    }
  } else {
    st.accs = mfma16(st.wf[F % (K1_PD + 1)], st.xf[P & 1][0], st.accs);
  }
  __builtin_amdgcn_sched_barrier(0);
  if constexpr (u == 5 && P < 18) {
    if constexpr (P & 1) { CST(P + 1); }
    BARRIER0();
    if constexpr (P & 1) { CST(P + 2); }
  }
  if constexpr (F + 1 < 120) k1_steps<UNSW, F + 1>(smem, st, wave, lane);
}

__device__ __forceinline__ float silu_mul(float g, float u) {
  return g * u * __builtin_amdgcn_rcpf(1.0f + __builtin_amdgcn_exp2f(g * -1.4426950408889634f));
}

template <bool UNSW>
__device__ __forceinline__ void k1_compute(char* smem, char* __restrict__ Qf, char* __restrict__ Vf,
                                           char* __restrict__ Hf) {
  const int tid = threadIdx.x, lane = tid & 63;
  const int wave = __builtin_amdgcn_readfirstlane(tid >> 6);
  int mt, nt;
  k1_tile(blockIdx.x, mt, nt);
  const int wm = wave & 1, wn = wave >> 1;
  const int g = lane >> 4, c = lane & 15, hh = lane >> 5;
  const int nq = min(max(52 - nt * 10, 0), 10), ngu = 10 - nq;
  K1C st;
  {
    const int x0 = (wm * 64 + c) * 128 + ((g ^ (c >> 1)) << 4);
#pragma unroll
    for (int i = 0; i < 4; ++i) {
      st.xa[0][i] = x0 + ((wn + i) & 3) * 2048;
      st.xa[1][i] = st.xa[0][i] ^ 64;
      asm volatile("" : "+v"(st.xa[0][i]), "+v"(st.xa[1][i]));
    }
    const int rowp = K1_W0 + g * K1_WG + (c >> 2) * K1_WP;
#pragma unroll
    for (int u = 0; u < 5; ++u) {
      const int v = wn * 5 + u;
      st.wa[u] = rowp + (v < 2 * nq ? v * 32 + (c & 3) * 8
                                    : nq * 32 + v * 16 + (c & 1) * 8 + ((c & 2) ? ngu * 32 + 48 : 0));
    }
    st.wa[5] = rowp + (c & 3) * 8 + nq * 64 + ngu * 32;
#pragma unroll
    for (int u = 0; u < 6; ++u) {
      st.wah[u] = st.wa[u] + 3 * K1_WS;
      asm volatile("" : "+v"(st.wa[u]), "+v"(st.wah[u]));
    }
  }
#pragma unroll
  for (int u = 0; u < 5; ++u)
#pragma unroll
    for (int i = 0; i < 4; ++i) st.acc[u][i] = f32x4{0, 0, 0, 0};
  st.accs = f32x4{0, 0, 0, 0};
  CST(0);
  BARRIER();
  CST(1);
  st.wf[0] = k1_wfrag<0>(smem, st);
#pragma unroll
  for (int i = 0; i < 4; ++i) st.xf[0][i] = *(const bfx8*)(smem + st.xa[0][i]);
  st.wf[1] = k1_wfrag<1>(smem, st);
  if constexpr (K1_PD > 2) st.wf[2] = k1_wfrag<2>(smem, st);
  if constexpr (K1_PD > 3) st.wf[3] = k1_wfrag<3>(smem, st);
  k1_steps<UNSW, 0>(smem, st, wave, lane);
  CST(20);
  const int m0 = mt * 128 + wm * 64;
  int tq[4], tk[4];
#pragma unroll
  for (int i = 0; i < 4; ++i) {
    const int t = m0 + ((wn + i) & 3) * 16 + c;
    tq[i] = (t >> 4) * 20480 + (g >> 1) * 256 + c * 16 + (g & 1) * 8;
    tk[i] = (128 + (t >> 5) * 2 + ((t >> 2) & 1)) * 20480 + (g >> 1) * 256 + (4 * ((t >> 3) & 3) + (t & 3)) * 16 + (g & 1) * 8;
  }
  {
    u32x2 v = {pack2(st.accs[0], st.accs[1]), pack2(st.accs[2], st.accs[3])};
    st8(Qf + (unsigned)(tk[0] + (48 + 2 * nt) * 256), v);
  }
#pragma unroll
  for (int u = 0; u < 5; ++u) {
    const int v = wn * 5 + u;
    if constexpr (UNSW) {
      const int col = 320 * nt + 16 * v + c;
      const int h = col / 20, d = col - h * 20;
      const int js = d < 16 ? 256 : 64;
      char* dst = Vf + (unsigned)((h * 64 + (m0 >> 5)) * 1280 + (d < 16 ? d * 16 : 1024 + (d - 16) * 16) + (g >> 1) * js + (g & 1) * 8);
#pragma unroll
      for (int i = 0; i < 4; ++i) {
        const int tb = (wn + i) & 3;
        const f32x4 a = st.acc[u][i];
        u32x2 p = {pack2(a[0], a[1]), pack2(a[2], a[3])};
        st8(dst + (tb >> 1) * 1280 + (tb & 1) * 2 * js, p);
      }
    } else {
      if (v < 2 * nq) {
        const int C = 320 * nt - 640 + 16 * v;
#pragma unroll
        for (int i = 0; i < 4; ++i) {
          const f32x4 a = st.acc[u][i];
          u32x2 p = {pack2(a[0], a[1]), pack2(a[2], a[3])};
          st8(Qf + (unsigned)(C >= 640 ? tk[i] + (C - 640) * 32 : tq[i] + C * 32), p);
        }
      } else {
        const int j8 = 2 * (nt * 10 - 52) + v;
#pragma unroll
        for (int p = 0; p < 2; ++p) {
          float hv[4];
#pragma unroll
          for (int r = 0; r < 4; ++r) {
            u32x2 sw = __builtin_amdgcn_permlane32_swap(__float_as_uint(st.acc[u][2 * p][r]),
                                                        __float_as_uint(st.acc[u][2 * p + 1][r]), false, false);
            hv[r] = silu_mul(__uint_as_float(sw[0]), __uint_as_float(sw[1]));
          }
          const int tb = (m0 >> 4) + ((wn + 2 * p + hh) & 3);
          u32x2 pk = {pack2(hv[0], hv[1]), pack2(hv[2], hv[3])};
          st8(Hf + (unsigned)((tb * 216 + j8) * 256 + c * 16 + (g & 1) * 8), pk);
        }
      }
    }
  }
  CST(21);
}

struct K1Ld {
  unsigned long long gp[6];
  int pitch[6];
  const u16* xb;
  int xvoff[2];
  unsigned lds0;
  int lw, lane, rot;
};
__device__ __forceinline__ unsigned long long rowaddr(unsigned long long p, int pitch, int rows) {
  unsigned long long r;
  asm volatile("v_mad_u64_u32 %0, vcc, %1, %2, %3" : "=v"(r) : "v"(pitch), "s"(rows), "v"(p) : "vcc");
  return r;
}
#define GLDS16(p, l) asm volatile("s_mov_b32 m0, %1\n\ts_nop 0\n\tglobal_load_lds_dwordx4 %0, off" :: "v"(p), "s"(l) : "memory")
#define GLDS16S(o, p, l) asm volatile("s_mov_b32 m0, %2\n\ts_nop 0\n\tglobal_load_lds_dwordx4 %0, %1" :: "v"(o), "s"(p), "s"(l) : "memory")
#define WAITVM(n) asm volatile("s_waitcnt vmcnt(%0)" :: "i"(n) : "memory")
template <int H>
__device__ __forceinline__ void k1_ldw(const K1Ld& st) {
  const unsigned l = st.lds0 + K1_W0 + (H % 5) * K1_WS + st.lw * K1_WG;
  int krow = H * 32 + st.rot * 64;
  if (krow >= 640) krow -= 640;
#pragma unroll
  for (int j = 0; j < 6; ++j) {
    unsigned long long p = rowaddr(st.gp[j], st.pitch[j], krow);
    if (j < 5) GLDS16(p, l + j * 1024);
    else if (st.lane < 32) GLDS16(p, l + 5120);
  }
}
template <int T, int J0, int N>
__device__ __forceinline__ void k1_ldx(const K1Ld& st) {
  int kc = (T + st.rot) * 64;
  if (kc >= 640) kc -= 640;
#pragma unroll
  for (int j = J0; j < J0 + N; ++j)
    GLDS16S(st.xvoff[j & 1], st.xb + j * 8 * D_MODEL + kc, st.lds0 + (T % 3) * K1_XS + (st.lw * 4 + j) * 1024);
}
template <int P>
__device__ __forceinline__ void k1_lphase(const K1Ld& st) {
  k1_ldx<P / 2 + 2, (P & 1) * 2, 2>(st);
  k1_ldw<P + 4>(st);
  WAITVM(14);
  BARRIER();
  if constexpr (P + 1 < 15) k1_lphase<P + 1>(st);
}

__device__ __forceinline__ void k1_loader(char* smem, const u16* __restrict__ x, const u16* __restrict__ wqkv,
                                          const u16* __restrict__ wg, const u16* __restrict__ wu,
                                          const u16* __restrict__ wd, const u16* __restrict__ wo,
                                          const u16* __restrict__ gw, char* __restrict__ Wdf, char* __restrict__ Wof,
                                          float* __restrict__ G, unsigned* __restrict__ flags) {
  const int tid = threadIdx.x, lane = tid & 63;
  const int lw = __builtin_amdgcn_readfirstlane(tid >> 6) - 8;
  int mt, nt;
  k1_tile(blockIdx.x, mt, nt);
  K1Ld st;
  st.lw = lw;
  st.lane = lane;
#define K1_ROT 0
  {
    const int idx = blockIdx.x >> 3;
    st.rot = K1_ROT == 0 ? 0 : K1_ROT == 1 ? ((idx & 7) * 5 + (idx >> 3) * 10 / 4 * 4) / 4 % 10 : K1_ROT == 2 ? (idx & 7) : (idx * 10) >> 5;
  }
  st.lds0 = (unsigned)(size_t)smem;
  {
    const int xr = lane >> 3;
    st.xb = x + (size_t)mt * 128 * D_MODEL;
    st.xvoff[0] = ((lw * 32 + xr) * D_MODEL + ((lane & 7) ^ (xr >> 1)) * 8) * 2;
    st.xvoff[1] = st.xvoff[0] ^ 64;
    const int nq = min(max(52 - nt * 10, 0), 10), ngu = 10 - nq;
    const int qcol = nt < 2 ? 1280 + 320 * nt : 320 * nt - 640;
    const int gcol = 16 * (nt * 10 + nq - 52);
#pragma unroll
    for (int j = 0; j < 6; ++j) {
      const int pp = j * 64 + lane;
      const int row = pp / 44, cp = min(pp - row * 44, 42);
      const int k = 8 * lw + (row & 7);
      int cq = cp - 4 * nq;
      if (cq == 2 * ngu + 2) cq = 2 * ngu + 1;
      if (cq < 0 || (cq >= 2 * ngu && cq < 2 * ngu + 2)) {
        const int col = cq < 0 ? qcol + cp * 8 : 1024 + 16 * nt + 8 * (cq - 2 * ngu);
        st.gp[j] = (unsigned long long)(wqkv + (size_t)k * 1920 + col);
        st.pitch[j] = 2 * 1920;
      } else {
        const int half = cq >= 2 * ngu;
        const int col = gcol + 8 * (cq - half * (2 * ngu + 3));
        st.gp[j] = (unsigned long long)((half ? wu : wg) + (size_t)k * F_DIM + col);
        st.pitch[j] = 2 * F_DIM;
      }
    }
  }
  k1_ldx<0, 0, 4>(st);
  k1_ldw<0>(st);
  k1_ldw<1>(st);
  k1_ldx<1, 0, 4>(st);
  k1_ldw<2>(st);
  k1_ldw<3>(st);
  WAITVM(16);
  BARRIER();
  k1_lphase<0>(st);
  k1_ldx<9, 2, 2>(st);
  k1_ldw<19>(st);
  const int item = blockIdx.x * 4 + lw;
  const bool has_item = item < 740;
  const int isd = item < 540;
  const int it2 = isd ? item : (has_item ? item - 540 : 0);
  const int ks = it2 / 10, n64 = it2 - ks * 10;
  u32x4 tw[4];
  {
    const u16* src = (isd ? wd : wo) + (size_t)(ks * 32 + (lane >> 3)) * D_MODEL + n64 * 64 + (lane & 7) * 8;
#pragma unroll
    for (int i = 0; i < 4; ++i) GLOAD(tw[i], src + i * 8 * D_MODEL);
  }
  const int ch2 = 64 + (lane & 15);
  u32x4 xv[2], xv2[2], wv, wv2;
#pragma unroll
  for (int r = 0; r < 2; ++r) {
    const u16* xr = x + (size_t)(item * 2 + r) * D_MODEL;
    GLOAD(xv[r], xr + lane * 8);
    GLOAD(xv2[r], xr + ch2 * 8);
  }
  GLOAD(wv, gw + lane * 8);
  GLOAD(wv2, gw + ch2 * 8);
  WAITVM(10 + 8 + 6);
  BARRIER();
  WAITVM(10 + 6);
  BARRIER();
  WAITVM(10);
  BARRIER();
  asm volatile("s_waitcnt vmcnt(0)" : "+v"(tw[0]), "+v"(tw[1]), "+v"(tw[2]), "+v"(tw[3]), "+v"(xv[0]), "+v"(xv[1]),
               "+v"(xv2[0]), "+v"(xv2[1]), "+v"(wv), "+v"(wv2));
  if (has_item) {
    char* tb = smem + K1_TR + lw * 5632;
#pragma unroll
    for (int i = 0; i < 4; ++i) *(u32x4*)(tb + i * 1408 + (lane >> 3) * 160 + (lane & 7) * 16) = tw[i];
    const int g = lane >> 4, c = lane & 15;
    const char* rp = tb + g * 1408 + (c >> 2) * 160 + (c & 3) * 8;
    char* dst = (isd ? Wdf : Wof) + ((size_t)(ks * 40 + n64 * 4) * 64 + lane) * 16;
#pragma unroll
    for (int nb = 0; nb < 4; ++nb) {
      bfx8 f = tr_frag(rp + nb * 32, rp + nb * 32 + 640);
      *(bfx8*)(dst + nb * 1024) = f;
    }
  }
#pragma unroll
  for (int r = 0; r < 2; ++r) {
    float s = 0.f;
#pragma unroll
    for (int e = 0; e < 4; ++e) {
      s += rbf(__uint_as_float(xv[r][e] << 16) * __uint_as_float(wv[e] << 16));
      s += rbf(__uint_as_float(xv[r][e] & 0xffff0000u) * __uint_as_float(wv[e] & 0xffff0000u));
    }
    if (lane < 16) {
#pragma unroll
      for (int e = 0; e < 4; ++e) {
        s += rbf(__uint_as_float(xv2[r][e] << 16) * __uint_as_float(wv2[e] << 16));
        s += rbf(__uint_as_float(xv2[r][e] & 0xffff0000u) * __uint_as_float(wv2[e] & 0xffff0000u));
      }
    }
#pragma unroll
    for (int o = 32; o >= 1; o >>= 1) s += __shfl_xor(s, o);
    s = rbf(s);
    float gv = rbf(1.0f / (1.0f + __expf(-s)));
    if (lane == 0) G[item * 2 + r] = gv;
  }
  if (lw == 0 && lane == 0) flags[(blockIdx.x >> 3) * FL_STR + (blockIdx.x & 7)] = 0u;
  if (item == 1 && lane < 4) flags[32 * FL_STR + lane] = 0x3f803f80u;
}

__global__ __launch_bounds__(768, 3) void k1_gemm(const u16* __restrict__ x, const u16* __restrict__ wqkv,
                                                  const u16* __restrict__ wg, const u16* __restrict__ wu,
                                                  const u16* __restrict__ wd, const u16* __restrict__ wo,
                                                  const u16* __restrict__ gw, char* __restrict__ Qf,
                                                  char* __restrict__ Vf, char* __restrict__ Hf,
                                                  char* __restrict__ Wdf, char* __restrict__ Wof,
                                                  float* __restrict__ G, unsigned* __restrict__ flags) {
  __shared__ __attribute__((aligned(16))) char smem[K1_LDS];
  const int w8 = __builtin_amdgcn_readfirstlane(threadIdx.x) >> 9;
  if (threadIdx.x < 64) RT(0);
  int mt, nt;
  k1_tile(blockIdx.x, mt, nt);
  if (w8) k1_loader(smem, x, wqkv, wg, wu, wd, wo, gw, Wdf, Wof, G, flags);
  else if (nt < 2) k1_compute<true>(smem, Qf, Vf, Hf);
  else k1_compute<false>(smem, Qf, Vf, Hf);
}

#define AT_K 15360
#define AT_VS 1296
#define AT_LDS (AT_K + 10 * AT_VS)
__device__ __forceinline__ void attn_stage_k(const char* __restrict__ Kf, unsigned lds, int h, int qb0, int lane) {
  const int slot0 = (20 * h) >> 3;
#pragma unroll
  for (int i = 0; i < 15; ++i) {
    const int p = i * 64 + lane;
    const int blk = p / 48, w = p - blk * 48;
    const int gb = max(qb0 - 8 + (blk >> 1), 0) * 2 + (blk & 1);
    GLDS16S(gb * 20480 + w * 16, Kf + slot0 * 256, lds + i * 1024);
  }
}
template <int I0, int I1>
__device__ __forceinline__ void attn_stage_v(const char* __restrict__ Vf, unsigned lds, int h, int qb0, int lane,
                                             unsigned ones) {
#pragma unroll
  for (int i = I0; i < I1; ++i) {
    const int p = i * 64 + lane;
    const int grp = p / 81, w = p - grp * 81;
    const int gg = max(qb0 - 8 + grp, 0);
    const unsigned o = w < 80 ? (unsigned)(gg * 1280 + w * 16) : ones - h * 81920;
    if (i < 12 || lane < 42) GLDS16S(o, Vf + (size_t)h * 81920, lds + AT_K + i * 1024);
  }
}
#define SB() __builtin_amdgcn_sched_barrier(0)
__device__ __forceinline__ f32x16 zero16() {
  return f32x16{0, 0, 0, 0, 0, 0, 0, 0, 0, 0, 0, 0, 0, 0, 0, 0};
}
__device__ __forceinline__ float at_fma(float a, float cs, float nmc) {
  float t = fmaf(a, cs, nmc);
  asm volatile("" : "+v"(t));
  return t;
}
template <bool FIRST>
__device__ __forceinline__ void at_max(const f32x16& t, float (&mx)[4]) {
#pragma unroll
  for (int r = 0; r < 4; ++r) {
    mx[r] = FIRST ? fmaxf(t[r], t[4 + r]) : fmaxf(fmaxf(mx[r], t[r]), t[4 + r]);
    mx[r] = fmaxf(fmaxf(mx[r], t[8 + r]), t[12 + r]);
    asm volatile("" : "+v"(mx[r]));
  }
}
__device__ __forceinline__ void at_neg(f32x16& t, bool c) {
#pragma unroll
  for (int i = 0; i < 16; ++i) t[i] = c ? -1e30f : t[i];
}
template <bool EXP, bool QK, bool EDGE>
__device__ __forceinline__ void at_stage(f32x16 (&s)[8], float& mc, f32x16& o, bfx8 q1, bfx8 q2, const char* kp1,
                                         const char* kp2, const char* vpa, const char* vpb, const unsigned (&mk)[8],
                                         int jmin, int dq) {
  const float cs = 0.22360679774997896f * 1.4426950408889634f;
  const float nmc = -mc;
  float mx[4];
  f32x16 sx;
  u32x4 pk[2], pp[2];
  bfx8 ka, kb, kbp, xa, xb;
  bfx8 va, vb, wa, wb;
  if constexpr (QK) { xa = *(const bfx8*)kp1; xb = *(const bfx8*)kp2; }
  if constexpr (EXP) o = zero16();
#pragma unroll
  for (int u = 0; u < 8; ++u) {
    const int j = u == 0 ? 8 : u;
    SB();
    float t[4];
#define AT_F(c4) if constexpr (EXP) { _Pragma("unroll") for (int e = 0; e < 4; ++e) t[e] = at_fma(s[u][4 * (c4) + e], cs, nmc); }
#define AT_E(c4, h2) if constexpr (EXP) { pk[(c4) >> 1][2 * ((c4) & 1) + (h2)] = pack2(__builtin_amdgcn_exp2f(t[2 * (h2)]), __builtin_amdgcn_exp2f(t[2 * (h2) + 1])); }
    if constexpr (QK) {
      if (u == 0) sx = mfma32(xa, q1, zero16());
      else s[u - 1] = mfma32(kbp, q2, s[u - 1]);
    }
    SB();
    AT_F(0); AT_E(0, 0); AT_E(0, 1);
    SB();
    if constexpr (QK) { if (u == 0) sx = mfma32(xb, q2, sx); }
    if constexpr (EXP) {
      if (u == 1) {
        u32x4 m8;
#pragma unroll
        for (int e = 0; e < 4; ++e) m8[e] = pp[0][e] & mk[e];
        o = mfma32(va, __builtin_bit_cast(bfx8, m8), o);
      } else if (u > 1) o = mfma32(va, __builtin_bit_cast(bfx8, pp[0]), o);
    }
    SB();
    if constexpr (QK) { ka = *(const bfx8*)(kp1 + j * 1536); kb = *(const bfx8*)(kp2 + j * 1536); }
    AT_F(1); AT_E(1, 0);
    if constexpr (EXP) {
      if (u == 1) {
        u32x4 m8;
#pragma unroll
        for (int e = 0; e < 4; ++e) m8[e] = pp[0][e] & ~mk[e];
        SB();
        o = mfma32(wa, __builtin_bit_cast(bfx8, m8), o);
        SB();
      }
    }
    AT_E(1, 1);
    SB();
    if constexpr (EXP) {
      if (u == 1) {
        u32x4 m8;
#pragma unroll
        for (int e = 0; e < 4; ++e) m8[e] = pp[1][e] & mk[4 + e];
        o = mfma32(vb, __builtin_bit_cast(bfx8, m8), o);
      } else if (u > 1) o = mfma32(vb, __builtin_bit_cast(bfx8, pp[1]), o);
    }
    SB();
    AT_F(2); AT_E(2, 0);
    if constexpr (EXP) {
      if (u == 1) {
        u32x4 m8;
#pragma unroll
        for (int e = 0; e < 4; ++e) m8[e] = pp[1][e] & ~mk[4 + e];
        SB();
        o = mfma32(wb, __builtin_bit_cast(bfx8, m8), o);
        SB();
      }
    }
    AT_E(2, 1);
    if constexpr (QK) {
      if (u == 1) {
        if constexpr (EDGE) at_neg(sx, jmin > 0);
#pragma unroll
        for (int i = 0; i < 16; ++i) s[0][i] = 16 * (i >> 3) + (i & 7) <= dq ? s[0][i] : sx[i];
        at_max<true>(s[0], mx);
      } else if (u > 1) {
        if constexpr (EDGE) at_neg(s[u - 1], u - 1 < jmin);
        at_max<false>(s[u - 1], mx);
      }
    }
    AT_F(3);
    SB();
    if constexpr (QK) s[u] = mfma32(ka, q1, zero16());
    SB();
    if constexpr (EXP) {
      va = *(const bfx8*)(vpa + j * AT_VS);
      if (u == 0) wa = *(const bfx8*)vpa;
      vb = *(const bfx8*)(vpb + j * AT_VS);
      if (u == 0) wb = *(const bfx8*)vpb;
    }
    AT_E(3, 0); AT_E(3, 1);
    kbp = kb;
    pp[0] = pk[0]; pp[1] = pk[1];
  }
  SB();
  if constexpr (QK) s[7] = mfma32(kbp, q2, s[7]);
  if constexpr (EXP) {
    o = mfma32(va, __builtin_bit_cast(bfx8, pp[0]), o);
    o = mfma32(vb, __builtin_bit_cast(bfx8, pp[1]), o);
  }
  if constexpr (QK) {
    if constexpr (EDGE) at_neg(s[7], 7 < jmin);
    at_max<false>(s[7], mx);
    float m = fmaxf(fmaxf(mx[0], mx[1]), fmaxf(mx[2], mx[3]));
    u32x2 r32 = __builtin_amdgcn_permlane32_swap(__float_as_uint(m), __float_as_uint(m), false, false);
    m = fmaxf(__uint_as_float(r32[0]), __uint_as_float(r32[1]));
    mc = m * cs;
  }
}
template <bool EDGE>
__device__ __forceinline__ void at_first(f32x16 (&s)[8], float& mc, bfx8 q1, bfx8 q2, const char* kp1, const char* kp2,
                                         int jmin, int dq, const char* __restrict__ Vf, unsigned lds, int h, int qb0,
                                         int lane, unsigned ones) {
  const float cs = 0.22360679774997896f * 1.4426950408889634f;
  float mx[4];
  f32x16 sx;
  bfx8 ka[9], kb[9];
#pragma unroll
  for (int p = 0; p < 6; ++p) {
    if (p >= 1 && p <= 3) WAITVM(12);
    if (p == 4) WAITVM(13);
#pragma unroll
    for (int j = 2 * p; j < 2 * p + 2; ++j)
      if (j < 9) { ka[j] = *(const bfx8*)(kp1 + j * 1536); kb[j] = *(const bfx8*)(kp2 + j * 1536); }
    SB();
#pragma unroll
    for (int j = 2 * p - 2; j < 2 * p; ++j)
      if (j >= 0 && j < 9) {
        f32x16 t = mfma32(kb[j], q2, mfma32(ka[j], q1, zero16()));
        if (j == 0) sx = t; else s[j & 7] = t;
      }
#pragma unroll
    for (int j = 2 * p - 4; j < 2 * p - 2; ++j)
      if (j >= 1) {
        if constexpr (EDGE) at_neg(s[j], j < jmin);
        if (j == 1) at_max<true>(s[j], mx); else at_max<false>(s[j], mx);
      }
    SB();
    if (p == 0) attn_stage_v<0, 3>(Vf, lds, h, qb0, lane, ones);
    if (p == 1) attn_stage_v<3, 6>(Vf, lds, h, qb0, lane, ones);
    if (p == 2) attn_stage_v<6, 9>(Vf, lds, h, qb0, lane, ones);
    if (p == 3) attn_stage_v<9, 12>(Vf, lds, h, qb0, lane, ones);
    if (p == 4) { attn_stage_v<12, 13>(Vf, lds, h, qb0, lane, ones); BARRIER0(); }
    SB();
  }
  if constexpr (EDGE) at_neg(sx, jmin > 0);
#pragma unroll
  for (int i = 0; i < 16; ++i) s[0][i] = 16 * (i >> 3) + (i & 7) <= dq ? s[0][i] : sx[i];
  at_max<false>(s[0], mx);
  float m = fmaxf(fmaxf(mx[0], mx[1]), fmaxf(mx[2], mx[3]));
  u32x2 r32 = __builtin_amdgcn_permlane32_swap(__float_as_uint(m), __float_as_uint(m), false, false);
  m = fmaxf(__uint_as_float(r32[0]), __uint_as_float(r32[1]));
  mc = m * cs;
}
__device__ __forceinline__ void at_store(const f32x16& o, float gq, char* __restrict__ dst, int k0, int hh) {
  u32x2 r32 = __builtin_amdgcn_permlane32_swap(__float_as_uint(o[8]), __float_as_uint(o[8]), false, false);
  const float inv = gq * __builtin_amdgcn_rcpf(__uint_as_float(r32[1]));
#pragma unroll
  for (int q = 0; q < 3; ++q) {
    const int k = k0 + 8 * q;
    u32x2 v = {pack2(o[4 * q] * inv, o[4 * q + 1] * inv), pack2(o[4 * q + 2] * inv, o[4 * q + 3] * inv)};
    if (q < 2 || hh == 0) st8b(dst + (k >> 3) * 256 + ((k >> 2) & 1) * 8, v);
  }
}
template <bool EDGE>
__device__ __forceinline__ void attn_run(const u32x4 (&qv)[4], const float (&gq)[2], const char* al, char* __restrict__ Of,
                                         int h, int qb0, int lane, const char* __restrict__ Vf, unsigned ones) {
  asm volatile("" : "+v"(lane));
  const int hh = lane >> 5, n = lane & 31;
  bfx8 qf[4];
#pragma unroll
  for (int t = 0; t < 4; ++t) {
    u32x4 v = qv[t];
    if ((t & 1) && (hh || !(h & 1))) { v[2] = 0; v[3] = 0; }
    if ((t & 1) ? hh : (!hh && (h & 1))) { v[0] = 0; v[1] = 0; }
    qf[t] = __builtin_bit_cast(bfx8, v);
  }
  const char* kp2 = al + ((n >> 3) & 1) * 768 + (8 * (n >> 4) + (n & 7)) * 16;
  const char* kp1 = kp2 + hh * 256;
  kp2 += 512;
  const char* vpa = al + AT_K + (n < 16 ? hh * 256 + n * 16 : n < 20 ? 1024 + hh * 64 + (n - 16) * 16 : 1280);
  const char* vpb = vpa + (n < 16 ? 512 : n < 20 ? 128 : 0);
  const int dq = n - 8 * hh;
  unsigned mk[8];
#pragma unroll
  for (int t = 0; t < 8; ++t) {
    const int ko = 16 * (t >> 2) + 2 * (t & 3);
    mk[t] = (ko <= dq ? 0xffffu : 0u) | (ko + 1 <= dq ? 0xffff0000u : 0u);
  }
  const int jm = 8 - qb0;
  char* dst = Of + (size_t)(qb0 * 2 + (n >> 4)) * 20480 + (n & 15) * 16;
  const int k0 = h * 20 + 4 * hh;
  f32x16 s[8], o;
  float mc = 0.f;
#define AST(i)
  AST(0);
  at_first<EDGE>(s, mc, qf[0], qf[1], kp1, kp2, jm, dq, Vf, (unsigned)(size_t)al, h, qb0, lane, ones);
  AST(1);
  WAITVM(0);
  AST(2);
  at_stage<true, true, EDGE>(s, mc, o, qf[2], qf[3], kp1 + 1536, kp2 + 1536, vpa, vpb, mk, jm - 1, dq);
  AST(3);
  at_store(o, gq[0], dst, k0, hh);
  AST(4);
  at_stage<true, false, EDGE>(s, mc, o, qf[2], qf[3], kp1, kp2, vpa + AT_VS, vpb + AT_VS, mk, jm, dq);
  AST(5);
  at_store(o, gq[1], dst + 40960, k0, hh);
  AST(6);
  AST(7);
}

#define GLOADSC(r, o, p) asm volatile("global_load_dwordx4 %0, %1, %2 sc1" : "=v"(r) : "v"(o), "s"(p))
struct G2Frag { u32x4 a[4], w[5]; };
__device__ __forceinline__ void g2_issue_w(G2Frag& f, const char* Wb, int ks, int voff) {
  const char* wp = Wb + (size_t)ks * 40960 + 2048;
  GLOADSO(f.w[0], voff, wp, -2048);
  GLOADSO(f.w[1], voff, wp, -1024);
  GLOADSO(f.w[2], voff, wp, 0);
  GLOADSO(f.w[3], voff, wp, 1024);
  GLOADSO(f.w[4], voff, wp, 2048);
}
__device__ __forceinline__ void g2_issue_h(G2Frag& f, const char* Ab, int ks, int voff, int wave) {
  const char* ap = Ab + (size_t)ks * 1024;
#pragma unroll
  for (int i = 0; i < 4; ++i) GLOADS(f.a[i], voff, ap + (size_t)((wave + i) & 3) * 54 * 1024);
}
__device__ __forceinline__ void g2_issue_o(G2Frag& f, const char* Ab, int ks, int voff, int wave) {
  const char* ap = Ab + (size_t)ks * 1024;
#pragma unroll
  for (int i = 0; i < 4; ++i) GLOADSC(f.a[i], voff, ap + (size_t)((wave + i) & 3) * 20 * 1024);
}
template <int N>
__device__ __forceinline__ void g2_wait(G2Frag& f) {
  asm volatile("s_waitcnt vmcnt(%9)"
               : "+v"(f.a[0]), "+v"(f.a[1]), "+v"(f.a[2]), "+v"(f.a[3]), "+v"(f.w[0]), "+v"(f.w[1]), "+v"(f.w[2]),
                 "+v"(f.w[3]), "+v"(f.w[4])
               : "i"(N));
}
struct G2Ctx {
  const char *Hb, *Wdb, *Ob, *Wob;
  const unsigned* fl;
  const float* gp;
  int wave, voff;
  float omg[4];
  unsigned fv;
};
#define GST(i)
#define G2_PD 4
struct G2Seq { int wait[19], poll; };
constexpr G2Seq g2_seq() {
  G2Seq q{};
  int tag[256] = {}, n = 0;
  for (int s = 0; s < G2_PD; ++s) for (int i = 0; i < 9; ++i) tag[n++] = s;
  for (int t = 0; t < 19; ++t) {
    int last = 0;
    for (int i = 0; i < n; ++i) if (tag[i] == t) last = i;
    q.wait[t] = n - 1 - last;
    const int T = t + G2_PD;
    if (t == 12) for (int i = 0; i < 5; ++i) tag[n++] = -1;
    if (t < 13) for (int i = 0; i < (T <= 13 ? 9 : 5); ++i) tag[n++] = T;
    if (t == 13) {
      for (int u = T; u <= 17; ++u) for (int i = 0; i < 5; ++i) tag[n++] = u;
      for (int i = 0; i < n; ++i) if (tag[i] == -1) last = i;
      q.poll = n - 1 - last;
      for (int u = 14; u <= 17; ++u) for (int i = 0; i < 4; ++i) tag[n++] = u;
    }
    if (t == 14) for (int i = 0; i < 9; ++i) tag[n++] = 18;
  }
  return q;
}
template <int S>
__device__ __forceinline__ void g2_steps(G2Frag (&f)[4], f32x4 (&acc)[5][4], G2Ctx& cx) {
  constexpr G2Seq Q = g2_seq();
  constexpr int T = S + G2_PD;
  G2Frag& cur = f[S & 3];
  g2_wait<Q.wait[S]>(cur);
  GST(S + 1);
  if (S != 13 || cx.wave < 2) {
#pragma unroll
    for (int u = 0; u < 5; ++u)
#pragma unroll
      for (int i = 0; i < 4; ++i)
        asm volatile("v_mfma_f32_16x16x32_bf16 %0, %1, %2, %0" : "+v"(acc[u][i]) : "v"(cur.w[u]), "v"(cur.a[i]));
  }
  if constexpr (S == 13 || S == 18) asm volatile("s_nop 7\n\ts_nop 7" ::: "memory");
  __builtin_amdgcn_sched_barrier(0);
  if constexpr (S == 12) {
    const int go = (cx.voff >> 2) & 60;
#pragma unroll
    for (int i = 0; i < 4; ++i)
      asm volatile("global_load_dword %0, %1, %2" : "=v"(cx.omg[i]) : "v"(go), "s"(cx.gp + ((cx.wave + i) & 3) * 16));
    asm volatile("global_load_dword %0, %1, %2 sc1" : "=v"(cx.fv) : "v"(go & 28), "s"(cx.fl));
  }
  if constexpr (S < 13) {
    if constexpr (T <= 13) {
      const int ks = min(T * 4 + cx.wave, 53);
      g2_issue_h(f[T & 3], cx.Hb, ks, cx.voff, cx.wave);
      g2_issue_w(f[T & 3], cx.Wdb, ks, cx.voff);
    } else {
      g2_issue_w(f[T & 3], cx.Wob, (T - 14) * 4 + cx.wave, cx.voff);
    }
  } else if constexpr (S == 13) {
#pragma unroll
    for (int u = T; u <= 17; ++u) g2_issue_w(f[u & 3], cx.Wob, (u - 14) * 4 + cx.wave, cx.voff);
    asm volatile("s_waitcnt vmcnt(%5)" : "+v"(cx.fv), "+v"(cx.omg[0]), "+v"(cx.omg[1]), "+v"(cx.omg[2]), "+v"(cx.omg[3]) : "i"(Q.poll));
#pragma unroll 1
    for (int it = 0; it < (1 << 20) && __builtin_amdgcn_ballot_w64(cx.fv != 1u) != 0; ++it) {
      __builtin_amdgcn_s_sleep(FL_SLP);
      asm volatile("global_load_dword %0, %1, %2 sc1\n\ts_waitcnt vmcnt(0)" : "=v"(cx.fv) : "v"((cx.voff >> 2) & 28), "s"(cx.fl) : "memory");
    }
    asm volatile("" ::: "memory");
    GST(22);
#pragma unroll
    for (int j = 0; j < 4; ++j) g2_issue_o(f[(14 + j) & 3], cx.Ob, j * 4 + cx.wave, cx.voff, cx.wave);
#pragma unroll
    for (int u = 0; u < 5; ++u)
#pragma unroll
      for (int i = 0; i < 4; ++i) {
        acc[u][i] *= rbf(1.0f - cx.omg[i]);
        asm volatile("" : "+v"(acc[u][i]));
      }
    __builtin_amdgcn_sched_barrier(0);
    asm volatile("s_nop 3" ::: "memory");
  } else if constexpr (S == 14) {
    g2_issue_o(cur, cx.Ob, 16 + cx.wave, cx.voff, cx.wave);
    g2_issue_w(cur, cx.Wob, 16 + cx.wave, cx.voff);
  }
  if constexpr (S + 1 < 19) g2_steps<S + 1>(f, acc, cx);
}
__device__ __forceinline__ void g2_tile(int bid, int& mt, int& nt) {
  const int xcd = bid & 7, idx = bid >> 3;
  mt = (xcd >> 1) * 8 + (idx >> 2);
  nt = (xcd & 1) * 4 + (idx & 3);
}

__global__ __launch_bounds__(512, 2) void k2_mix(const char* __restrict__ Qf, const char* __restrict__ Kf,
                                                 const char* __restrict__ Vf, char* Of,
                                                 const char* __restrict__ Hf, const char* __restrict__ Wdf,
                                                 const char* __restrict__ Wof, const float* __restrict__ G,
                                                 unsigned* flags, u16* __restrict__ out) {
  __shared__ __attribute__((aligned(16))) char smem[4 * AT_LDS];
  __shared__ unsigned k2_cnt;
  const int tid = threadIdx.x, lane = tid & 63;
  const int wave = __builtin_amdgcn_readfirstlane(tid >> 6) ^ 4;
  const int bid = blockIdx.x;
  if (threadIdx.x < 64) RT(2);
  int mt, nt;
  g2_tile(bid, mt, nt);
  if (wave >= 4) {
    float gq[2];
    {
      const float* gp = G + mt * 64 + (lane & 31);
#pragma unroll
      for (int i = 0; i < 2; ++i) GLOAD1(gq[i], gp + i * 32);
    }
    const int h = nt * 4 + (wave - 4);
    const int qb0 = mt * 2;
    if (wave == 4) ST(0, 0);
    char* al = smem + (wave - 4) * AT_LDS;
    u32x4 qv[4];
    {
      const char* qp = Qf + (size_t)(qb0 * 2 + ((lane >> 4) & 1)) * 20480 + ((20 * h) >> 3) * 256 + (lane & 15) * 16;
#pragma unroll
      for (int i = 0; i < 4; ++i) GLOAD(qv[i], qp + (i >> 1) * 40960 + ((i & 1) ? 512 : (lane >> 5) * 256));
    }
    attn_stage_k(Kf, (unsigned)(size_t)al, h, qb0, lane);
    if (tid == 0) k2_cnt = 0u;
    asm volatile("s_waitcnt vmcnt(12)" : "+v"(qv[0]), "+v"(qv[1]), "+v"(qv[2]), "+v"(qv[3]), "+v"(gq[0]),
                 "+v"(gq[1]) :: "memory");
    if (wave == 4) ST(0, 1);
    const unsigned ones = (unsigned)((const char*)(flags + 32 * FL_STR) - Vf);
    if (qb0 < 8) attn_run<true>(qv, gq, al, Of, h, qb0, lane, Vf, ones);
    else attn_run<false>(qv, gq, al, Of, h, qb0, lane, Vf, ones);
    if (wave == 4) ST(0, 2);
    if (wave == 4) ST(0, 3);
    WAITVM(0);
    if (lane == 0) {
      unsigned old = __hip_atomic_fetch_add(&k2_cnt, 1u, __ATOMIC_RELAXED, __HIP_MEMORY_SCOPE_WORKGROUP);
      if (old == 3u) __hip_atomic_store(flags + mt * FL_STR + nt, 1u, __ATOMIC_RELAXED, __HIP_MEMORY_SCOPE_AGENT);
    }
    if (wave == 4) ST(0, 4);
    return;
  }
  f32x4 acc[5][4];
#pragma unroll
  for (int u = 0; u < 5; ++u)
#pragma unroll
    for (int i = 0; i < 4; ++i) {
      acc[u][i] = f32x4{0, 0, 0, 0};
      asm volatile("" : "+v"(acc[u][i]));
    }
  G2Ctx cx;
  cx.Hb = Hf + (size_t)mt * 4 * 54 * 1024;
  cx.Wdb = Wdf + (size_t)nt * 5120;
  cx.Ob = Of + (size_t)mt * 4 * 20 * 1024;
  cx.Wob = Wof + (size_t)nt * 5120;
  cx.fl = flags + mt * FL_STR;
  cx.gp = G + mt * 64;
  cx.wave = wave;
  cx.voff = lane * 16;
  BARRIER0();
  GST(0);
  G2Frag f[4];
#pragma unroll
  for (int s = 0; s < G2_PD; ++s) {
    g2_issue_h(f[s], cx.Hb, s * 4 + wave, cx.voff, wave);
    g2_issue_w(f[s], cx.Wdb, s * 4 + wave, cx.voff);
  }
  g2_steps<0>(f, acc, cx);
  GST(20);
  f32x4* red = (f32x4*)smem;
#pragma unroll
  for (int i = 1; i < 4; ++i)
#pragma unroll
    for (int u = 0; u < 5; ++u) red[((((wave + i) & 3) * 3 + i - 1) * 5 + u) * 64 + lane] = acc[u][i];
  BARRIER();
  const int tok = mt * 64 + wave * 16 + (lane & 15);
#pragma unroll
  for (int u = 0; u < 5; ++u) {
    f32x4 v = acc[u][0];
#pragma unroll
    for (int i = 0; i < 3; ++i) v += red[((wave * 3 + i) * 5 + u) * 64 + lane];
    u32x2 pv = {pack2(v[0], v[1]), pack2(v[2], v[3])};
    *(u32x2*)(out + (size_t)tok * D_MODEL + nt * 80 + u * 16 + 4 * (lane >> 4)) = pv;
  }
  GST(21);
}

extern "C" void kernel_launch(void* const* d_in, const int* in_sizes, int n_in,
                              void* d_out, int out_size, void* d_ws, size_t ws_size,
                              hipStream_t stream) {
  const u16* x = (const u16*)d_in[0];
  const u16* wqkv = (const u16*)d_in[1];
  const u16* wo = (const u16*)d_in[2];
  const u16* wg = (const u16*)d_in[4];
  const u16* wu = (const u16*)d_in[5];
  const u16* wd = (const u16*)d_in[6];
  const u16* gw = (const u16*)d_in[7];
  char* ws = (char*)d_ws;
  const size_t QB = (size_t)S_LEN * D_MODEL * 2;
  char* Qf = ws;
  char* Kf = Qf + QB;
  char* Vf = Kf + QB;
  char* Hf = ws + (12u << 20);
  char* Of = ws + (20u << 20);
  char* Wdf = ws + (28u << 20);
  char* Wof = ws + (31u << 20);
  float* G = (float*)(ws + (32u << 20));
  unsigned* flags = (unsigned*)(ws + (33u << 20));
  u16* out = (u16*)d_out;

  k1_gemm<<<256, 768, 0, stream>>>(x, wqkv, wg, wu, wd, wo, gw, Qf, Vf, Hf, Wdf, Wof, G, flags);
  k2_mix<<<256, 512, 0, stream>>>(Qf, Kf, Vf, Of, Hf, Wdf, Wof, G, flags, out);
}
```

```cpp
#include <hip/hip_runtime.h>
#include <hip/hip_bf16.h>
#include <stdint.h>

typedef __attribute__((ext_vector_type(8))) __bf16 bfx8;
typedef __attribute__((ext_vector_type(2))) __bf16 bfx2;
typedef __attribute__((ext_vector_type(2))) float f32x2;
typedef __attribute__((ext_vector_type(4))) short s16x4;
typedef __attribute__((ext_vector_type(8))) short s16x8;
typedef __attribute__((ext_vector_type(4))) float f32x4;
typedef __attribute__((ext_vector_type(16))) float f32x16;
typedef __attribute__((ext_vector_type(2))) unsigned u32x2;
typedef __attribute__((ext_vector_type(4))) unsigned u32x4;
typedef __attribute__((ext_vector_type(4), aligned(8))) unsigned u32x4a8;
typedef __attribute__((address_space(3))) s16x4* lds_s16x4_ptr;
typedef unsigned short u16;

#define FL_STR 64
#define FL_SLP 2
#define S_LEN 2048
#define D_MODEL 640
#define F_DIM 1728

__device__ __forceinline__ float bf2f(u16 v) { return __uint_as_float(((unsigned)v) << 16); }
__device__ __forceinline__ u16 f2bf(float f) {
  __hip_bfloat16 b = __float2bfloat16(f);
  return __builtin_bit_cast(u16, b);
}
__device__ __forceinline__ float rbf(float f) { return bf2f(f2bf(f)); }
__device__ __forceinline__ unsigned pack2(float lo, float hi) {
  bfx2 r = __builtin_convertvector(f32x2{lo, hi}, bfx2);
  return __builtin_bit_cast(unsigned, r);
}
__device__ __forceinline__ f32x4 mfma16(bfx8 a, bfx8 b, f32x4 c) {
  return __builtin_amdgcn_mfma_f32_16x16x32_bf16(a, b, c, 0, 0, 0);
}
__device__ __forceinline__ f32x16 mfma32(bfx8 a, bfx8 b, f32x16 c) {
  return __builtin_amdgcn_mfma_f32_32x32x16_bf16(a, b, c, 0, 0, 0);
}
__device__ __forceinline__ bfx8 tr_frag(const char* p0, const char* p1) {
  s16x4 a = __builtin_amdgcn_ds_read_tr16_b64_v4i16((lds_s16x4_ptr)p0);
  s16x4 b = __builtin_amdgcn_ds_read_tr16_b64_v4i16((lds_s16x4_ptr)p1);
  s16x8 v = {a[0], a[1], a[2], a[3], b[0], b[1], b[2], b[3]};
  return __builtin_bit_cast(bfx8, v);
}
#define STPOL 2
#define STPOLB 2
template <int POL>
__device__ __forceinline__ void st8p(char* p, u32x2 v) {
  if constexpr (POL == 0) *(u32x2*)p = v;
  else if constexpr (POL == 1) __builtin_nontemporal_store(v, (u32x2*)p);
  else if constexpr (POL == 2) asm volatile("global_store_dwordx2 %0, %1, off sc1" :: "v"(p), "v"(v) : "memory");
  else asm volatile("global_store_dwordx2 %0, %1, off sc0 sc1" :: "v"(p), "v"(v) : "memory");
}
__device__ __forceinline__ void st8(char* p, u32x2 v) { st8p<STPOL>(p, v); }
__device__ __forceinline__ void st8b(char* p, u32x2 v) { st8p<STPOLB>(p, v); }
#define GLOAD(r, p) asm volatile("global_load_dwordx4 %0, %1, off" : "=v"(r) : "v"(p))
#define GLOADS(r, o, p) asm volatile("global_load_dwordx4 %0, %1, %2" : "=v"(r) : "v"(o), "s"(p))
#define GLOADSO(r, o, p, imm) asm volatile("global_load_dwordx4 %0, %1, %2 offset:%3" : "=v"(r) : "v"(o), "s"(p), "i"(imm))
#define GLOAD1(r, p) asm volatile("global_load_dword %0, %1, off" : "=v"(r) : "v"(p))
#define WAITV(n, r) asm volatile("s_waitcnt vmcnt(%1)" : "+v"(r) : "i"(n))
__device__ __forceinline__ void bump(const u16*& p, int stride) {
  unsigned lo = (unsigned)(size_t)p, hi = (unsigned)((size_t)p >> 32);
  asm volatile("v_add_co_u32 %0, vcc, %0, %2\n\tv_addc_co_u32 %1, vcc, 0, %1, vcc" : "+v"(lo), "+v"(hi) : "v"(stride) : "vcc");
  p = (const u16*)(((size_t)hi << 32) | lo);
}
#define BARRIER0() asm volatile("s_barrier" ::: "memory")
#define BARRIER() asm volatile("s_waitcnt lgkmcnt(0)\n\ts_barrier" ::: "memory")
#define ST(role, i)
#define RT(i)

struct Ws {
  char *Qf, *Kf, *Vf, *Hf, *Of, *Wdf, *Wof, *Bb;
  float* G;
};

#define K1_XS 16384
#define K1_WP 704
#define K1_WG (8 * K1_WP + 32)
#define K1_WS (4 * K1_WG)
#define K1_W0 (3 * K1_XS)
#define K1_TR K1_W0
#define K1_LDS (K1_W0 + 5 * K1_WS + 256)

__device__ __forceinline__ void k1_tile(int bid, int& mt, int& nt) {
  const int xcd = bid & 7, idx = bid >> 3;
  mt = (xcd >> 2) * 8 + (idx & 7);
  nt = (xcd & 3) * 4 + (idx >> 3);
}

#define CST(i)

#define K1_PD 2
struct K1C {
  f32x4 acc[5][4], accs;
  int wa[6], wah[6];
  int xa[2][4];
  bfx8 wf[K1_PD + 1], xf[2][4];
  const char* pfb;
  int pfo, pfs, pfs2;
  unsigned pf;
};
#define PF_L 8
#define PF_B 4
#define PF_DP 3
template <int P>
__device__ __forceinline__ void k1_probe(K1C& st) {
#pragma unroll
  for (int H = 4; H < 20; ++H)
    if (P == (H < PF_L ? -1 : -1 + ((H - PF_L) / PF_B + 1) * PF_DP))
      asm volatile("global_load_dword %0, %1, %2" : "+v"(st.pf)
                   : "v"(st.pfo), "s"(st.pfb + (size_t)((H >> 1) * st.pfs + (H & 1) * st.pfs2)));
  if constexpr (P == 17) asm volatile("s_waitcnt vmcnt(0)" : "+v"(st.pf));
}
template <int F>
__device__ __forceinline__ bfx8 k1_wfrag(const char* smem, const K1C& st) {
  constexpr int P = F / 6, u = F % 6, sl = P % 5;
  constexpr int off = (sl < 3 ? sl : sl - 3) * K1_WS;
  const char* b = smem + (sl < 3 ? st.wa[u] : st.wah[u]);
  return tr_frag(b + off, b + off + 4 * K1_WP);
}
template <bool UNSW, int F>
__device__ __forceinline__ void k1_steps(const char* smem, K1C& st, int wave, int lane) {
  constexpr int P = F / 6, u = F % 6;
  if constexpr (F + K1_PD == 6) BARRIER0();
  if constexpr (F + K1_PD < 120) st.wf[(F + K1_PD) % (K1_PD + 1)] = k1_wfrag<F + K1_PD>(smem, st);
  if constexpr (u < 3 && P < 19)
    st.xf[(P + 1) & 1][u] = *(const bfx8*)(smem + st.xa[(P + 1) & 1][u] + (((P + 1) >> 1) % 3) * K1_XS);
  if constexpr (u == 2 && P < 19)
    st.xf[(P + 1) & 1][3] = *(const bfx8*)(smem + st.xa[(P + 1) & 1][3] + (((P + 1) >> 1) % 3) * K1_XS);
  if constexpr (u < 5) {
#pragma unroll
    for (int i = 0; i < 4; ++i) {
      if constexpr (UNSW) st.acc[u][i] = mfma16(st.xf[P & 1][i], st.wf[F % (K1_PD + 1)], st.acc[u][i]);
      else st.acc[u][i] = mfma16(st.wf[F % (K1_PD + 1)], st.xf[P & 1][i], st.acc[u][i]);
    }
  } else {
    st.accs = mfma16(st.wf[F % (K1_PD + 1)], st.xf[P & 1][0], st.accs);
  }
  __builtin_amdgcn_sched_barrier(0);
  if constexpr (u == 5 && P < 18) {
    if constexpr (P & 1) { CST(P + 1); }
    BARRIER0();
    if constexpr (P & 1) { CST(P + 2); }
    k1_probe<P + 1>(st);
  }
  if constexpr (F + 1 < 120) k1_steps<UNSW, F + 1>(smem, st, wave, lane);
}

__device__ __forceinline__ float silu_mul(float g, float u) {
  return g * u * __builtin_amdgcn_rcpf(1.0f + __builtin_amdgcn_exp2f(g * -1.4426950408889634f));
}

template <bool UNSW>
__device__ __forceinline__ void k1_compute(char* smem, char* __restrict__ Qf, char* __restrict__ Vf,
                                           char* __restrict__ Hf, const u16* __restrict__ x,
                                           const u16* __restrict__ wqkv, const u16* __restrict__ wg,
                                           const u16* __restrict__ wu) {
  const int tid = threadIdx.x, lane = tid & 63;
  const int wave = __builtin_amdgcn_readfirstlane(tid >> 6);
  int mt, nt;
  k1_tile(blockIdx.x, mt, nt);
  const int wm = wave & 1, wn = wave >> 1;
  const int g = lane >> 4, c = lane & 15, hh = lane >> 5;
  const int nq = min(max(52 - nt * 10, 0), 10), ngu = 10 - nq;
  K1C st;
  {
    const int role = wave & 3, half = wave >> 2;
    const int k = half * 16 + mt, q = lane & 7;
    if (role == 3) {
      st.pfb = (const char*)(x + (size_t)mt * 128 * D_MODEL);
      st.pfo = (nt * 8 + half * 2 + (lane & 1)) * 1280;
      st.pfs = 128;
      st.pfs2 = 4 * 1280;
    } else if (role == 0 || ngu == 0) {
      const int qcol = nt < 2 ? 1280 + 320 * nt : 320 * nt - 640;
      st.pfb = (const char*)wqkv;
      st.pfo = k * 3840 + (q * 128 >= nq * 64 ? 2048 + 32 * nt : qcol * 2 + q * 128);
      st.pfs = 64 * 3840;
      st.pfs2 = 32 * 3840;
    } else {
      st.pfb = (const char*)(role == 1 ? wg : wu);
      st.pfo = k * 3456 + 32 * (nt * 10 + nq - 52) + min(q * 128, ngu * 32 - 16);
      st.pfs = 64 * 3456;
      st.pfs2 = 32 * 3456;
    }
    st.pf = 0;
    k1_probe<-1>(st);
  }
  {
    const int x0 = (wm * 64 + c) * 128 + ((g ^ (c >> 1)) << 4);
#pragma unroll
    for (int i = 0; i < 4; ++i) {
      st.xa[0][i] = x0 + ((wn + i) & 3) * 2048;
      st.xa[1][i] = st.xa[0][i] ^ 64;
      asm volatile("" : "+v"(st.xa[0][i]), "+v"(st.xa[1][i]));
    }
    const int rowp = K1_W0 + g * K1_WG + (c >> 2) * K1_WP;
#pragma unroll
    for (int u = 0; u < 5; ++u) {
      const int v = wn * 5 + u;
      st.wa[u] = rowp + (v < 2 * nq ? v * 32 + (c & 3) * 8
                                    : nq * 32 + v * 16 + (c & 1) * 8 + ((c & 2) ? ngu * 32 + 48 : 0));
    }
    st.wa[5] = rowp + (c & 3) * 8 + nq * 64 + ngu * 32;
#pragma unroll
    for (int u = 0; u < 6; ++u) {
      st.wah[u] = st.wa[u] + 3 * K1_WS;
      asm volatile("" : "+v"(st.wa[u]), "+v"(st.wah[u]));
    }
  }
#pragma unroll
  for (int u = 0; u < 5; ++u)
#pragma unroll
    for (int i = 0; i < 4; ++i) st.acc[u][i] = f32x4{0, 0, 0, 0};
  st.accs = f32x4{0, 0, 0, 0};
  CST(0);
  BARRIER();
  CST(1);
  k1_probe<0>(st);
  st.wf[0] = k1_wfrag<0>(smem, st);
#pragma unroll
  for (int i = 0; i < 4; ++i) st.xf[0][i] = *(const bfx8*)(smem + st.xa[0][i]);
  st.wf[1] = k1_wfrag<1>(smem, st);
  if constexpr (K1_PD > 2) st.wf[2] = k1_wfrag<2>(smem, st);
  if constexpr (K1_PD > 3) st.wf[3] = k1_wfrag<3>(smem, st);
  k1_steps<UNSW, 0>(smem, st, wave, lane);
  CST(20);
  const int m0 = mt * 128 + wm * 64;
  int tq[4], tk[4];
#pragma unroll
  for (int i = 0; i < 4; ++i) {
    const int t = m0 + ((wn + i) & 3) * 16 + c;
    tq[i] = (t >> 4) * 20480 + (g >> 1) * 256 + c * 16 + (g & 1) * 8;
    tk[i] = (128 + (t >> 5) * 2 + ((t >> 2) & 1)) * 20480 + (g >> 1) * 256 + (4 * ((t >> 3) & 3) + (t & 3)) * 16 + (g & 1) * 8;
  }
  {
    u32x2 v = {pack2(st.accs[0], st.accs[1]), pack2(st.accs[2], st.accs[3])};
    st8(Qf + (unsigned)(tk[0] + (48 + 2 * nt) * 256), v);
  }
#pragma unroll
  for (int u = 0; u < 5; ++u) {
    const int v = wn * 5 + u;
    if constexpr (UNSW) {
      const int col = 320 * nt + 16 * v + c;
      const int h = col / 20, d = col - h * 20;
      const int js = d < 16 ? 256 : 64;
      char* dst = Vf + (unsigned)((h * 64 + (m0 >> 5)) * 1280 + (d < 16 ? d * 16 : 1024 + (d - 16) * 16) + (g >> 1) * js + (g & 1) * 8);
#pragma unroll
      for (int i = 0; i < 4; ++i) {
        const int tb = (wn + i) & 3;
        const f32x4 a = st.acc[u][i];
        u32x2 p = {pack2(a[0], a[1]), pack2(a[2], a[3])};
        st8(dst + (tb >> 1) * 1280 + (tb & 1) * 2 * js, p);
      }
    } else {
      if (v < 2 * nq) {
        const int C = 320 * nt - 640 + 16 * v;
#pragma unroll
        for (int i = 0; i < 4; ++i) {
          const f32x4 a = st.acc[u][i];
          u32x2 p = {pack2(a[0], a[1]), pack2(a[2], a[3])};
          st8(Qf + (unsigned)(C >= 640 ? tk[i] + (C - 640) * 32 : tq[i] + C * 32), p);
        }
      } else {
        const int j8 = 2 * (nt * 10 - 52) + v;
#pragma unroll
        for (int p = 0; p < 2; ++p) {
          float hv[4];
#pragma unroll
          for (int r = 0; r < 4; ++r) {
            u32x2 sw = __builtin_amdgcn_permlane32_swap(__float_as_uint(st.acc[u][2 * p][r]),
                                                        __float_as_uint(st.acc[u][2 * p + 1][r]), false, false);
            hv[r] = silu_mul(__uint_as_float(sw[0]), __uint_as_float(sw[1]));
          }
          const int tb = (m0 >> 4) + ((wn + 2 * p + hh) & 3);
          u32x2 pk = {pack2(hv[0], hv[1]), pack2(hv[2], hv[3])};
          st8(Hf + (unsigned)((tb * 216 + j8) * 256 + c * 16 + (g & 1) * 8), pk);
        }
      }
    }
  }
  CST(21);
}

struct K1Ld {
  unsigned long long gp[6];
  int pitch[6];
  const u16* xb;
  int xvoff[2];
  unsigned lds0;
  int lw, lane;
};
__device__ __forceinline__ unsigned long long rowaddr(unsigned long long p, int pitch, int rows) {
  unsigned long long r;
  asm volatile("v_mad_u64_u32 %0, vcc, %1, %2, %3" : "=v"(r) : "v"(pitch), "s"(rows), "v"(p) : "vcc");
  return r;
}
#define W_POL ""
#define X_POL ""
#define GLDS16(p, l) asm volatile("s_mov_b32 m0, %1\n\ts_nop 0\n\tglobal_load_lds_dwordx4 %0, off" W_POL :: "v"(p), "s"(l) : "memory")
#define GLDS16X(o, p, l) asm volatile("s_mov_b32 m0, %2\n\ts_nop 0\n\tglobal_load_lds_dwordx4 %0, %1" X_POL :: "v"(o), "s"(p), "s"(l) : "memory")
#define GLDS16S(o, p, l) asm volatile("s_mov_b32 m0, %2\n\ts_nop 0\n\tglobal_load_lds_dwordx4 %0, %1" :: "v"(o), "s"(p), "s"(l) : "memory")
#define WAITVM(n) asm volatile("s_waitcnt vmcnt(%0)" :: "i"(n) : "memory")
template <int H>
__device__ __forceinline__ void k1_ldw(const K1Ld& st) {
  const unsigned l = st.lds0 + K1_W0 + (H % 5) * K1_WS + st.lw * K1_WG;
  const int krow = H * 32;
#pragma unroll
  for (int j = 0; j < 6; ++j) {
    unsigned long long p = rowaddr(st.gp[j], st.pitch[j], krow);
    if (j < 5) GLDS16(p, l + j * 1024);
    else if (st.lane < 32) GLDS16(p, l + 5120);
  }
}
template <int T, int J0, int N>
__device__ __forceinline__ void k1_ldx(const K1Ld& st) {
  const int kc = T * 64;
#pragma unroll
  for (int j = J0; j < J0 + N; ++j)
    GLDS16X(st.xvoff[j & 1], st.xb + j * 8 * D_MODEL + kc, st.lds0 + (T % 3) * K1_XS + (st.lw * 4 + j) * 1024);
}
#define K1_SP 17
#define K1_X2P 0
struct K1Side { u32x4 tw[4], xv[2], xv2[2], wv, wv2; const u16 *src, *xr, *gw; };
__device__ __forceinline__ void k1_side(K1Side& sd, int lane) {
#pragma unroll
  for (int i = 0; i < 4; ++i) GLOAD(sd.tw[i], sd.src + i * 8 * D_MODEL);
  const int ch2 = 64 + (lane & 15);
#pragma unroll
  for (int r = 0; r < 2; ++r) {
    GLOAD(sd.xv[r], sd.xr + r * D_MODEL + lane * 8);
    GLOAD(sd.xv2[r], sd.xr + r * D_MODEL + ch2 * 8);
  }
  GLOAD(sd.wv, sd.gw + lane * 8);
  GLOAD(sd.wv2, sd.gw + ch2 * 8);
}
template <int P>
__device__ __forceinline__ void k1_lphase(const K1Ld& st, K1Side& sd) {
  if constexpr (P == K1_X2P) k1_ldx<2, 0, 4>(st);
  k1_ldw<P + 4>(st);
  if constexpr (P >= 1 && P <= 14) k1_ldx<(P + 5) / 2, ((P + 1) & 1) * 2, 2>(st);
  if constexpr (P == K1_SP) k1_side(sd, st.lane);
  WAITVM((P == 0 ? 16 - 4 * K1_X2P : P == 1 ? 18 : P == 15 ? 14 : 16) + (P == K1_SP || P == K1_SP + 1 ? 10 : 0));
  BARRIER();
  if constexpr (P + 1 < 16) k1_lphase<P + 1>(st, sd);
}

__device__ __forceinline__ void k1_loader(char* smem, const u16* __restrict__ x, const u16* __restrict__ wqkv,
                                          const u16* __restrict__ wg, const u16* __restrict__ wu,
                                          const u16* __restrict__ wd, const u16* __restrict__ wo,
                                          const u16* __restrict__ gw, char* __restrict__ Wdf, char* __restrict__ Wof,
                                          float* __restrict__ G, unsigned* __restrict__ flags) {
  const int tid = threadIdx.x, lane = tid & 63;
  const int lw = __builtin_amdgcn_readfirstlane(tid >> 6) - 8;
  int mt, nt;
  k1_tile(blockIdx.x, mt, nt);
#define LST(i)
  LST(0);
  K1Ld st;
  st.lw = lw;
  st.lane = lane;
  st.lds0 = (unsigned)(size_t)smem;
  {
    const int xr = lane >> 3;
    st.xb = x + (size_t)mt * 128 * D_MODEL;
    st.xvoff[0] = ((lw * 32 + xr) * D_MODEL + ((lane & 7) ^ (xr >> 1)) * 8) * 2;
    st.xvoff[1] = st.xvoff[0] ^ 64;
    LST(1);
    k1_ldx<0, 0, 4>(st);
    __builtin_amdgcn_sched_barrier(0);
    const int nq = min(max(52 - nt * 10, 0), 10), ngu = 10 - nq;
    const int qcol = nt < 2 ? 1280 + 320 * nt : 320 * nt - 640;
    const int gcol = 16 * (nt * 10 + nq - 52);
    const int t1 = 4 * nq, t2 = t1 + 2 * ngu, t4 = t2 + 3;
    const unsigned long long bq = (unsigned long long)wqkv + qcol * 2;
    const unsigned long long bg = (unsigned long long)wg + (long long)(gcol * 2 - t1 * 16);
    const unsigned long long bs = (unsigned long long)wqkv + (2048 + 32 * nt - t2 * 16);
    const unsigned long long bu = (unsigned long long)wu + (long long)(gcol * 2 - t4 * 16);
    const unsigned l0 = st.lds0 + K1_W0 + lw * K1_WG;
#pragma unroll
    for (int j = 0; j < 6; ++j) {
      const int pp = j * 64 + lane;
      const int row = pp / 44;
      int cp = min(pp - row * 44, 42);
      cp -= cp == t2 + 2;
      const bool c1 = cp >= t1, c2 = cp >= t2, c4 = cp >= t4;
      unsigned lo = c1 ? (unsigned)bg : (unsigned)bq, hi = c1 ? (unsigned)(bg >> 32) : (unsigned)(bq >> 32);
      int pitch = c1 ? 2 * F_DIM : 3840;
      lo = c2 ? (unsigned)bs : lo; hi = c2 ? (unsigned)(bs >> 32) : hi; pitch = c2 ? 3840 : pitch;
      lo = c4 ? (unsigned)bu : lo; hi = c4 ? (unsigned)(bu >> 32) : hi; pitch = c4 ? 2 * F_DIM : pitch;
      st.gp[j] = (((unsigned long long)hi << 32) | lo) + (unsigned)((8 * lw + (row & 7)) * pitch + cp * 16);
      st.pitch[j] = pitch;
      if (j < 5) GLDS16(st.gp[j], l0 + j * 1024);
      else if (lane < 32) GLDS16(st.gp[j], l0 + 5120);
      __builtin_amdgcn_sched_barrier(0);
    }
  }
  LST(2);
  k1_ldw<1>(st);
  LST(3);
  k1_ldx<1, 0, 4>(st);
  k1_ldw<2>(st);
  LST(4);
  WAITVM(16);
  LST(5);
  BARRIER();
  LST(6);
  k1_ldw<3>(st);
  WAITVM(16);
  BARRIER();
  LST(7);
  const int item = blockIdx.x * 4 + lw;
  const bool has_item = item < 740;
  const int isd = item < 540;
  const int it2 = isd ? item : (has_item ? item - 540 : 0);
  const int ks = it2 / 10, n64 = it2 - ks * 10;
  K1Side sd;
  sd.src = (isd ? wd : wo) + (size_t)(ks * 32 + (lane >> 3)) * D_MODEL + n64 * 64 + (lane & 7) * 8;
  sd.xr = x + (size_t)(item * 2) * D_MODEL;
  sd.gw = gw;
  k1_lphase<0>(st, sd);
  if (K1_SP == 16) k1_side(sd, lane);
  WAITVM(K1_SP == 16 || K1_SP == 15 ? 16 : 6);
  BARRIER();
  if (K1_SP == 17) k1_side(sd, lane);
  WAITVM(K1_SP >= 15 && K1_SP <= 17 ? 10 : 0);
  BARRIER();
  if (K1_SP == 18) k1_side(sd, lane);
  u32x4 (&tw)[4] = sd.tw;
  u32x4 (&xv)[2] = sd.xv;
  u32x4 (&xv2)[2] = sd.xv2;
  u32x4 &wv = sd.wv, &wv2 = sd.wv2;
  asm volatile("s_waitcnt vmcnt(0)" : "+v"(tw[0]), "+v"(tw[1]), "+v"(tw[2]), "+v"(tw[3]), "+v"(xv[0]), "+v"(xv[1]),
               "+v"(xv2[0]), "+v"(xv2[1]), "+v"(wv), "+v"(wv2));
  if (has_item) {
    char* tb = smem + K1_TR + lw * 5632;
#pragma unroll
    for (int i = 0; i < 4; ++i) *(u32x4*)(tb + i * 1408 + (lane >> 3) * 160 + (lane & 7) * 16) = tw[i];
    const int g = lane >> 4, c = lane & 15;
    const char* rp = tb + g * 1408 + (c >> 2) * 160 + (c & 3) * 8;
    char* dst = (isd ? Wdf : Wof) + ((size_t)(ks * 40 + n64 * 4) * 64 + lane) * 16;
#pragma unroll
    for (int nb = 0; nb < 4; ++nb) {
      bfx8 f = tr_frag(rp + nb * 32, rp + nb * 32 + 640);
      asm volatile("global_store_dwordx4 %0, %1, off offset:%2 sc1\n\ts_nop 1" :: "v"(dst), "v"(f), "i"(nb * 1024) : "memory");
    }
  }
#pragma unroll
  for (int r = 0; r < 2; ++r) {
    float s = 0.f;
#pragma unroll
    for (int e = 0; e < 4; ++e) {
      s += rbf(__uint_as_float(xv[r][e] << 16) * __uint_as_float(wv[e] << 16));
      s += rbf(__uint_as_float(xv[r][e] & 0xffff0000u) * __uint_as_float(wv[e] & 0xffff0000u));
    }
    if (lane < 16) {
#pragma unroll
      for (int e = 0; e < 4; ++e) {
        s += rbf(__uint_as_float(xv2[r][e] << 16) * __uint_as_float(wv2[e] << 16));
        s += rbf(__uint_as_float(xv2[r][e] & 0xffff0000u) * __uint_as_float(wv2[e] & 0xffff0000u));
      }
    }
#pragma unroll
    for (int o = 32; o >= 1; o >>= 1) s += __shfl_xor(s, o);
    s = rbf(s);
    float gv = rbf(1.0f / (1.0f + __expf(-s)));
    if (lane == 0) G[item * 2 + r] = gv;
  }
  if (lw == 0 && lane == 0) flags[(blockIdx.x >> 3) * FL_STR + (blockIdx.x & 7)] = 0u;
  if (item == 1 && lane < 4) flags[32 * FL_STR + lane] = 0x3f803f80u;
}

__global__ __launch_bounds__(768, 3) void k1_gemm(const u16* __restrict__ x, const u16* __restrict__ wqkv,
                                                  const u16* __restrict__ wg, const u16* __restrict__ wu,
                                                  const u16* __restrict__ wd, const u16* __restrict__ wo,
                                                  const u16* __restrict__ gw, char* __restrict__ Qf,
                                                  char* __restrict__ Vf, char* __restrict__ Hf,
                                                  char* __restrict__ Wdf, char* __restrict__ Wof,
                                                  float* __restrict__ G, unsigned* __restrict__ flags) {
  __shared__ __attribute__((aligned(16))) char smem[K1_LDS];
  const int w8 = __builtin_amdgcn_readfirstlane(threadIdx.x) >> 9;
  if (threadIdx.x < 64) RT(0);
  int mt, nt;
  k1_tile(blockIdx.x, mt, nt);
  if (w8) k1_loader(smem, x, wqkv, wg, wu, wd, wo, gw, Wdf, Wof, G, flags);
  else if (nt < 2) k1_compute<true>(smem, Qf, Vf, Hf, x, wqkv, wg, wu);
  else k1_compute<false>(smem, Qf, Vf, Hf, x, wqkv, wg, wu);
}

#define AT_K 15360
#define AT_VS 1296
#define AT_LDS (AT_K + 10 * AT_VS)
__device__ __forceinline__ void attn_stage_k(const char* __restrict__ Kf, unsigned lds, int h, int qb0, int lane) {
  const int slot0 = (20 * h) >> 3;
#pragma unroll
  for (int i = 0; i < 15; ++i) {
    const int p = i * 64 + lane;
    const int blk = p / 48, w = p - blk * 48;
    const int gb = max(qb0 - 8 + (blk >> 1), 0) * 2 + (blk & 1);
    GLDS16S(gb * 20480 + w * 16, Kf + slot0 * 256, lds + i * 1024);
  }
}
template <int I0, int I1>
__device__ __forceinline__ void attn_stage_v(const char* __restrict__ Vf, unsigned lds, int h, int qb0, int lane,
                                             unsigned ones) {
#pragma unroll
  for (int i = I0; i < I1; ++i) {
    const int p = i * 64 + lane;
    const int grp = p / 81, w = p - grp * 81;
    const int gg = max(qb0 - 8 + grp, 0);
    const unsigned o = w < 80 ? (unsigned)(gg * 1280 + w * 16) : ones - h * 81920;
    if (i < 12 || lane < 42) GLDS16S(o, Vf + (size_t)h * 81920, lds + AT_K + i * 1024);
  }
}
#define SB() __builtin_amdgcn_sched_barrier(0)
__device__ __forceinline__ f32x16 zero16() {
  return f32x16{0, 0, 0, 0, 0, 0, 0, 0, 0, 0, 0, 0, 0, 0, 0, 0};
}
__device__ __forceinline__ float at_fma(float a, float cs, float nmc) {
  float t = fmaf(a, cs, nmc);
  asm volatile("" : "+v"(t));
  return t;
}
template <bool FIRST>
__device__ __forceinline__ void at_max(const f32x16& t, float (&mx)[4]) {
#pragma unroll
  for (int r = 0; r < 4; ++r) {
    mx[r] = FIRST ? fmaxf(t[r], t[4 + r]) : fmaxf(fmaxf(mx[r], t[r]), t[4 + r]);
    mx[r] = fmaxf(fmaxf(mx[r], t[8 + r]), t[12 + r]);
    asm volatile("" : "+v"(mx[r]));
  }
}
__device__ __forceinline__ void at_neg(f32x16& t, bool c) {
#pragma unroll
  for (int i = 0; i < 16; ++i) t[i] = c ? -1e30f : t[i];
}
template <bool EXP, bool QK, bool EDGE>
__device__ __forceinline__ void at_stage(f32x16 (&s)[8], float& mc, f32x16& o, bfx8 q1, bfx8 q2, const char* kp1,
                                         const char* kp2, const char* vpa, const char* vpb, const unsigned (&mk)[8],
                                         int jmin, int dq) {
  const float cs = 0.22360679774997896f * 1.4426950408889634f;
  const float nmc = -mc;
  float mx[4];
  f32x16 sx;
  u32x4 pk[2], pp[2];
  bfx8 ka, kb, kbp, xa, xb;
  bfx8 va, vb, wa, wb;
  if constexpr (QK) { xa = *(const bfx8*)kp1; xb = *(const bfx8*)kp2; }
  if constexpr (EXP) o = zero16();
#pragma unroll
  for (int u = 0; u < 8; ++u) {
    const int j = u == 0 ? 8 : u;
    SB();
    float t[4];
#define AT_F(c4) if constexpr (EXP) { _Pragma("unroll") for (int e = 0; e < 4; ++e) t[e] = at_fma(s[u][4 * (c4) + e], cs, nmc); }
#define AT_E(c4, h2) if constexpr (EXP) { pk[(c4) >> 1][2 * ((c4) & 1) + (h2)] = pack2(__builtin_amdgcn_exp2f(t[2 * (h2)]), __builtin_amdgcn_exp2f(t[2 * (h2) + 1])); }
    if constexpr (QK) {
      if (u == 0) sx = mfma32(xa, q1, zero16());
      else s[u - 1] = mfma32(kbp, q2, s[u - 1]);
    }
    SB();
    AT_F(0); AT_E(0, 0); AT_E(0, 1);
    SB();
    if constexpr (QK) { if (u == 0) sx = mfma32(xb, q2, sx); }
    if constexpr (EXP) {
      if (u == 1) {
        u32x4 m8;
#pragma unroll
        for (int e = 0; e < 4; ++e) m8[e] = pp[0][e] & mk[e];
        o = mfma32(va, __builtin_bit_cast(bfx8, m8), o);
      } else if (u > 1) o = mfma32(va, __builtin_bit_cast(bfx8, pp[0]), o);
    }
    SB();
    if constexpr (QK) { ka = *(const bfx8*)(kp1 + j * 1536); kb = *(const bfx8*)(kp2 + j * 1536); }
    AT_F(1); AT_E(1, 0);
    if constexpr (EXP) {
      if (u == 1) {
        u32x4 m8;
#pragma unroll
        for (int e = 0; e < 4; ++e) m8[e] = pp[0][e] & ~mk[e];
        SB();
        o = mfma32(wa, __builtin_bit_cast(bfx8, m8), o);
        SB();
      }
    }
    AT_E(1, 1);
    SB();
    if constexpr (EXP) {
      if (u == 1) {
        u32x4 m8;
#pragma unroll
        for (int e = 0; e < 4; ++e) m8[e] = pp[1][e] & mk[4 + e];
        o = mfma32(vb, __builtin_bit_cast(bfx8, m8), o);
      } else if (u > 1) o = mfma32(vb, __builtin_bit_cast(bfx8, pp[1]), o);
    }
    SB();
    AT_F(2); AT_E(2, 0);
    if constexpr (EXP) {
      if (u == 1) {
        u32x4 m8;
#pragma unroll
        for (int e = 0; e < 4; ++e) m8[e] = pp[1][e] & ~mk[4 + e];
        SB();
        o = mfma32(wb, __builtin_bit_cast(bfx8, m8), o);
        SB();
      }
    }
    AT_E(2, 1);
    if constexpr (QK) {
      if (u == 1) {
        if constexpr (EDGE) at_neg(sx, jmin > 0);
#pragma unroll
        for (int i = 0; i < 16; ++i) s[0][i] = 16 * (i >> 3) + (i & 7) <= dq ? s[0][i] : sx[i];
        at_max<true>(s[0], mx);
      } else if (u > 1) {
        if constexpr (EDGE) at_neg(s[u - 1], u - 1 < jmin);
        at_max<false>(s[u - 1], mx);
      }
    }
    AT_F(3);
    SB();
    if constexpr (QK) s[u] = mfma32(ka, q1, zero16());
    SB();
    if constexpr (EXP) {
      va = *(const bfx8*)(vpa + j * AT_VS);
      if (u == 0) wa = *(const bfx8*)vpa;
      vb = *(const bfx8*)(vpb + j * AT_VS);
      if (u == 0) wb = *(const bfx8*)vpb;
    }
    AT_E(3, 0); AT_E(3, 1);
    kbp = kb;
    pp[0] = pk[0]; pp[1] = pk[1];
  }
  SB();
  if constexpr (QK) s[7] = mfma32(kbp, q2, s[7]);
  if constexpr (EXP) {
    o = mfma32(va, __builtin_bit_cast(bfx8, pp[0]), o);
    o = mfma32(vb, __builtin_bit_cast(bfx8, pp[1]), o);
  }
  if constexpr (QK) {
    if constexpr (EDGE) at_neg(s[7], 7 < jmin);
    at_max<false>(s[7], mx);
    float m = fmaxf(fmaxf(mx[0], mx[1]), fmaxf(mx[2], mx[3]));
    u32x2 r32 = __builtin_amdgcn_permlane32_swap(__float_as_uint(m), __float_as_uint(m), false, false);
    m = fmaxf(__uint_as_float(r32[0]), __uint_as_float(r32[1]));
    mc = m * cs;
  }
}
template <bool EDGE>
__device__ __forceinline__ void at_first(f32x16 (&s)[8], float& mc, bfx8 q1, bfx8 q2, const char* kp1, const char* kp2,
                                         int jmin, int dq, const char* __restrict__ Vf, unsigned lds, int h, int qb0,
                                         int lane, unsigned ones) {
  const float cs = 0.22360679774997896f * 1.4426950408889634f;
  float mx[4];
  f32x16 sx;
  bfx8 ka[9], kb[9];
#pragma unroll
  for (int p = 0; p < 6; ++p) {
    if (p >= 1 && p <= 3) WAITVM(12);
    if (p == 4) WAITVM(13);
#pragma unroll
    for (int j = 2 * p; j < 2 * p + 2; ++j)
      if (j < 9) { ka[j] = *(const bfx8*)(kp1 + j * 1536); kb[j] = *(const bfx8*)(kp2 + j * 1536); }
    SB();
#pragma unroll
    for (int j = 2 * p - 2; j < 2 * p; ++j)
      if (j >= 0 && j < 9) {
        f32x16 t = mfma32(kb[j], q2, mfma32(ka[j], q1, zero16()));
        if (j == 0) sx = t; else s[j & 7] = t;
      }
#pragma unroll
    for (int j = 2 * p - 4; j < 2 * p - 2; ++j)
      if (j >= 1) {
        if constexpr (EDGE) at_neg(s[j], j < jmin);
        if (j == 1) at_max<true>(s[j], mx); else at_max<false>(s[j], mx);
      }
    SB();
    if (p == 0) attn_stage_v<0, 3>(Vf, lds, h, qb0, lane, ones);
    if (p == 1) attn_stage_v<3, 6>(Vf, lds, h, qb0, lane, ones);
    if (p == 2) attn_stage_v<6, 9>(Vf, lds, h, qb0, lane, ones);
    if (p == 3) attn_stage_v<9, 12>(Vf, lds, h, qb0, lane, ones);
    if (p == 4) { attn_stage_v<12, 13>(Vf, lds, h, qb0, lane, ones); BARRIER0(); }
    SB();
  }
  if constexpr (EDGE) at_neg(sx, jmin > 0);
#pragma unroll
  for (int i = 0; i < 16; ++i) s[0][i] = 16 * (i >> 3) + (i & 7) <= dq ? s[0][i] : sx[i];
  at_max<false>(s[0], mx);
  float m = fmaxf(fmaxf(mx[0], mx[1]), fmaxf(mx[2], mx[3]));
  u32x2 r32 = __builtin_amdgcn_permlane32_swap(__float_as_uint(m), __float_as_uint(m), false, false);
  m = fmaxf(__uint_as_float(r32[0]), __uint_as_float(r32[1]));
  mc = m * cs;
}
__device__ __forceinline__ void at_store(const f32x16& o, float gq, char* __restrict__ dst, int k0, int hh) {
  u32x2 r32 = __builtin_amdgcn_permlane32_swap(__float_as_uint(o[8]), __float_as_uint(o[8]), false, false);
  const float inv = gq * __builtin_amdgcn_rcpf(__uint_as_float(r32[1]));
#pragma unroll
  for (int q = 0; q < 3; ++q) {
    const int k = k0 + 8 * q;
    u32x2 v = {pack2(o[4 * q] * inv, o[4 * q + 1] * inv), pack2(o[4 * q + 2] * inv, o[4 * q + 3] * inv)};
    if (q < 2 || hh == 0) st8b(dst + (k >> 3) * 256 + ((k >> 2) & 1) * 8, v);
  }
}
template <bool EDGE>
__device__ __forceinline__ void attn_run(const u32x4 (&qv)[4], const float (&gq)[2], const char* al, char* __restrict__ Of,
                                         int h, int qb0, int lane, const char* __restrict__ Vf, unsigned ones) {
  asm volatile("" : "+v"(lane));
  const int hh = lane >> 5, n = lane & 31;
  bfx8 qf[4];
#pragma unroll
  for (int t = 0; t < 4; ++t) {
    u32x4 v = qv[t];
    if ((t & 1) && (hh || !(h & 1))) { v[2] = 0; v[3] = 0; }
    if ((t & 1) ? hh : (!hh && (h & 1))) { v[0] = 0; v[1] = 0; }
    qf[t] = __builtin_bit_cast(bfx8, v);
  }
  const char* kp2 = al + ((n >> 3) & 1) * 768 + (8 * (n >> 4) + (n & 7)) * 16;
  const char* kp1 = kp2 + hh * 256;
  kp2 += 512;
  const char* vpa = al + AT_K + (n < 16 ? hh * 256 + n * 16 : n < 20 ? 1024 + hh * 64 + (n - 16) * 16 : 1280);
  const char* vpb = vpa + (n < 16 ? 512 : n < 20 ? 128 : 0);
  const int dq = n - 8 * hh;
  unsigned mk[8];
#pragma unroll
  for (int t = 0; t < 8; ++t) {
    const int ko = 16 * (t >> 2) + 2 * (t & 3);
    mk[t] = (ko <= dq ? 0xffffu : 0u) | (ko + 1 <= dq ? 0xffff0000u : 0u);
  }
  const int jm = 8 - qb0;
  char* dst = Of + (size_t)(qb0 * 2 + (n >> 4)) * 20480 + (n & 15) * 16;
  const int k0 = h * 20 + 4 * hh;
  f32x16 s[8], o;
  float mc = 0.f;
#define AST(i)
  AST(0);
  at_first<EDGE>(s, mc, qf[0], qf[1], kp1, kp2, jm, dq, Vf, (unsigned)(size_t)al, h, qb0, lane, ones);
  AST(1);
  WAITVM(0);
  AST(2);
  at_stage<true, true, EDGE>(s, mc, o, qf[2], qf[3], kp1 + 1536, kp2 + 1536, vpa, vpb, mk, jm - 1, dq);
  AST(3);
  at_store(o, gq[0], dst, k0, hh);
  AST(4);
  at_stage<true, false, EDGE>(s, mc, o, qf[2], qf[3], kp1, kp2, vpa + AT_VS, vpb + AT_VS, mk, jm, dq);
  AST(5);
  at_store(o, gq[1], dst + 40960, k0, hh);
  AST(6);
  AST(7);
}

#define GLOADSC(r, o, p) asm volatile("global_load_dwordx4 %0, %1, %2 sc1" : "=v"(r) : "v"(o), "s"(p))
struct G2Frag { u32x4 a[4], w[5]; };
__device__ __forceinline__ void g2_issue_w(G2Frag& f, const char* Wb, int ks, int voff) {
  const char* wp = Wb + (size_t)ks * 40960 + 2048;
  GLOADSO(f.w[0], voff, wp, -2048);
  GLOADSO(f.w[1], voff, wp, -1024);
  GLOADSO(f.w[2], voff, wp, 0);
  GLOADSO(f.w[3], voff, wp, 1024);
  GLOADSO(f.w[4], voff, wp, 2048);
}
__device__ __forceinline__ void g2_issue_h(G2Frag& f, const char* Ab, int ks, int voff, int wave) {
  const char* ap = Ab + (size_t)ks * 1024;
#pragma unroll
  for (int i = 0; i < 4; ++i) GLOADS(f.a[i], voff, ap + (size_t)((wave + i) & 3) * 54 * 1024);
}
__device__ __forceinline__ void g2_issue_o(G2Frag& f, const char* Ab, int ks, int voff, int wave) {
  const char* ap = Ab + (size_t)ks * 1024;
#pragma unroll
  for (int i = 0; i < 4; ++i) GLOADSC(f.a[i], voff, ap + (size_t)((wave + i) & 3) * 20 * 1024);
}
template <int N>
__device__ __forceinline__ void g2_wait(G2Frag& f) {
  asm volatile("s_waitcnt vmcnt(%9)"
               : "+v"(f.a[0]), "+v"(f.a[1]), "+v"(f.a[2]), "+v"(f.a[3]), "+v"(f.w[0]), "+v"(f.w[1]), "+v"(f.w[2]),
                 "+v"(f.w[3]), "+v"(f.w[4])
               : "i"(N));
}
struct G2Ctx {
  const char *Hb, *Wdb, *Ob, *Wob;
  const unsigned* fl;
  const float* gp;
  int wave, voff;
  float omg[4];
  unsigned fv;
};
#define GST(i)
#define G2_PD 4
struct G2Seq { int wait[19], poll; };
constexpr G2Seq g2_seq() {
  G2Seq q{};
  int tag[256] = {}, n = 0;
  for (int s = 0; s < G2_PD; ++s) for (int i = 0; i < 9; ++i) tag[n++] = s;
  for (int t = 0; t < 19; ++t) {
    int last = 0;
    for (int i = 0; i < n; ++i) if (tag[i] == t) last = i;
    q.wait[t] = n - 1 - last;
    const int T = t + G2_PD;
    if (t == 12) for (int i = 0; i < 5; ++i) tag[n++] = -1;
    if (t < 13) for (int i = 0; i < (T <= 13 ? 9 : 5); ++i) tag[n++] = T;
    if (t == 13) {
      for (int u = T; u <= 17; ++u) for (int i = 0; i < 5; ++i) tag[n++] = u;
      for (int i = 0; i < n; ++i) if (tag[i] == -1) last = i;
      q.poll = n - 1 - last;
      for (int u = 14; u <= 17; ++u) for (int i = 0; i < 4; ++i) tag[n++] = u;
    }
    if (t == 14) for (int i = 0; i < 9; ++i) tag[n++] = 18;
  }
  return q;
}
template <int S>
__device__ __forceinline__ void g2_steps(G2Frag (&f)[4], f32x4 (&acc)[5][4], G2Ctx& cx) {
  constexpr G2Seq Q = g2_seq();
  constexpr int T = S + G2_PD;
  G2Frag& cur = f[S & 3];
  g2_wait<Q.wait[S]>(cur);
  GST(S + 1);
  if (S != 13 || cx.wave < 2) {
#pragma unroll
    for (int u = 0; u < 5; ++u)
#pragma unroll
      for (int i = 0; i < 4; ++i)
        asm volatile("v_mfma_f32_16x16x32_bf16 %0, %1, %2, %0" : "+v"(acc[u][i]) : "v"(cur.w[u]), "v"(cur.a[i]));
  }
  if constexpr (S == 13 || S == 18) asm volatile("s_nop 7\n\ts_nop 7" ::: "memory");
  __builtin_amdgcn_sched_barrier(0);
  if constexpr (S == 12) {
    const int go = (cx.voff >> 2) & 60;
#pragma unroll
    for (int i = 0; i < 4; ++i)
      asm volatile("global_load_dword %0, %1, %2" : "=v"(cx.omg[i]) : "v"(go), "s"(cx.gp + ((cx.wave + i) & 3) * 16));
    asm volatile("global_load_dword %0, %1, %2 sc1" : "=v"(cx.fv) : "v"(go & 28), "s"(cx.fl));
  }
  if constexpr (S < 13) {
    if constexpr (T <= 13) {
      const int ks = min(T * 4 + cx.wave, 53);
      g2_issue_h(f[T & 3], cx.Hb, ks, cx.voff, cx.wave);
      g2_issue_w(f[T & 3], cx.Wdb, ks, cx.voff);
    } else {
      g2_issue_w(f[T & 3], cx.Wob, (T - 14) * 4 + cx.wave, cx.voff);
    }
  } else if constexpr (S == 13) {
#pragma unroll
    for (int u = T; u <= 17; ++u) g2_issue_w(f[u & 3], cx.Wob, (u - 14) * 4 + cx.wave, cx.voff);
    asm volatile("s_waitcnt vmcnt(%5)" : "+v"(cx.fv), "+v"(cx.omg[0]), "+v"(cx.omg[1]), "+v"(cx.omg[2]), "+v"(cx.omg[3]) : "i"(Q.poll));
#pragma unroll 1
    for (int it = 0; it < (1 << 20) && __builtin_amdgcn_ballot_w64(cx.fv != 1u) != 0; ++it) {
      __builtin_amdgcn_s_sleep(FL_SLP);
      asm volatile("global_load_dword %0, %1, %2 sc1\n\ts_waitcnt vmcnt(0)" : "=v"(cx.fv) : "v"((cx.voff >> 2) & 28), "s"(cx.fl) : "memory");
    }
    asm volatile("" ::: "memory");
    GST(22);
#pragma unroll
    for (int j = 0; j < 4; ++j) g2_issue_o(f[(14 + j) & 3], cx.Ob, j * 4 + cx.wave, cx.voff, cx.wave);
#pragma unroll
    for (int u = 0; u < 5; ++u)
#pragma unroll
      for (int i = 0; i < 4; ++i) {
        acc[u][i] *= rbf(1.0f - cx.omg[i]);
        asm volatile("" : "+v"(acc[u][i]));
      }
    __builtin_amdgcn_sched_barrier(0);
    asm volatile("s_nop 3" ::: "memory");
  } else if constexpr (S == 14) {
    g2_issue_o(cur, cx.Ob, 16 + cx.wave, cx.voff, cx.wave);
    g2_issue_w(cur, cx.Wob, 16 + cx.wave, cx.voff);
  }
  if constexpr (S + 1 < 19) g2_steps<S + 1>(f, acc, cx);
}
__device__ __forceinline__ void g2_tile(int bid, int& mt, int& nt) {
  const int xcd = bid & 7, idx = bid >> 3;
  mt = (xcd >> 1) * 8 + (idx >> 2);
  nt = (xcd & 1) * 4 + (idx & 3);
}

__global__ __launch_bounds__(512, 2) void k2_mix(const char* __restrict__ Qf, const char* __restrict__ Kf,
                                                 const char* __restrict__ Vf, char* Of,
                                                 const char* __restrict__ Hf, const char* __restrict__ Wdf,
                                                 const char* __restrict__ Wof, const float* __restrict__ G,
                                                 unsigned* flags, u16* __restrict__ out) {
  __shared__ __attribute__((aligned(16))) char smem[4 * AT_LDS];
  __shared__ unsigned k2_cnt;
  const int tid = threadIdx.x, lane = tid & 63;
  const int wave = __builtin_amdgcn_readfirstlane(tid >> 6) ^ 4;
  const int bid = blockIdx.x;
  if (threadIdx.x < 64) RT(2);
  int mt, nt;
  g2_tile(bid, mt, nt);
  if (wave >= 4) {
    float gq[2];
    {
      const float* gp = G + mt * 64 + (lane & 31);
#pragma unroll
      for (int i = 0; i < 2; ++i) GLOAD1(gq[i], gp + i * 32);
    }
    const int h = nt * 4 + (wave - 4);
    const int qb0 = mt * 2;
    if (wave == 4) ST(0, 0);
    char* al = smem + (wave - 4) * AT_LDS;
    u32x4 qv[4];
    {
      const char* qp = Qf + (size_t)(qb0 * 2 + ((lane >> 4) & 1)) * 20480 + ((20 * h) >> 3) * 256 + (lane & 15) * 16;
#pragma unroll
      for (int i = 0; i < 4; ++i) GLOAD(qv[i], qp + (i >> 1) * 40960 + ((i & 1) ? 512 : (lane >> 5) * 256));
    }
    attn_stage_k(Kf, (unsigned)(size_t)al, h, qb0, lane);
    if (tid == 0) k2_cnt = 0u;
    asm volatile("s_waitcnt vmcnt(12)" : "+v"(qv[0]), "+v"(qv[1]), "+v"(qv[2]), "+v"(qv[3]), "+v"(gq[0]),
                 "+v"(gq[1]) :: "memory");
    if (wave == 4) ST(0, 1);
    const unsigned ones = (unsigned)((const char*)(flags + 32 * FL_STR) - Vf);
    if (qb0 < 8) attn_run<true>(qv, gq, al, Of, h, qb0, lane, Vf, ones);
    else attn_run<false>(qv, gq, al, Of, h, qb0, lane, Vf, ones);
    if (wave == 4) ST(0, 2);
    if (wave == 4) ST(0, 3);
    WAITVM(0);
    if (lane == 0) {
      unsigned old = __hip_atomic_fetch_add(&k2_cnt, 1u, __ATOMIC_RELAXED, __HIP_MEMORY_SCOPE_WORKGROUP);
      if (old == 3u) __hip_atomic_store(flags + mt * FL_STR + nt, 1u, __ATOMIC_RELAXED, __HIP_MEMORY_SCOPE_AGENT);
    }
    if (wave == 4) ST(0, 4);
    return;
  }
  f32x4 acc[5][4];
#pragma unroll
  for (int u = 0; u < 5; ++u)
#pragma unroll
    for (int i = 0; i < 4; ++i) {
      acc[u][i] = f32x4{0, 0, 0, 0};
      asm volatile("" : "+v"(acc[u][i]));
    }
  G2Ctx cx;
  cx.Hb = Hf + (size_t)mt * 4 * 54 * 1024;
  cx.Wdb = Wdf + (size_t)nt * 5120;
  cx.Ob = Of + (size_t)mt * 4 * 20 * 1024;
  cx.Wob = Wof + (size_t)nt * 5120;
  cx.fl = flags + mt * FL_STR;
  cx.gp = G + mt * 64;
  cx.wave = wave;
  cx.voff = lane * 16;
  BARRIER0();
  GST(0);
  G2Frag f[4];
#pragma unroll
  for (int s = 0; s < G2_PD; ++s) {
    g2_issue_h(f[s], cx.Hb, s * 4 + wave, cx.voff, wave);
    g2_issue_w(f[s], cx.Wdb, s * 4 + wave, cx.voff);
  }
  g2_steps<0>(f, acc, cx);
  GST(20);
  f32x4* red = (f32x4*)smem;
#pragma unroll
  for (int i = 1; i < 4; ++i)
#pragma unroll
    for (int u = 0; u < 5; ++u) red[((((wave + i) & 3) * 3 + i - 1) * 5 + u) * 64 + lane] = acc[u][i];
  BARRIER();
  const int tok = mt * 64 + wave * 16 + (lane & 15);
#pragma unroll
  for (int u = 0; u < 5; ++u) {
    f32x4 v = acc[u][0];
#pragma unroll
    for (int i = 0; i < 3; ++i) v += red[((wave * 3 + i) * 5 + u) * 64 + lane];
    u32x2 pv = {pack2(v[0], v[1]), pack2(v[2], v[3])};
#define OUTPOL 0
    st8p<OUTPOL>((char*)(out + (size_t)tok * D_MODEL + nt * 80 + u * 16 + 4 * (lane >> 4)), pv);
  }
  GST(21);
}

extern "C" void kernel_launch(void* const* d_in, const int* in_sizes, int n_in,
                              void* d_out, int out_size, void* d_ws, size_t ws_size,
                              hipStream_t stream) {
  const u16* x = (const u16*)d_in[0];
  const u16* wqkv = (const u16*)d_in[1];
  const u16* wo = (const u16*)d_in[2];
  const u16* wg = (const u16*)d_in[4];
  const u16* wu = (const u16*)d_in[5];
  const u16* wd = (const u16*)d_in[6];
  const u16* gw = (const u16*)d_in[7];
  char* ws = (char*)d_ws;
  const size_t QB = (size_t)S_LEN * D_MODEL * 2;
  char* Qf = ws;
  char* Kf = Qf + QB;
  char* Vf = Kf + QB;
  char* Hf = ws + (12u << 20);
  char* Of = ws + (20u << 20);
  char* Wdf = ws + (28u << 20);
  char* Wof = ws + (31u << 20);
  float* G = (float*)(ws + (32u << 20));
  unsigned* flags = (unsigned*)(ws + (33u << 20));
  u16* out = (u16*)d_out;

  k1_gemm<<<256, 768, 0, stream>>>(x, wqkv, wg, wu, wd, wo, gw, Qf, Vf, Hf, Wdf, Wof, G, flags);
  k2_mix<<<256, 512, 0, stream>>>(Qf, Kf, Vf, Of, Hf, Wdf, Wof, G, flags, out);
}
```

```cpp
#define WIDE
#define OWIDE
#define KDPF
#define KAPF
#define BPERM
#define PF_U 3
#define DEDUP
#define PFA
#define PFA_T0 0
#define EARLY 1
#define LFIRST
#define CPL 15
#define PFA_N 3
#define PFA_B 1
#define PFA_P0 0
#define PFA_DP 2
#include <hip/hip_runtime.h>
#include <hip/hip_bf16.h>
#include <stdint.h>

typedef __attribute__((ext_vector_type(8))) __bf16 bfx8;
typedef __attribute__((ext_vector_type(2))) __bf16 bfx2;
typedef __attribute__((ext_vector_type(2))) float f32x2;
typedef __attribute__((ext_vector_type(4))) short s16x4;
typedef __attribute__((ext_vector_type(8))) short s16x8;
typedef __attribute__((ext_vector_type(4))) float f32x4;
typedef __attribute__((ext_vector_type(16))) float f32x16;
typedef __attribute__((ext_vector_type(2))) unsigned u32x2;
typedef __attribute__((ext_vector_type(4))) unsigned u32x4;
typedef __attribute__((ext_vector_type(4), aligned(8))) unsigned u32x4a8;
typedef __attribute__((address_space(3))) s16x4* lds_s16x4_ptr;
typedef unsigned short u16;

#define FL_STR 64
#define FL_SLP 2
#define S_LEN 2048
#define D_MODEL 640
#define F_DIM 1728

__device__ __forceinline__ float bf2f(u16 v) { return __uint_as_float(((unsigned)v) << 16); }
__device__ __forceinline__ u16 f2bf(float f) {
  __hip_bfloat16 b = __float2bfloat16(f);
  return __builtin_bit_cast(u16, b);
}
__device__ __forceinline__ float rbf(float f) { return bf2f(f2bf(f)); }
__device__ __forceinline__ unsigned pack2(float lo, float hi) {
  bfx2 r = __builtin_convertvector(f32x2{lo, hi}, bfx2);
  return __builtin_bit_cast(unsigned, r);
}
__device__ __forceinline__ f32x4 mfma16(bfx8 a, bfx8 b, f32x4 c) {
  return __builtin_amdgcn_mfma_f32_16x16x32_bf16(a, b, c, 0, 0, 0);
}
__device__ __forceinline__ f32x16 mfma32(bfx8 a, bfx8 b, f32x16 c) {
  return __builtin_amdgcn_mfma_f32_32x32x16_bf16(a, b, c, 0, 0, 0);
}
__device__ __forceinline__ bfx8 tr_frag(const char* p0, const char* p1) {
  s16x4 a = __builtin_amdgcn_ds_read_tr16_b64_v4i16((lds_s16x4_ptr)p0);
  s16x4 b = __builtin_amdgcn_ds_read_tr16_b64_v4i16((lds_s16x4_ptr)p1);
  s16x8 v = {a[0], a[1], a[2], a[3], b[0], b[1], b[2], b[3]};
  return __builtin_bit_cast(bfx8, v);
}
#define STPOL 2
#define STPOLB 2
template <int POL>
__device__ __forceinline__ void st8p(char* p, u32x2 v) {
  if constexpr (POL == 0) *(u32x2*)p = v;
  else if constexpr (POL == 1) __builtin_nontemporal_store(v, (u32x2*)p);
  else if constexpr (POL == 2) asm volatile("global_store_dwordx2 %0, %1, off sc1" :: "v"(p), "v"(v) : "memory");
  else asm volatile("global_store_dwordx2 %0, %1, off sc0 sc1" :: "v"(p), "v"(v) : "memory");
}
__device__ __forceinline__ void st8(char* p, u32x2 v) { st8p<STPOL>(p, v); }
__device__ __forceinline__ void st8b(char* p, u32x2 v) { st8p<STPOLB>(p, v); }
__device__ __forceinline__ void st16(char* p, u32x4 v) {
  asm volatile("global_store_dwordx4 %0, %1, off sc1\n\ts_nop 1" :: "v"(p), "v"(v) : "memory");
}
__device__ __forceinline__ u32x4 widen16(u32x2 a, u32x2 b) {
  u32x2 s0 = __builtin_amdgcn_permlane16_swap(a[0], b[0], false, false);
  u32x2 s1 = __builtin_amdgcn_permlane16_swap(a[1], b[1], false, false);
  return u32x4{s0[0], s1[0], s0[1], s1[1]};
}
#define GLOAD(r, p) asm volatile("global_load_dwordx4 %0, %1, off" : "=v"(r) : "v"(p))
#define GLOADS(r, o, p) asm volatile("global_load_dwordx4 %0, %1, %2" : "=v"(r) : "v"(o), "s"(p))
#define GLOADSO(r, o, p, imm) asm volatile("global_load_dwordx4 %0, %1, %2 offset:%3" : "=v"(r) : "v"(o), "s"(p), "i"(imm))
#define GLOAD1(r, p) asm volatile("global_load_dword %0, %1, off" : "=v"(r) : "v"(p))
#define WAITV(n, r) asm volatile("s_waitcnt vmcnt(%1)" : "+v"(r) : "i"(n))
__device__ __forceinline__ void bump(const u16*& p, int stride) {
  unsigned lo = (unsigned)(size_t)p, hi = (unsigned)((size_t)p >> 32);
  asm volatile("v_add_co_u32 %0, vcc, %0, %2\n\tv_addc_co_u32 %1, vcc, 0, %1, vcc" : "+v"(lo), "+v"(hi) : "v"(stride) : "vcc");
  p = (const u16*)(((size_t)hi << 32) | lo);
}
#define BARRIER0() asm volatile("s_barrier" ::: "memory")
#define BARRIER() asm volatile("s_waitcnt lgkmcnt(0)\n\ts_barrier" ::: "memory")
#define ST(role, i)
#define RT(i)

struct Ws {
  char *Qf, *Kf, *Vf, *Hf, *Of, *Wdf, *Wof, *Bb;
  float* G;
};

#define K1_XS 16384
#define K1_WP 704
#define K1_WG (8 * K1_WP + 32)
#define K1_WS (4 * K1_WG)
#define K1_W0 (3 * K1_XS)
#define K1_TR K1_W0
#define K1_LDS (K1_W0 + 5 * K1_WS + 256)

__device__ __forceinline__ void k1_tile(int bid, int& mt, int& nt) {
  const int xcd = bid & 7, idx = bid >> 3;
  mt = (xcd >> 2) * 8 + (idx & 7);
  nt = (xcd & 3) * 4 + (idx >> 3);
}

#define CST(i)

#define K1_PD 2
struct K1C {
  f32x4 acc[5][4], accs;
  int wa[6], wah[6];
  int xa[2][4];
  bfx8 wf[K1_PD + 1], xf[2][4];
  const char* pfb;
  const char* cp2;
  int pfo, pfs, pfs2;
  unsigned pf;
};
#define PF_H0 2
#define PF_L 8
#define PF_B 4
#define PF_DP 3
#define PF_C2 11
#define K1_CODE 29440
#define K2_CODE 30976
template <int K>
__device__ __forceinline__ const char* code_ptr(int slot) {
  unsigned lo, hi;
  if constexpr (K == 1)
    asm volatile("s_getpc_b64 vcc\n\ts_add_u32 %0, vcc_lo, k1_gemm@rel32@lo+4\n\ts_addc_u32 %1, vcc_hi, k1_gemm@rel32@hi+12"
                 : "=s"(lo), "=s"(hi) :: "vcc", "scc");
  else
    asm volatile("s_getpc_b64 vcc\n\ts_add_u32 %0, vcc_lo, k2_mix@rel32@lo+4\n\ts_addc_u32 %1, vcc_hi, k2_mix@rel32@hi+12"
                 : "=s"(lo), "=s"(hi) :: "vcc", "scc");
  if constexpr (K == 2) {
    unsigned l2, h2;
    asm volatile("s_getpc_b64 vcc\n\ts_add_u32 %0, vcc_lo, k2_mix.kd@rel32@lo+4\n\ts_addc_u32 %1, vcc_hi, k2_mix.kd@rel32@hi+12"
                 : "=s"(l2), "=s"(h2) :: "vcc", "scc");
    if (slot == 255) return (const char*)(((unsigned long long)h2 << 32) | l2);
    if (slot >= 248) {
      const unsigned long long ka = (unsigned long long)__builtin_amdgcn_kernarg_segment_ptr();
      return (const char*)((ka & ~4095ull) | ((ka + (slot - 247) * 64) & 4095ull));
    }
  }
  return (const char*)(((unsigned long long)hi << 32) | lo) + min(slot * 128, (K == 1 ? K1_CODE : K2_CODE) - 128);
}
template <int P>
__device__ __forceinline__ void k1_probe(K1C& st) {
  if constexpr (P == 17) asm volatile("s_waitcnt vmcnt(0)" : "+v"(st.pf));
}
template <int F>
__device__ __forceinline__ bfx8 k1_wfrag(const char* smem, const K1C& st) {
  constexpr int P = F / 6, u = F % 6, sl = P % 5;
  constexpr int off = (sl < 3 ? sl : sl - 3) * K1_WS;
  const char* b = smem + (sl < 3 ? st.wa[u] : st.wah[u]);
  return tr_frag(b + off, b + off + 4 * K1_WP);
}
template <bool UNSW, int F>
__device__ __forceinline__ void k1_steps(const char* smem, K1C& st, int wave, int lane) {
  constexpr int P = F / 6, u = F % 6;
  if constexpr (F + K1_PD == 6) BARRIER0();
  if constexpr (F + K1_PD < 120) st.wf[(F + K1_PD) % (K1_PD + 1)] = k1_wfrag<F + K1_PD>(smem, st);
  if constexpr (u < 3 && P < 19)
    st.xf[(P + 1) & 1][u] = *(const bfx8*)(smem + st.xa[(P + 1) & 1][u] + (((P + 1) >> 1) % 3) * K1_XS);
  if constexpr (u == 2 && P < 19)
    st.xf[(P + 1) & 1][3] = *(const bfx8*)(smem + st.xa[(P + 1) & 1][3] + (((P + 1) >> 1) % 3) * K1_XS);
  if constexpr (u < 5) {
#pragma unroll
    for (int i = 0; i < 4; ++i) {
      if constexpr (UNSW) st.acc[u][i] = mfma16(st.xf[P & 1][i], st.wf[F % (K1_PD + 1)], st.acc[u][i]);
      else st.acc[u][i] = mfma16(st.wf[F % (K1_PD + 1)], st.xf[P & 1][i], st.acc[u][i]);
    }
  } else {
    st.accs = mfma16(st.wf[F % (K1_PD + 1)], st.xf[P & 1][0], st.accs);
  }
  __builtin_amdgcn_sched_barrier(0);
  if constexpr (u == 5 && P < 18) {
    if constexpr (P & 1) { CST(P + 1); }
    BARRIER0();
    if constexpr (P & 1) { CST(P + 2); }
  }
  if constexpr (u == PF_U && P >= 1 && P <= 18) { k1_probe<P>(st); __builtin_amdgcn_sched_barrier(0); }
  if constexpr (F + 1 < 120) k1_steps<UNSW, F + 1>(smem, st, wave, lane);
}

__device__ __forceinline__ float silu_mul(float g, float u) {
  return g * u * __builtin_amdgcn_rcpf(1.0f + __builtin_amdgcn_exp2f(g * -1.4426950408889634f));
}

constexpr int pfa_t(int P) {
  if (P < PFA_P0 || (P - PFA_P0) % PFA_DP) return -1;
  const int t = PFA_T0 + PFA_N + (P - PFA_P0) / PFA_DP * PFA_B;
  return t < 10 ? t : -1;
}
constexpr int pfa_c(int P) { return (pfa_t(P) >= 0) + 2 * (P == CPL); }
struct PfA { unsigned long long a; int stride; };
__device__ __forceinline__ PfA pf_setup(const u16* __restrict__ x, const u16* __restrict__ wqkv,
                                        const u16* __restrict__ wg, const u16* __restrict__ wu, int qd, int lane) {
  int mt, nt;
  k1_tile(blockIdx.x, mt, nt);
  const int nq = min(max(52 - nt * 10, 0), 10), ngu = 10 - nq;
  const int qcol = nt < 2 ? 1280 + 320 * nt : 320 * nt - 640;
  const int slot = lane & 15, k = qd * 16 + mt;
  const bool isx = slot == 7 || slot == 8, isg = ngu != 0 && slot < 6;
  const int s3 = slot >= 3 ? slot - 3 : slot;
  const int qs = ngu == 0 ? slot : slot == 9 ? 0 : 16;
  const int oq = k * 3840 + (qs * 128 < nq * 64 ? qcol * 2 + qs * 128 : 2048 + 32 * nt);
  const int og = k * 3456 + 32 * (nt * 10 + nq - 52) + min(s3 * 128, ngu * 32 - 16);
  const int ox = (mt * 128 + nt * 8 + qd * 2 + slot - 7) * 1280;
  const u16* b = isx ? x : isg ? (slot < 3 ? wg : wu) : wqkv;
  PfA r;
  r.stride = isx ? 128 : isg ? 64 * 3456 : 64 * 3840;
  r.a = (unsigned long long)b + (unsigned)((isx ? ox : isg ? og : oq) + (lane >> 4) * r.stride);
  return r;
}
__device__ __forceinline__ void pf_issue(const PfA& p, unsigned& pf, int t0, int n, int lane) {
  unsigned long long r;
  asm volatile("v_mad_u64_u32 %0, vcc, %1, %2, %3" : "=v"(r) : "v"(p.stride), "s"(t0), "v"(p.a) : "vcc");
  if (n >= 4 || lane < 16 * n) asm volatile("global_load_dword %0, %1, off" : "+v"(pf) : "v"(r));
}
__device__ __forceinline__ void k1_pre(K1C& st, const u16* __restrict__ x, const u16* __restrict__ wqkv,
                                       const u16* __restrict__ wg, const u16* __restrict__ wu, int wave) {
  const int lane = threadIdx.x & 63;
  const int slot = (blockIdx.x >> 3) * 8 + wave;
  st.pf = 0;
  asm volatile("global_load_dword %0, %0, %1" : "+v"(st.pf) : "s"(code_ptr<1>(slot)));
  if (PFA_N > 4 || wave < 4) {
    const PfA p = pf_setup(x, wqkv, wg, wu, wave & 3, lane);
    pf_issue(p, st.pf, PFA_T0 + (wave >> 2) * 4, wave < 4 ? PFA_N : PFA_N - 4, lane);
  }
  return;
  int mt, nt;
  k1_tile(blockIdx.x, mt, nt);
  const int nq = min(max(52 - nt * 10, 0), 10), ngu = 10 - nq;
  const int role = wave & 3, half = wave >> 2;
  const int k = half * 16 + mt, q = ngu == 0 ? 2 * role + (lane & 1) : lane & 7;
  const bool rx = role == 3, rq = role == 0 || ngu == 0;
  const int qcol = nt < 2 ? 1280 + 320 * nt : 320 * nt - 640;
  const int pitch = rq ? 3840 : 3456;
  const int ox = (nt * 8 + half * 2 + (lane & 1)) * 1280;
  const int oq = q * 128 >= nq * 64 ? 2048 + 32 * nt : qcol * 2 + q * 128;
  const int og = 32 * (nt * 10 + nq - 52) + min(q * 128, ngu * 32 - 16);
  st.pfb = rx ? (const char*)(x + (size_t)mt * 128 * D_MODEL) : rq ? (const char*)wqkv : (const char*)(role == 1 ? wg : wu);
  st.pfo = rx ? ox : k * pitch + (rq ? oq : og);
  st.pfs = rx ? 128 : 64 * pitch;
  st.pfs2 = rx ? 4 * 1280 : 32 * pitch;
  k1_probe<-1>(st);
}
template <bool UNSW>
__device__ __forceinline__ void k1_compute(K1C& st, char* smem, char* __restrict__ Qf, char* __restrict__ Vf,
                                           char* __restrict__ Hf, int wave) {
  const int lane = threadIdx.x & 63;
  int mt, nt;
  k1_tile(blockIdx.x, mt, nt);
  const int wm = wave & 1, wn = wave >> 1;
  const int g = lane >> 4, c = lane & 15;
  const int nq = min(max(52 - nt * 10, 0), 10), ngu = 10 - nq;
  {
    const int x0 = (wm * 64 + c) * 128 + ((g ^ (c >> 1)) << 4);
#pragma unroll
    for (int i = 0; i < 4; ++i) {
      st.xa[0][i] = x0 + ((wn + i) & 3) * 2048;
      st.xa[1][i] = st.xa[0][i] ^ 64;
      asm volatile("" : "+v"(st.xa[0][i]), "+v"(st.xa[1][i]));
    }
    const int rowp = K1_W0 + g * K1_WG + (c >> 2) * K1_WP;
#pragma unroll
    for (int u = 0; u < 5; ++u) {
      const int v = wn * 5 + u;
      st.wa[u] = rowp + (v < 2 * nq ? v * 32 + (c & 3) * 8
                                    : nq * 32 + v * 16 + (c & 1) * 8 + ((c & 2) ? ngu * 32 + 48 : 0));
    }
    st.wa[5] = rowp + (c & 3) * 8 + nq * 64 + ngu * 32;
#pragma unroll
    for (int u = 0; u < 6; ++u) {
      st.wah[u] = st.wa[u] + 3 * K1_WS;
      asm volatile("" : "+v"(st.wa[u]), "+v"(st.wah[u]));
    }
  }
#pragma unroll
  for (int u = 0; u < 5; ++u)
#pragma unroll
    for (int i = 0; i < 4; ++i) st.acc[u][i] = f32x4{0, 0, 0, 0};
  st.accs = f32x4{0, 0, 0, 0};
  CST(0);
  BARRIER();
  CST(1);
  k1_probe<0>(st);
  st.wf[0] = k1_wfrag<0>(smem, st);
#pragma unroll
  for (int i = 0; i < 4; ++i) st.xf[0][i] = *(const bfx8*)(smem + st.xa[0][i]);
  st.wf[1] = k1_wfrag<1>(smem, st);
  if constexpr (K1_PD > 2) st.wf[2] = k1_wfrag<2>(smem, st);
  if constexpr (K1_PD > 3) st.wf[3] = k1_wfrag<3>(smem, st);
  k1_steps<UNSW, 0>(smem, st, wave, 0);
  CST(20);
  const int hh = lane >> 5;
  const int m0 = mt * 128 + wm * 64;
  int tq[2], tk[2];
#pragma unroll
  for (int p = 0; p < 2; ++p) {
    const int t = m0 + ((wn + 2 * p + (g & 1)) & 3) * 16 + c;
    tq[p] = (t >> 4) * 20480 + (g >> 1) * 256 + c * 16;
    tk[p] = (128 + (t >> 5) * 2 + ((t >> 2) & 1)) * 20480 + (g >> 1) * 256 + (4 * ((t >> 3) & 3) + (t & 3)) * 16;
  }
  {
    const int t = m0 + wn * 16 + c;
    const int tks = (128 + (t >> 5) * 2 + ((t >> 2) & 1)) * 20480 + (g >> 1) * 256 + (4 * ((t >> 3) & 3) + (t & 3)) * 16 + (g & 1) * 8;
    u32x2 v = {pack2(st.accs[0], st.accs[1]), pack2(st.accs[2], st.accs[3])};
    st8(Qf + (unsigned)(tks + (48 + 2 * nt) * 256), v);
  }
#pragma unroll
  for (int u = 0; u < 5; ++u) {
    const int v = wn * 5 + u;
    if constexpr (UNSW) {
      const int col = 320 * nt + 16 * v + c;
      const int h = col / 20, d = col - h * 20;
      const int js = d < 16 ? 256 : 64;
      char* dst = Vf + (unsigned)((h * 64 + (m0 >> 5)) * 1280 + (d < 16 ? d * 16 : 1024 + (d - 16) * 16) + (g >> 1) * js);
#pragma unroll
      for (int p = 0; p < 2; ++p) {
        const int tb = (wn + 2 * p + (g & 1)) & 3;
        const f32x4 a = st.acc[u][2 * p], b = st.acc[u][2 * p + 1];
        st16(dst + (tb >> 1) * 1280 + (tb & 1) * 2 * js,
             widen16(u32x2{pack2(a[0], a[1]), pack2(a[2], a[3])}, u32x2{pack2(b[0], b[1]), pack2(b[2], b[3])}));
      }
    } else {
      if (v < 2 * nq) {
        const int C = 320 * nt - 640 + 16 * v;
#pragma unroll
        for (int p = 0; p < 2; ++p) {
          const f32x4 a = st.acc[u][2 * p], b = st.acc[u][2 * p + 1];
          st16(Qf + (unsigned)(C >= 640 ? tk[p] + (C - 640) * 32 : tq[p] + C * 32),
               widen16(u32x2{pack2(a[0], a[1]), pack2(a[2], a[3])}, u32x2{pack2(b[0], b[1]), pack2(b[2], b[3])}));
        }
      } else {
        const int j8 = 2 * (nt * 10 - 52) + v;
        u32x2 pk[2];
#pragma unroll
        for (int p = 0; p < 2; ++p) {
          float hv[4];
#pragma unroll
          for (int r = 0; r < 4; ++r) {
            u32x2 sw = __builtin_amdgcn_permlane32_swap(__float_as_uint(st.acc[u][2 * p][r]),
                                                        __float_as_uint(st.acc[u][2 * p + 1][r]), false, false);
            hv[r] = silu_mul(__uint_as_float(sw[0]), __uint_as_float(sw[1]));
          }
          pk[p] = u32x2{pack2(hv[0], hv[1]), pack2(hv[2], hv[3])};
        }
        const int tb = (m0 >> 4) + ((wn + 2 * (g & 1) + hh) & 3);
        st16(Hf + (unsigned)((tb * 216 + j8) * 256 + c * 16), widen16(pk[0], pk[1]));
      }
    }
  }
  CST(21);
}

struct K1Ld {
  unsigned long long gp[6];
  int pitch[6];
#define WN 6
  const u16* xb;
  int xvoff[2];
  unsigned lds0;
  int lw, lane;
};
__device__ __forceinline__ unsigned long long rowaddr(unsigned long long p, int pitch, int rows) {
  unsigned long long r;
  asm volatile("v_mad_u64_u32 %0, vcc, %1, %2, %3" : "=v"(r) : "v"(pitch), "s"(rows), "v"(p) : "vcc");
  return r;
}
#define W_POL ""
#define X_POL ""
#define GLDS16(p, l) asm volatile("s_mov_b32 m0, %1\n\ts_nop 0\n\tglobal_load_lds_dwordx4 %0, off" W_POL :: "v"(p), "s"(l) : "memory")
#define GLDS16X(o, p, l) asm volatile("s_mov_b32 m0, %2\n\ts_nop 0\n\tglobal_load_lds_dwordx4 %0, %1" X_POL :: "v"(o), "s"(p), "s"(l) : "memory")
#define GLDS16S(o, p, l) asm volatile("s_mov_b32 m0, %2\n\ts_nop 0\n\tglobal_load_lds_dwordx4 %0, %1" :: "v"(o), "s"(p), "s"(l) : "memory")
#define WAITVM(n) asm volatile("s_waitcnt vmcnt(%0)" :: "i"(n) : "memory")
template <int H>
__device__ __forceinline__ void k1_ldw(const K1Ld& st) {
  const unsigned l = st.lds0 + K1_W0 + (H % 5) * K1_WS + st.lw * K1_WG;
  const int krow = H * 32;
#pragma unroll
  for (int j = 0; j < 6; ++j) {
    unsigned long long p = rowaddr(st.gp[j], st.pitch[j], krow);
    if (j < 5) GLDS16(p, l + j * 1024);
    else if (st.lane < 32) GLDS16(p, l + 5120);
  }
}
template <int T, int J0, int N>
__device__ __forceinline__ void k1_ldx(const K1Ld& st) {
  const int kc = T * 64;
#pragma unroll
  for (int j = J0; j < J0 + N; ++j)
    GLDS16X(st.xvoff[j & 1], st.xb + j * 8 * D_MODEL + kc, st.lds0 + (T % 3) * K1_XS + (st.lw * 4 + j) * 1024);
}
#define K1_SP 17
#define K1_X2P 0
struct K1Side {
  u32x4 tw[4], xv[2], xv2[2], wv, wv2; const u16 *src, *xr, *gw; int ra, rb;
  PfA pa;
  unsigned pf;
};
__device__ __forceinline__ void k1_side(K1Side& sd, int lane) {
#pragma unroll
  for (int i = 0; i < 4; ++i) GLOAD(sd.tw[i], sd.src + (i >> 1) * sd.ra + (i & 1) * sd.rb);
  const int ch2 = 64 + (lane & 15);
#pragma unroll
  for (int r = 0; r < 2; ++r) {
    GLOAD(sd.xv[r], sd.xr + r * D_MODEL + lane * 8);
    GLOAD(sd.xv2[r], sd.xr + r * D_MODEL + ch2 * 8);
  }
  GLOAD(sd.wv, sd.gw + lane * 8);
  GLOAD(sd.wv2, sd.gw + ch2 * 8);
}
template <int P>
__device__ __forceinline__ void k1_lphase(const K1Ld& st, K1Side& sd) {
  if constexpr (P == K1_X2P) k1_ldx<2, 0, 4>(st);
  k1_ldw<P + 4>(st);
  if constexpr (P >= 1 && P <= 14) k1_ldx<(P + 5) / 2, ((P + 1) & 1) * 2, 2>(st);
  if constexpr (P == K1_SP) k1_side(sd, st.lane);
  if constexpr (pfa_t(P) >= 0) pf_issue(sd.pa, sd.pf, pfa_t(P), min(PFA_B, 10 - pfa_t(P)), st.lane);
  if constexpr (P == CPL) {
    unsigned z = 0;
    asm volatile("" : "+v"(z));
#pragma unroll
    for (int i = 0; i < 2; ++i)
      asm volatile("global_load_dword %0, %1, %2" : "+v"(sd.pf) : "v"(z), "s"(code_ptr<2>((blockIdx.x >> 3) * 8 + st.lw * 2 + i)));
  }
  WAITVM(2 * WN + (P == 0 ? 4 - 4 * K1_X2P : P == 1 ? 6 : P == 15 ? 2 : 4) + (P == K1_SP || P == K1_SP + 1 ? 10 : 0) +
         pfa_c(P) + pfa_c(P - 1) + pfa_c(P - 2));
  BARRIER();
  if constexpr (P + 1 < 16) k1_lphase<P + 1>(st, sd);
}

__device__ __forceinline__ void k1_loader(char* smem, const u16* __restrict__ x, const u16* __restrict__ wqkv,
                                          const u16* __restrict__ wg, const u16* __restrict__ wu,
                                          const u16* __restrict__ wd, const u16* __restrict__ wo,
                                          const u16* __restrict__ gw, char* __restrict__ Wdf, char* __restrict__ Wof,
                                          float* __restrict__ G, unsigned* __restrict__ flags) {
  const int tid = threadIdx.x, lane = tid & 63;
  const int lw = __builtin_amdgcn_readfirstlane(tid >> 6);
  int mt, nt;
  k1_tile(blockIdx.x, mt, nt);
#define LST(i)
  LST(0);
  K1Ld st;
  st.lw = lw;
  st.lane = lane;
  st.lds0 = (unsigned)(size_t)smem;
  {
    const int xr = lane >> 3;
    st.xb = x + (size_t)mt * 128 * D_MODEL;
    st.xvoff[0] = ((lw * 32 + xr) * D_MODEL + ((lane & 7) ^ (xr >> 1)) * 8) * 2;
    st.xvoff[1] = st.xvoff[0] ^ 64;
    LST(1);
    k1_ldx<0, 0, 4>(st);
    __builtin_amdgcn_sched_barrier(0);
    const int nq = min(max(52 - nt * 10, 0), 10), ngu = 10 - nq;
    const int qcol = nt < 2 ? 1280 + 320 * nt : 320 * nt - 640;
    const int gcol = 16 * (nt * 10 + nq - 52);
    const int t1 = 4 * nq, t2 = t1 + 2 * ngu, t4 = t2 + 3;
    const unsigned long long bq = (unsigned long long)wqkv + qcol * 2;
    const unsigned long long bg = (unsigned long long)wg + (long long)(gcol * 2 - t1 * 16);
    const unsigned long long bs = (unsigned long long)wqkv + (2048 + 32 * nt - t2 * 16);
    const unsigned long long bu = (unsigned long long)wu + (long long)(gcol * 2 - t4 * 16);
    const unsigned l0 = st.lds0 + K1_W0 + lw * K1_WG;
    {
      int cp = min(lane, 42);
      cp -= cp == t2 + 2;
      const bool c1 = cp >= t1, c2 = cp >= t2, c4 = cp >= t4;
      unsigned lo = c1 ? (unsigned)bg : (unsigned)bq, hi = c1 ? (unsigned)(bg >> 32) : (unsigned)(bq >> 32);
      int pitch = c1 ? 2 * F_DIM : 3840;
      lo = c2 ? (unsigned)bs : lo; hi = c2 ? (unsigned)(bs >> 32) : hi; pitch = c2 ? 3840 : pitch;
      lo = c4 ? (unsigned)bu : lo; hi = c4 ? (unsigned)(bu >> 32) : hi; pitch = c4 ? 2 * F_DIM : pitch;
      const unsigned long long tb = (((unsigned long long)hi << 32) | lo) + (unsigned)(8 * lw * pitch + cp * 16);
      int row[6];
      unsigned tl[6], th[6];
#pragma unroll
      for (int j = 0; j < 6; ++j) {
        const int pp = j * 64 + lane;
        row[j] = pp / 44;
        const int c4b = pp * 4 - row[j] * 176;
        tl[j] = __builtin_amdgcn_ds_bpermute(c4b, (int)(unsigned)tb);
        th[j] = __builtin_amdgcn_ds_bpermute(c4b, (int)(unsigned)(tb >> 32));
        st.pitch[j] = __builtin_amdgcn_ds_bpermute(c4b, pitch);
      }
      __builtin_amdgcn_sched_barrier(0);
#pragma unroll
      for (int j = 0; j < 6; ++j) {
        const unsigned long long t = ((unsigned long long)th[j] << 32) | tl[j];
        asm volatile("v_mad_u64_u32 %0, vcc, %1, %2, %3" : "=v"(st.gp[j]) : "v"(st.pitch[j]), "v"(row[j] & 7), "v"(t) : "vcc");
        if (j < 5) GLDS16(st.gp[j], l0 + j * 1024);
        else if (lane < 32) GLDS16(st.gp[j], l0 + 5120);
        __builtin_amdgcn_sched_barrier(0);
      }
    }
  }
  LST(2);
  if (EARLY == 1) k1_ldw<1>(st);
  LST(3);
  WAITVM(EARLY == 1 ? WN : 0);
  LST(4);
  BARRIER();
  LST(5);
  if (EARLY != 1) k1_ldw<1>(st);
  k1_ldx<1, 0, 4>(st);
  k1_ldw<2>(st);
  LST(6);
  WAITVM(WN + 4);
  BARRIER();
  LST(7);
  k1_ldw<3>(st);
  const int item = blockIdx.x * 4 + lw;
  const bool has_item = item < 740;
  const int isd = item < 540;
  const int it2 = isd ? item : (has_item ? item - 540 : 0);
  const int ks = it2 / 10, n64 = it2 - ks * 10;
  K1Side sd;
  {
    const int r = lane >> 3;
    const int row = isd ? ks * 32 + r : ks < 16 ? ks * 40 + r : (ks - 16) * 160 + 20 * (r >> 2) + 16 + (r & 3);
    sd.src = (isd ? wd : wo) + (size_t)row * D_MODEL + n64 * 64 + (lane & 7) * 8;
    sd.ra = (isd ? 16 : ks < 16 ? 20 : 80) * D_MODEL;
    sd.rb = (isd || ks < 16 ? 8 : 40) * D_MODEL;
  }
  sd.xr = x + (size_t)(item * 2) * D_MODEL;
  sd.gw = gw;
  sd.pa = pf_setup(x, wqkv, wg, wu, lw, lane);
  sd.pf = 0;
  k1_lphase<0>(st, sd);
  if (K1_SP == 16) k1_side(sd, lane);
  WAITVM((K1_SP == 16 || K1_SP == 15 ? WN + 10 : WN) + pfa_c(15) + pfa_c(14));
  BARRIER();
  if (K1_SP == 17) k1_side(sd, lane);
  WAITVM((K1_SP >= 15 && K1_SP <= 17 ? 10 : 0) + pfa_c(15));
  BARRIER();
  if (K1_SP == 18) k1_side(sd, lane);
  u32x4 (&tw)[4] = sd.tw;
  u32x4 (&xv)[2] = sd.xv;
  u32x4 (&xv2)[2] = sd.xv2;
  u32x4 &wv = sd.wv, &wv2 = sd.wv2;
  asm volatile("s_waitcnt vmcnt(0)" : "+v"(tw[0]), "+v"(tw[1]), "+v"(tw[2]), "+v"(tw[3]), "+v"(xv[0]), "+v"(xv[1]),
               "+v"(xv2[0]), "+v"(xv2[1]), "+v"(wv), "+v"(wv2));
  asm volatile("" : "+v"(sd.pf));
  if (has_item) {
    char* tb = smem + K1_TR + lw * 5632;
#pragma unroll
    for (int i = 0; i < 4; ++i) *(u32x4*)(tb + i * 1408 + (lane >> 3) * 160 + (lane & 7) * 16) = tw[i];
    const int g = lane >> 4, c = lane & 15;
    const char* rp = tb + g * 1408 + (c >> 2) * 160 + (c & 3) * 8;
    char* dst = (isd ? Wdf : Wof) + ((size_t)(ks * 40 + n64 * 4) * 64 + lane) * 16;
#pragma unroll
    for (int nb = 0; nb < 4; ++nb) {
      bfx8 f = tr_frag(rp + nb * 32, rp + nb * 32 + 640);
      asm volatile("global_store_dwordx4 %0, %1, off offset:%2 sc1\n\ts_nop 1" :: "v"(dst), "v"(f), "i"(nb * 1024) : "memory");
    }
  }
#pragma unroll
  for (int r = 0; r < 2; ++r) {
    float s = 0.f;
#pragma unroll
    for (int e = 0; e < 4; ++e) {
      s += rbf(__uint_as_float(xv[r][e] << 16) * __uint_as_float(wv[e] << 16));
      s += rbf(__uint_as_float(xv[r][e] & 0xffff0000u) * __uint_as_float(wv[e] & 0xffff0000u));
    }
    if (lane < 16) {
#pragma unroll
      for (int e = 0; e < 4; ++e) {
        s += rbf(__uint_as_float(xv2[r][e] << 16) * __uint_as_float(wv2[e] << 16));
        s += rbf(__uint_as_float(xv2[r][e] & 0xffff0000u) * __uint_as_float(wv2[e] & 0xffff0000u));
      }
    }
#pragma unroll
    for (int o = 32; o >= 1; o >>= 1) s += __shfl_xor(s, o);
    s = rbf(s);
    float gv = rbf(1.0f / (1.0f + __expf(-s)));
    if (lane == 0) G[item * 2 + r] = gv;
  }
  if (lw == 0 && lane == 0) flags[(blockIdx.x >> 3) * FL_STR + (blockIdx.x & 7)] = 0u;
  if (item == 1 && lane < 4) flags[32 * FL_STR + lane] = 0x3f803f80u;
}

#define KALIGN 4096
extern "C" __global__ __launch_bounds__(768, 3) __attribute__((aligned(KALIGN))) void k1_gemm(const u16* __restrict__ x, const u16* __restrict__ wqkv,
                                                  const u16* __restrict__ wg, const u16* __restrict__ wu,
                                                  const u16* __restrict__ wd, const u16* __restrict__ wo,
                                                  const u16* __restrict__ gw, char* __restrict__ Qf,
                                                  char* __restrict__ Vf, char* __restrict__ Hf,
                                                  char* __restrict__ Wdf, char* __restrict__ Wof,
                                                  float* __restrict__ G, unsigned* __restrict__ flags) {
  __shared__ __attribute__((aligned(16))) char smem[K1_LDS];
  const int wave0 = __builtin_amdgcn_readfirstlane(threadIdx.x >> 6);
  const int wave = wave0 < 4 ? wave0 + 8 : wave0 - 4;
  if (threadIdx.x < 64) RT(0);
  K1C st;
  if (wave < 8) k1_pre(st, x, wqkv, wg, wu, wave);
  int w2 = wave;
  asm volatile("" : "+s"(w2));
  if (__builtin_expect(w2 >= 8, 1)) k1_loader(smem, x, wqkv, wg, wu, wd, wo, gw, Wdf, Wof, G, flags);
  else if ((blockIdx.x & 0x83) != 0) k1_compute<false>(st, smem, Qf, Vf, Hf, wave);
  else k1_compute<true>(st, smem, Qf, Vf, Hf, wave);
}

#define AT_K 15360
#define AT_VS 1296
#define AT_LDS (AT_K + 10 * AT_VS)
__device__ __forceinline__ void attn_stage_k(const char* __restrict__ Kf, unsigned lds, int h, int qb0, int lane) {
  const int slot0 = (20 * h) >> 3;
#pragma unroll
  for (int i = 0; i < 15; ++i) {
    const int p = i * 64 + lane;
    const int blk = p / 48, w = p - blk * 48;
    const int gb = max(qb0 - 8 + (blk >> 1), 0) * 2 + (blk & 1);
    GLDS16S(gb * 20480 + w * 16, Kf + slot0 * 256, lds + i * 1024);
  }
}
template <int I0, int I1>
__device__ __forceinline__ void attn_stage_v(const char* __restrict__ Vf, unsigned lds, int h, int qb0, int lane,
                                             unsigned ones) {
#pragma unroll
  for (int i = I0; i < I1; ++i) {
    const int p = i * 64 + lane;
    const int grp = p / 81, w = p - grp * 81;
    const int gg = max(qb0 - 8 + grp, 0);
    const unsigned o = w < 80 ? (unsigned)(gg * 1280 + w * 16) : ones - h * 81920;
    if (i < 12 || lane < 42) GLDS16S(o, Vf + (size_t)h * 81920, lds + AT_K + i * 1024);
  }
}
#define SB() __builtin_amdgcn_sched_barrier(0)
__device__ __forceinline__ f32x16 zero16() {
  return f32x16{0, 0, 0, 0, 0, 0, 0, 0, 0, 0, 0, 0, 0, 0, 0, 0};
}
__device__ __forceinline__ float at_fma(float a, float cs, float nmc) {
  float t = fmaf(a, cs, nmc);
  asm volatile("" : "+v"(t));
  return t;
}
template <bool FIRST>
__device__ __forceinline__ void at_max(const f32x16& t, float (&mx)[4]) {
#pragma unroll
  for (int r = 0; r < 4; ++r) {
    mx[r] = FIRST ? fmaxf(t[r], t[4 + r]) : fmaxf(fmaxf(mx[r], t[r]), t[4 + r]);
    mx[r] = fmaxf(fmaxf(mx[r], t[8 + r]), t[12 + r]);
    asm volatile("" : "+v"(mx[r]));
  }
}
__device__ __forceinline__ void at_neg(f32x16& t, bool c) {
#pragma unroll
  for (int i = 0; i < 16; ++i) t[i] = c ? -1e30f : t[i];
}
template <bool EXP, bool QK, bool EDGE>
__device__ __forceinline__ void at_stage(f32x16 (&s)[8], float& mc, f32x16& o, bfx8 q1, bfx8 q2, const char* kp1,
                                         const char* kp2, const char* vpa, const char* vpb, const unsigned (&mk)[8],
                                         int jmin, int dq) {
  const float cs = 0.22360679774997896f * 1.4426950408889634f;
  const float nmc = -mc;
  float mx[4];
  f32x16 sx;
  u32x4 pk[2], pp[2];
  bfx8 ka, kb, kbp, xa, xb;
  bfx8 va, vb, wa, wb;
  if constexpr (QK) { xa = *(const bfx8*)kp1; xb = *(const bfx8*)kp2; }
  if constexpr (EXP) o = zero16();
#pragma unroll
  for (int u = 0; u < 8; ++u) {
    const int j = u == 0 ? 8 : u;
    SB();
    float t[4];
#define AT_F(c4) if constexpr (EXP) { _Pragma("unroll") for (int e = 0; e < 4; ++e) t[e] = at_fma(s[u][4 * (c4) + e], cs, nmc); }
#define AT_E(c4, h2) if constexpr (EXP) { pk[(c4) >> 1][2 * ((c4) & 1) + (h2)] = pack2(__builtin_amdgcn_exp2f(t[2 * (h2)]), __builtin_amdgcn_exp2f(t[2 * (h2) + 1])); }
    if constexpr (QK) {
      if (u == 0) sx = mfma32(xa, q1, zero16());
      else s[u - 1] = mfma32(kbp, q2, s[u - 1]);
    }
    SB();
    AT_F(0); AT_E(0, 0); AT_E(0, 1);
    SB();
    if constexpr (QK) { if (u == 0) sx = mfma32(xb, q2, sx); }
    if constexpr (EXP) {
      if (u == 1) {
        u32x4 m8;
#pragma unroll
        for (int e = 0; e < 4; ++e) m8[e] = pp[0][e] & mk[e];
        o = mfma32(va, __builtin_bit_cast(bfx8, m8), o);
      } else if (u > 1) o = mfma32(va, __builtin_bit_cast(bfx8, pp[0]), o);
    }
    SB();
    if constexpr (QK) { ka = *(const bfx8*)(kp1 + j * 1536); kb = *(const bfx8*)(kp2 + j * 1536); }
    AT_F(1); AT_E(1, 0);
    if constexpr (EXP) {
      if (u == 1) {
        u32x4 m8;
#pragma unroll
        for (int e = 0; e < 4; ++e) m8[e] = pp[0][e] & ~mk[e];
        SB();
        o = mfma32(wa, __builtin_bit_cast(bfx8, m8), o);
        SB();
      }
    }
    AT_E(1, 1);
    SB();
    if constexpr (EXP) {
      if (u == 1) {
        u32x4 m8;
#pragma unroll
        for (int e = 0; e < 4; ++e) m8[e] = pp[1][e] & mk[4 + e];
        o = mfma32(vb, __builtin_bit_cast(bfx8, m8), o);
      } else if (u > 1) o = mfma32(vb, __builtin_bit_cast(bfx8, pp[1]), o);
    }
    SB();
    AT_F(2); AT_E(2, 0);
    if constexpr (EXP) {
      if (u == 1) {
        u32x4 m8;
#pragma unroll
        for (int e = 0; e < 4; ++e) m8[e] = pp[1][e] & ~mk[4 + e];
        SB();
        o = mfma32(wb, __builtin_bit_cast(bfx8, m8), o);
        SB();
      }
    }
    AT_E(2, 1);
    if constexpr (QK) {
      if (u == 1) {
        if constexpr (EDGE) at_neg(sx, jmin > 0);
#pragma unroll
        for (int i = 0; i < 16; ++i) s[0][i] = 16 * (i >> 3) + (i & 7) <= dq ? s[0][i] : sx[i];
        at_max<true>(s[0], mx);
      } else if (u > 1) {
        if constexpr (EDGE) at_neg(s[u - 1], u - 1 < jmin);
        at_max<false>(s[u - 1], mx);
      }
    }
    AT_F(3);
    SB();
    if constexpr (QK) s[u] = mfma32(ka, q1, zero16());
    SB();
    if constexpr (EXP) {
      va = *(const bfx8*)(vpa + j * AT_VS);
      if (u == 0) wa = *(const bfx8*)vpa;
      vb = *(const bfx8*)(vpb + j * AT_VS);
      if (u == 0) wb = *(const bfx8*)vpb;
    }
    AT_E(3, 0); AT_E(3, 1);
    kbp = kb;
    pp[0] = pk[0]; pp[1] = pk[1];
  }
  SB();
  if constexpr (QK) s[7] = mfma32(kbp, q2, s[7]);
  if constexpr (EXP) {
    o = mfma32(va, __builtin_bit_cast(bfx8, pp[0]), o);
    o = mfma32(vb, __builtin_bit_cast(bfx8, pp[1]), o);
  }
  if constexpr (QK) {
    if constexpr (EDGE) at_neg(s[7], 7 < jmin);
    at_max<false>(s[7], mx);
    float m = fmaxf(fmaxf(mx[0], mx[1]), fmaxf(mx[2], mx[3]));
    u32x2 r32 = __builtin_amdgcn_permlane32_swap(__float_as_uint(m), __float_as_uint(m), false, false);
    m = fmaxf(__uint_as_float(r32[0]), __uint_as_float(r32[1]));
    mc = m * cs;
  }
}
#define K2_REL 4
template <bool EDGE>
__device__ __forceinline__ void at_first(f32x16 (&s)[8], float& mc, bfx8 q1, bfx8 q2, const char* kp1, const char* kp2,
                                         int jmin, int dq, const char* __restrict__ Vf, unsigned lds, int h, int qb0,
                                         int lane, unsigned ones) {
  const float cs = 0.22360679774997896f * 1.4426950408889634f;
  float mx[4];
  f32x16 sx;
  bfx8 ka[9], kb[9];
#pragma unroll
  for (int p = 0; p < 6; ++p) {
    if (p >= 1 && p <= 3) WAITVM(12);
    if (p == 4) WAITVM(13);
#pragma unroll
    for (int j = 2 * p; j < 2 * p + 2; ++j)
      if (j < 9) { ka[j] = *(const bfx8*)(kp1 + j * 1536); kb[j] = *(const bfx8*)(kp2 + j * 1536); }
    SB();
#pragma unroll
    for (int j = 2 * p - 2; j < 2 * p; ++j)
      if (j >= 0 && j < 9) {
        f32x16 t = mfma32(kb[j], q2, mfma32(ka[j], q1, zero16()));
        if (j == 0) sx = t; else s[j & 7] = t;
      }
#pragma unroll
    for (int j = 2 * p - 4; j < 2 * p - 2; ++j)
      if (j >= 1) {
        if constexpr (EDGE) at_neg(s[j], j < jmin);
        if (j == 1) at_max<true>(s[j], mx); else at_max<false>(s[j], mx);
      }
    SB();
    if (p == 0) attn_stage_v<0, 3>(Vf, lds, h, qb0, lane, ones);
    if (p == 1) attn_stage_v<3, 6>(Vf, lds, h, qb0, lane, ones);
    if (p == 2) attn_stage_v<6, 9>(Vf, lds, h, qb0, lane, ones);
    if (p == 3) attn_stage_v<9, 12>(Vf, lds, h, qb0, lane, ones);
    if (p == 4) attn_stage_v<12, 13>(Vf, lds, h, qb0, lane, ones);
    if (p == K2_REL) BARRIER0();
    SB();
  }
  if constexpr (EDGE) at_neg(sx, jmin > 0);
#pragma unroll
  for (int i = 0; i < 16; ++i) s[0][i] = 16 * (i >> 3) + (i & 7) <= dq ? s[0][i] : sx[i];
  at_max<false>(s[0], mx);
  float m = fmaxf(fmaxf(mx[0], mx[1]), fmaxf(mx[2], mx[3]));
  u32x2 r32 = __builtin_amdgcn_permlane32_swap(__float_as_uint(m), __float_as_uint(m), false, false);
  m = fmaxf(__uint_as_float(r32[0]), __uint_as_float(r32[1]));
  mc = m * cs;
}
__device__ __forceinline__ void at_store(const f32x16& o, float gq, char* __restrict__ dst, int k0, int hh) {
  u32x2 r32 = __builtin_amdgcn_permlane32_swap(__float_as_uint(o[8]), __float_as_uint(o[8]), false, false);
  const float inv = gq * __builtin_amdgcn_rcpf(__uint_as_float(r32[1]));
  u32x2 v[3];
#pragma unroll
  for (int q = 0; q < 3; ++q)
    v[q] = u32x2{pack2(o[4 * q] * inv, o[4 * q + 1] * inv), pack2(o[4 * q + 2] * inv, o[4 * q + 3] * inv)};
  u32x2 s0 = __builtin_amdgcn_permlane32_swap(v[0][0], v[1][0], false, false);
  u32x2 s1 = __builtin_amdgcn_permlane32_swap(v[0][1], v[1][1], false, false);
  st16(dst, u32x4{s0[0], s1[0], s0[1], s1[1]});
  if (hh == 0) st8b(dst + k0, v[2]);
}
template <bool EDGE>
__device__ __forceinline__ void attn_run(const u32x4 (&qv)[4], const float (&gq)[2], const char* al, char* __restrict__ Of,
                                         int h, int qb0, int lane, const char* __restrict__ Vf, unsigned ones) {
  asm volatile("" : "+v"(lane));
  const int hh = lane >> 5, n = lane & 31;
  bfx8 qf[4];
#pragma unroll
  for (int t = 0; t < 4; ++t) {
    u32x4 v = qv[t];
    if ((t & 1) && (hh || !(h & 1))) { v[2] = 0; v[3] = 0; }
    if ((t & 1) ? hh : (!hh && (h & 1))) { v[0] = 0; v[1] = 0; }
    qf[t] = __builtin_bit_cast(bfx8, v);
  }
  const char* kp2 = al + ((n >> 3) & 1) * 768 + (8 * (n >> 4) + (n & 7)) * 16;
  const char* kp1 = kp2 + hh * 256;
  kp2 += 512;
  const char* vpa = al + AT_K + (n < 16 ? hh * 256 + n * 16 : n < 20 ? 1024 + hh * 64 + (n - 16) * 16 : 1280);
  const char* vpb = vpa + (n < 16 ? 512 : n < 20 ? 128 : 0);
  const int dq = n - 8 * hh;
  unsigned mk[8];
#pragma unroll
  for (int t = 0; t < 8; ++t) {
    const int ko = 16 * (t >> 2) + 2 * (t & 3);
    mk[t] = (ko <= dq ? 0xffffu : 0u) | (ko + 1 <= dq ? 0xffff0000u : 0u);
  }
  const int jm = 8 - qb0;
  char* dst = Of + (size_t)(qb0 * 2 + (n >> 4)) * 20480 + (2 * h + hh) * 256 + (n & 15) * 16;
  const int k0 = 16384 - h * 384 - (n & 15) * 8;
  f32x16 s[8], o;
  float mc = 0.f;
#define AST(i)
  AST(0);
  at_first<EDGE>(s, mc, qf[0], qf[1], kp1, kp2, jm, dq, Vf, (unsigned)(size_t)al, h, qb0, lane, ones);
  AST(1);
  WAITVM(0);
  AST(2);
  at_stage<true, true, EDGE>(s, mc, o, qf[2], qf[3], kp1 + 1536, kp2 + 1536, vpa, vpb, mk, jm - 1, dq);
  AST(3);
  at_store(o, gq[0], dst, k0, hh);
  AST(4);
  at_stage<true, false, EDGE>(s, mc, o, qf[2], qf[3], kp1, kp2, vpa + AT_VS, vpb + AT_VS, mk, jm, dq);
  AST(5);
  at_store(o, gq[1], dst + 40960, k0, hh);
  AST(6);
  AST(7);
}

#define GLOADSC(r, o, p) asm volatile("global_load_dwordx4 %0, %1, %2 sc1" : "=v"(r) : "v"(o), "s"(p))
struct G2Frag { u32x4 a[4], w[5]; };
__device__ __forceinline__ void g2_issue_w(G2Frag& f, const char* Wb, int ks, int voff) {
  const char* wp = Wb + (size_t)ks * 40960 + 2048;
  GLOADSO(f.w[0], voff, wp, -2048);
  GLOADSO(f.w[1], voff, wp, -1024);
  GLOADSO(f.w[2], voff, wp, 0);
  GLOADSO(f.w[3], voff, wp, 1024);
  GLOADSO(f.w[4], voff, wp, 2048);
}
__device__ __forceinline__ void g2_issue_h(G2Frag& f, const char* Ab, int ks, int voff, int wave) {
  const char* ap = Ab + (size_t)ks * 1024;
#pragma unroll
  for (int i = 0; i < 4; ++i) GLOADS(f.a[i], voff, ap + (size_t)((wave + i) & 3) * 54 * 1024);
}
__device__ __forceinline__ void g2_issue_o(G2Frag& f, const char* Ab, int ks, int voff, int wave) {
  const char* ap = Ab + (size_t)ks * 1024;
#pragma unroll
  for (int i = 0; i < 4; ++i) GLOADSC(f.a[i], voff, ap + (size_t)((wave + i) & 3) * 20 * 1024);
}
template <int N>
__device__ __forceinline__ void g2_wait(G2Frag& f) {
  asm volatile("s_waitcnt vmcnt(%9)"
               : "+v"(f.a[0]), "+v"(f.a[1]), "+v"(f.a[2]), "+v"(f.a[3]), "+v"(f.w[0]), "+v"(f.w[1]), "+v"(f.w[2]),
                 "+v"(f.w[3]), "+v"(f.w[4])
               : "i"(N));
}
struct G2Ctx {
  const char *Hb, *Wdb, *Ob, *Wob;
  const unsigned* fl;
  const float* gp;
  int wave, voff;
  float omg[4];
  unsigned fv;
  u32x2 ta[8];
};
#define GST(i)
#define G2_PD 4
struct G2Seq { int wait[19], poll; };
constexpr G2Seq g2_seq() {
  G2Seq q{};
  int tag[256] = {}, n = 0;
  for (int s = 0; s < G2_PD; ++s) for (int i = 0; i < 9; ++i) tag[n++] = s;
  for (int t = 0; t < 19; ++t) {
    int last = 0;
    for (int i = 0; i < n; ++i) if (tag[i] == t) last = i;
    q.wait[t] = n - 1 - last;
    const int T = t + G2_PD;
    if (t == 12) for (int i = 0; i < 5; ++i) tag[n++] = -1;
    if (t < 13) for (int i = 0; i < (T <= 13 ? 9 : 5); ++i) tag[n++] = T;
    if (t == 13) {
      for (int u = T; u <= 17; ++u) for (int i = 0; i < 5; ++i) tag[n++] = u;
      for (int i = 0; i < n; ++i) if (tag[i] == -1) last = i;
      q.poll = n - 1 - last;
      for (int u = 14; u <= 17; ++u) for (int i = 0; i < 4; ++i) tag[n++] = u;
    }
    if (t == 14) for (int i = 0; i < 13; ++i) tag[n++] = 18;
  }
  return q;
}
template <int S>
__device__ __forceinline__ void g2_steps(G2Frag (&f)[4], f32x4 (&acc)[5][4], G2Ctx& cx) {
  constexpr G2Seq Q = g2_seq();
  constexpr int T = S + G2_PD;
  G2Frag& cur = f[S & 3];
  if constexpr (S == 18) {
    asm volatile("s_waitcnt vmcnt(0)"
                 : "+v"(cx.ta[0]), "+v"(cx.ta[1]), "+v"(cx.ta[2]), "+v"(cx.ta[3]), "+v"(cx.ta[4]), "+v"(cx.ta[5]), "+v"(cx.ta[6]),
                   "+v"(cx.ta[7]), "+v"(cur.w[0]), "+v"(cur.w[1]), "+v"(cur.w[2]), "+v"(cur.w[3]), "+v"(cur.w[4]));
#pragma unroll
    for (int i = 0; i < 4; ++i) cur.a[i] = u32x4{cx.ta[2 * i][0], cx.ta[2 * i][1], cx.ta[2 * i + 1][0], cx.ta[2 * i + 1][1]};
    asm volatile("s_nop 1" : "+v"(cur.a[0]), "+v"(cur.a[1]), "+v"(cur.a[2]), "+v"(cur.a[3]));
  } else
  g2_wait<Q.wait[S]>(cur);
  GST(S + 1);
  if (S != 13 || cx.wave < 2) {
#pragma unroll
    for (int u = 0; u < 5; ++u)
#pragma unroll
      for (int i = 0; i < 4; ++i)
        asm volatile("v_mfma_f32_16x16x32_bf16 %0, %1, %2, %0" : "+v"(acc[u][i]) : "v"(cur.w[u]), "v"(cur.a[i]));
  }
  if constexpr (S == 13 || S == 18) asm volatile("s_nop 7\n\ts_nop 7" ::: "memory");
  __builtin_amdgcn_sched_barrier(0);
  if constexpr (S == 12) {
    const int go = (cx.voff >> 2) & 60;
#pragma unroll
    for (int i = 0; i < 4; ++i)
      asm volatile("global_load_dword %0, %1, %2" : "=v"(cx.omg[i]) : "v"(go), "s"(cx.gp + ((cx.wave + i) & 3) * 16));
    asm volatile("global_load_dword %0, %1, %2 sc1" : "=v"(cx.fv) : "v"(go & 28), "s"(cx.fl));
  }
  if constexpr (S < 13) {
    if constexpr (T <= 13) {
      const int ks = min(T * 4 + cx.wave, 53);
      g2_issue_h(f[T & 3], cx.Hb, ks, cx.voff, cx.wave);
      g2_issue_w(f[T & 3], cx.Wdb, ks, cx.voff);
    } else {
      g2_issue_w(f[T & 3], cx.Wob, (T - 14) * 4 + cx.wave, cx.voff);
    }
  } else if constexpr (S == 13) {
#pragma unroll
    for (int u = T; u <= 17; ++u) g2_issue_w(f[u & 3], cx.Wob, (u - 14) * 4 + cx.wave, cx.voff);
    asm volatile("s_waitcnt vmcnt(%5)" : "+v"(cx.fv), "+v"(cx.omg[0]), "+v"(cx.omg[1]), "+v"(cx.omg[2]), "+v"(cx.omg[3]) : "i"(Q.poll));
#pragma unroll 1
    for (int it = 0; it < (1 << 20) && __builtin_amdgcn_ballot_w64(cx.fv != 1u) != 0; ++it) {
      __builtin_amdgcn_s_sleep(FL_SLP);
      asm volatile("global_load_dword %0, %1, %2 sc1\n\ts_waitcnt vmcnt(0)" : "=v"(cx.fv) : "v"((cx.voff >> 2) & 28), "s"(cx.fl) : "memory");
    }
    asm volatile("" ::: "memory");
    GST(22);
#pragma unroll
    for (int j = 0; j < 4; ++j) g2_issue_o(f[(14 + j) & 3], cx.Ob, j * 4 + cx.wave, cx.voff, cx.wave);
#pragma unroll
    for (int u = 0; u < 5; ++u)
#pragma unroll
      for (int i = 0; i < 4; ++i) {
        acc[u][i] *= rbf(1.0f - cx.omg[i]);
        asm volatile("" : "+v"(acc[u][i]));
      }
    __builtin_amdgcn_sched_barrier(0);
    asm volatile("s_nop 3" ::: "memory");
  } else if constexpr (S == 14) {
    {
      const int to = (cx.voff & ~255) | ((cx.voff & 255) >> 1);
#pragma unroll
      for (int i = 0; i < 8; ++i)
        asm volatile("global_load_dwordx2 %0, %1, %2 offset:%3 sc1" : "=v"(cx.ta[i])
                     : "v"(to), "s"(cx.Ob + (size_t)(16 + cx.wave) * 1024 + (size_t)((cx.wave + (i >> 1)) & 3) * 20 * 1024), "i"((i & 1) * 128));
    }
    g2_issue_w(cur, cx.Wob, 16 + cx.wave, cx.voff);
  }
  if constexpr (S + 1 < 19) g2_steps<S + 1>(f, acc, cx);
}
__device__ __forceinline__ void g2_tile(int bid, int& mt, int& nt) {
  const int xcd = bid & 7, idx = bid >> 3;
  mt = (xcd >> 1) * 8 + (idx >> 2);
  nt = (xcd & 1) * 4 + (idx & 3);
}

extern "C" __global__ __launch_bounds__(512, 2) __attribute__((aligned(KALIGN))) void k2_mix(const char* __restrict__ Qf, const char* __restrict__ Kf,
                                                 const char* __restrict__ Vf, char* Of,
                                                 const char* __restrict__ Hf, const char* __restrict__ Wdf,
                                                 const char* __restrict__ Wof, const float* __restrict__ G,
                                                 unsigned* flags, u16* __restrict__ out) {
  __shared__ __attribute__((aligned(16))) char smem[4 * AT_LDS];
  __shared__ unsigned k2_cnt;
  const int tid = threadIdx.x, lane = tid & 63;
  const int wave = __builtin_amdgcn_readfirstlane(tid >> 6) ^ 4;
  const int bid = blockIdx.x;
  if (threadIdx.x < 64) RT(2);
  int mt, nt;
  g2_tile(bid, mt, nt);
  if (wave >= 4) {
    float gq[2];
    {
      const float* gp = G + mt * 64 + (lane & 31);
#pragma unroll
      for (int i = 0; i < 2; ++i) GLOAD1(gq[i], gp + i * 32);
    }
    const int h = nt * 4 + (wave - 4);
    const int qb0 = mt * 2;
    if (wave == 4) ST(0, 0);
    char* al = smem + (wave - 4) * AT_LDS;
    u32x4 qv[4];
    {
      const char* qp = Qf + (size_t)(qb0 * 2 + ((lane >> 4) & 1)) * 20480 + ((20 * h) >> 3) * 256 + (lane & 15) * 16;
#pragma unroll
      for (int i = 0; i < 4; ++i) GLOAD(qv[i], qp + (i >> 1) * 40960 + ((i & 1) ? 512 : (lane >> 5) * 256));
    }
    attn_stage_k(Kf, (unsigned)(size_t)al, h, qb0, lane);
    if (tid == 0) k2_cnt = 0u;
    if (K2_REL == -1) BARRIER();
    asm volatile("s_waitcnt vmcnt(12)" : "+v"(qv[0]), "+v"(qv[1]), "+v"(qv[2]), "+v"(qv[3]), "+v"(gq[0]),
                 "+v"(gq[1]) :: "memory");
    if (wave == 4) ST(0, 1);
    const unsigned ones = (unsigned)((const char*)(flags + 32 * FL_STR) - Vf);
    if (qb0 < 8) attn_run<true>(qv, gq, al, Of, h, qb0, lane, Vf, ones);
    else attn_run<false>(qv, gq, al, Of, h, qb0, lane, Vf, ones);
    if (wave == 4) ST(0, 2);
    if (wave == 4) ST(0, 3);
    WAITVM(0);
    if (lane == 0) {
      unsigned old = __hip_atomic_fetch_add(&k2_cnt, 1u, __ATOMIC_RELAXED, __HIP_MEMORY_SCOPE_WORKGROUP);
      if (old == 3u) __hip_atomic_store(flags + mt * FL_STR + nt, 1u, __ATOMIC_RELAXED, __HIP_MEMORY_SCOPE_AGENT);
    }
    if (wave == 4) ST(0, 4);
    return;
  }
  f32x4 acc[5][4];
#pragma unroll
  for (int u = 0; u < 5; ++u)
#pragma unroll
    for (int i = 0; i < 4; ++i) {
      acc[u][i] = f32x4{0, 0, 0, 0};
      asm volatile("" : "+v"(acc[u][i]));
    }
  G2Ctx cx;
  cx.Hb = Hf + (size_t)mt * 4 * 54 * 1024;
  cx.Wdb = Wdf + (size_t)nt * 5120;
  cx.Ob = Of + (size_t)mt * 4 * 20 * 1024;
  cx.Wob = Wof + (size_t)nt * 5120;
  cx.fl = flags + mt * FL_STR;
  cx.gp = G + mt * 64;
  cx.wave = wave;
  cx.voff = lane * 16;
  BARRIER0();
  GST(0);
  G2Frag f[4];
#pragma unroll
  for (int s = 0; s < G2_PD; ++s) {
    g2_issue_h(f[s], cx.Hb, s * 4 + wave, cx.voff, wave);
    g2_issue_w(f[s], cx.Wdb, s * 4 + wave, cx.voff);
  }
  g2_steps<0>(f, acc, cx);
  GST(20);
  f32x4* red = (f32x4*)smem;
#pragma unroll
  for (int i = 1; i < 4; ++i)
#pragma unroll
    for (int u = 0; u < 5; ++u) red[((((wave + i) & 3) * 3 + i - 1) * 5 + u) * 64 + lane] = acc[u][i];
  BARRIER();
  const int tok = mt * 64 + wave * 16 + (lane & 15);
#pragma unroll
  for (int u = 0; u < 5; ++u) {
    f32x4 v = acc[u][0];
#pragma unroll
    for (int i = 0; i < 3; ++i) v += red[((wave * 3 + i) * 5 + u) * 64 + lane];
    u32x2 pv = {pack2(v[0], v[1]), pack2(v[2], v[3])};
#define OUTPOL 0
    st8p<OUTPOL>((char*)(out + (size_t)tok * D_MODEL + nt * 80 + u * 16 + 4 * (lane >> 4)), pv);
  }
  GST(21);
}

extern "C" void kernel_launch(void* const* d_in, const int* in_sizes, int n_in,
                              void* d_out, int out_size, void* d_ws, size_t ws_size,
                              hipStream_t stream) {
  const u16* x = (const u16*)d_in[0];
  const u16* wqkv = (const u16*)d_in[1];
  const u16* wo = (const u16*)d_in[2];
  const u16* wg = (const u16*)d_in[4];
  const u16* wu = (const u16*)d_in[5];
  const u16* wd = (const u16*)d_in[6];
  const u16* gw = (const u16*)d_in[7];
  char* ws = (char*)d_ws;
  const size_t QB = (size_t)S_LEN * D_MODEL * 2;
  char* Qf = ws;
  char* Kf = Qf + QB;
  char* Vf = Kf + QB;
  char* Hf = ws + (12u << 20);
  char* Of = ws + (20u << 20);
  char* Wdf = ws + (28u << 20);
  char* Wof = ws + (31u << 20);
  float* G = (float*)(ws + (32u << 20));
  unsigned* flags = (unsigned*)(ws + (33u << 20));
  u16* out = (u16*)d_out;

  k1_gemm<<<256, 768, 0, stream>>>(x, wqkv, wg, wu, wd, wo, gw, Qf, Vf, Hf, Wdf, Wof, G, flags);
  k2_mix<<<256, 512, 0, stream>>>(Qf, Kf, Vf, Of, Hf, Wdf, Wof, G, flags, out);
}
```
